```python
import torch

def _softmax(x, axis=-1):
    return torch.softmax(x, dim=axis)

def _gelu(x):
    return torch.nn.functional.gelu(x, approximate='tanh')

def _silu(x):
    return torch.nn.functional.silu(x)

def _sigmoid(x):
    return torch.sigmoid(x)

def _layernorm(x, axis=-1, eps=1e-05):
    m = x.mean(dim=axis, keepdim=True)
    v = x.var(dim=axis, keepdim=True, unbiased=False)
    return (x - m) / torch.sqrt(v + eps)

def _scatter_add(tgt, idx, src, axis=0):
    return tgt.clone().index_add_(axis, idx, src)

def _segment_sum(src, seg, n):
    out = torch.zeros((n,) + src.shape[1:], dtype=src.dtype)
    return out.index_add_(0, seg, src)

def _pad(x, pw):
    flat = tuple((v for pair in reversed(pw) for v in pair))
    return torch.nn.functional.pad(x, flat)
kernel_name = 'gated_mix__compressed_kv_cla__memory_aug_recurrent_mem_tr__prompt'

def setup_inputs():

    _w = lambda a: (lambda t: t.to(torch.bfloat16) if t.is_floating_point() else t)(torch.as_tensor(a))

    def _A_setup():
        B, H, Sq, Sk, D, M = (1, 32, 2048, 2048, 128, 512)
        Dc = 4 * D
        return {'x': _w((lambda *s: torch.randn(*s, dtype=torch.bfloat16) * 0.05)(B, Sq, M)), 'c_kv': _w((lambda *s: torch.randn(*s, dtype=torch.bfloat16) * 0.8)(B, Sk, Dc)), 'w_q': _w((lambda *s: torch.randn(*s, dtype=torch.bfloat16) * 6.4)(M, H, D) * M ** (-0.5)), 'w_uk': _w((lambda *s: torch.randn(*s, dtype=torch.bfloat16) * 6.4)(Dc, H, D) * Dc ** (-0.5)), 'w_uv': _w((lambda *s: torch.randn(*s, dtype=torch.bfloat16) * 0.8)(Dc, H, D) * Dc ** (-0.5)), 'w_o': _w((lambda *s: torch.randn(*s, dtype=torch.bfloat16) * 0.8)(H, D, M) * (H * D) ** (-0.5))}
    _A_in = _A_setup()

    def _B_setup():
        B, S, Dm = (1, 2048, 512)
        H, Dh = (32, 128)
        M, seg = (8, 32)
        seg = min(seg, S)
        S = S // seg * seg
        return {'x': _w((lambda *s: torch.randn(*s, dtype=torch.bfloat16) * 0.05)(B, S, Dm)), 'mem0': _w((lambda *s: torch.randn(*s, dtype=torch.bfloat16) * 0.8)(B, M, Dm)), 'Wq': _w((lambda *s: torch.randn(*s, dtype=torch.bfloat16) * 3.2)(Dm, H, Dh) * Dm ** (-0.5)), 'Wk': _w((lambda *s: torch.randn(*s, dtype=torch.bfloat16) * 3.2)(Dm, H, Dh) * Dm ** (-0.5)), 'Wv': _w((lambda *s: torch.randn(*s, dtype=torch.bfloat16) * 0.8)(Dm, H, Dh) * Dm ** (-0.5)), 'Wo': _w((lambda *s: torch.randn(*s, dtype=torch.bfloat16) * 0.8)(H, Dh, Dm) * (H * Dh) ** (-0.5)), 'seg_len': seg}
    _B_in = _B_setup()
    return {'A_x': _A_in['x'], 'A_c_kv': _A_in['c_kv'], 'A_w_q': _A_in['w_q'], 'A_w_uk': _A_in['w_uk'], 'A_w_uv': _A_in['w_uv'], 'A_w_o': _A_in['w_o'], 'B_x': _B_in['x'], 'B_mem0': _B_in['mem0'], 'B_Wq': _B_in['Wq'], 'B_Wk': _B_in['Wk'], 'B_Wv': _B_in['Wv'], 'B_Wo': _B_in['Wo'], 'B_seg_len': _B_in['seg_len'], 'gate_w': _w((lambda *s: torch.randn(*s, dtype=torch.bfloat16) * 0.05)(512) * 16.0)}

def reference(A_x, A_c_kv, A_w_q, A_w_uk, A_w_uv, A_w_o, B_x, B_mem0, B_Wq, B_Wk, B_Wv, B_Wo, B_seg_len, gate_w):

    def _A_ref(x, c_kv, w_q, w_uk, w_uv, w_o):
        B, Sq, M = (x.shape[0], x.shape[1], x.shape[2])
        Sk = c_kv.shape[1]
        H, D = (w_q.shape[1], w_q.shape[2])
        q = torch.einsum('bqm,mhd->bhqd', x, w_q)
        k = torch.einsum('bkc,chd->bhkd', c_kv, w_uk)
        v = torch.einsum('bkc,chd->bhkd', c_kv, w_uv)
        scores = torch.einsum('bhqd,bhkd->bhqk', q, k) * D ** (-0.5)
        off = Sk - Sq
        qi = torch.arange(Sq)[:, None]
        kj = torch.arange(Sk)[None, :]
        cmask = kj <= qi + off
        scores = torch.where(cmask[None, None], scores, -1000000000.0)
        p = _softmax(scores, axis=-1)
        ctx = torch.einsum('bhqk,bhkd->bhqd', p, v)
        return torch.einsum('bhqd,hdm->bqm', ctx, w_o)

    def _B_ref(x, mem0, Wq, Wk, Wv, Wo, seg_len):
        B, S, Dm = x.shape
        H, Dh = (Wq.shape[1], Wq.shape[2])
        M = mem0.shape[1]
        scale = Dh ** (-0.5)
        n_seg = S // seg_len
        L = M + seg_len
        idx = torch.arange(L)
        mask = (idx[:, None] < M) | (idx[:, None] >= idx[None, :])
        mem = mem0
        outs = []
        for s in range(n_seg):
            xs = x[:, s * seg_len:(s + 1) * seg_len, :].clone()
            h = torch.concatenate([mem, xs], axis=1)
            q = torch.einsum('btd,dhe->bhte', h, Wq)
            k = torch.einsum('btd,dhe->bhte', h, Wk)
            v = torch.einsum('btd,dhe->bhte', h, Wv)
            scores = torch.einsum('bhqe,bhke->bhqk', q, k) * scale
            scores = torch.where(mask[None, None], scores, -1000000000.0)
            p = _softmax(scores, axis=-1)
            o = torch.einsum('bhqk,bhke->bhqe', p, v)
            o = torch.einsum('bhte,hed->btd', o, Wo)
            mem = o[:, :M, :].clone()
            outs.append(o[:, M:, :])
        return torch.concatenate(outs, axis=1)
    x = A_x
    g = _sigmoid((x * gate_w).sum(-1, keepdims=True))
    a = _A_ref(x, A_c_kv, A_w_q, A_w_uk, A_w_uv, A_w_o)
    b = _B_ref(x, B_mem0, B_Wq, B_Wk, B_Wv, B_Wo, B_seg_len)
    return g * a + (1 - g) * b
```

```cpp
#include <hip/hip_runtime.h>
#include <hip/hip_bf16.h>
#include <cstdint>
#include <type_traits>
#include <utility>

using bf16 = __hip_bfloat16;
typedef short bf16x8 __attribute__((ext_vector_type(8)));
typedef short s16x4 __attribute__((ext_vector_type(4)));
typedef float f32x4 __attribute__((ext_vector_type(4)));
typedef float f32x2 __attribute__((ext_vector_type(2)));
typedef unsigned u32x32 __attribute__((ext_vector_type(32)));
typedef unsigned u32x8 __attribute__((ext_vector_type(8)));
typedef unsigned u32x4 __attribute__((ext_vector_type(4)));
typedef unsigned u32x2 __attribute__((ext_vector_type(2)));
typedef __bf16 bfv8 __attribute__((ext_vector_type(8)));
typedef __bf16 bfv16 __attribute__((ext_vector_type(16)));
#define LAS __attribute__((address_space(3)))

constexpr int SEQ = 2048, DM = 512, NH = 32, DH = 128, HD = NH * DH;
constexpr int NSEG = 64, SEGL = 32;
constexpr float ATT_SCALE = 0.08838834764831845f, C2 = 1.4426950408889634f * ATT_SCALE;
constexpr size_t SZ_ACT = (size_t)SEQ * HD * 2;

#define AM 2560
#define AX 1400
#define AP 1540
#define AFM 600
#define ADELAY 9000
#define FT 800
#define NPRE 9
#define ROT 0
#define CALN 8
#define GMAP 0x76543210u
#define TCH 1
#define XT 6
#define WARM 1
#define PF 3
#define ADS 32u
#define ADM 192u
#define AS0 6
constexpr int NR = 4, SL_MEM = 0, SL_P = 3, SL_VM = 12, SLOTS = 44;
constexpr size_t SZ_RING = (size_t)(NR * SLOTS + 8) * 16384;
constexpr size_t SZ_PREG = SZ_RING + 8 * 65536;
constexpr size_t SZ_POISON = SZ_PREG + 16384;
constexpr int NPAGE = (int)(SZ_RING >> 21) + 2;
static_assert(NPAGE <= 4, "parity bits");
constexpr size_t OFF_TMO = (SZ_POISON + 255) & ~(size_t)255;
constexpr size_t OFF_MAIL = OFF_TMO + 65536;
constexpr size_t OFF_QA = OFF_MAIL + 65536;
constexpr size_t OFF_OA = OFF_QA + 3 * SZ_ACT;
constexpr size_t OFF_QB = OFF_OA + SZ_ACT;
constexpr size_t OFF_KB = OFF_QB + SZ_ACT;
constexpr size_t OFF_VOX = OFF_KB + SZ_ACT;
constexpr size_t OFF_PART = OFF_VOX + 4 * SZ_ACT;
constexpr size_t SZ_PART = (size_t)5 * SEQ * DM * 4;
constexpr size_t OFF_BB = OFF_PART + SZ_PART;
constexpr size_t OFF_G = OFF_BB + (size_t)SEQ * DM * 2;
constexpr size_t OFF_WT = OFF_G + 8192;
constexpr size_t WS_END = OFF_WT + (size_t)HD * DM * 2;
constexpr int W_CTRA = 288, W_AQ = 128, W_CTRP = 320, W_PROG = 1024, W_QCNT = 2048, W_OQ = 3584, W_LIST = 4352, W_CAL = 6656, W_VO = 8192, W_BB = 10240, W_FA = 12288;
constexpr int MASTER = 159;

typedef __bf16 bf16x2_t __attribute__((ext_vector_type(2)));
__device__ __forceinline__ unsigned cvtpk(float lo, float hi) {
    const f32x2 f = {lo, hi}; return __builtin_bit_cast(unsigned, __builtin_convertvector(f, bf16x2_t));
}
__device__ __forceinline__ float bf2f(unsigned short b) { return __uint_as_float((unsigned)b << 16); }
__device__ __forceinline__ float rbf(float x) { return bf2f((unsigned short)(cvtpk(x, x) & 0xFFFF)); }

__device__ __forceinline__ unsigned off_b(unsigned row, unsigned ch) { return 256u * row + 16u * (ch ^ (((row & 3) << 2) | ((row >> 2) & 3))); }
__device__ __forceinline__ unsigned tr_addr16(unsigned lane, unsigned c, unsigned t) {
    const unsigned g = lane >> 4, q = (lane & 15) >> 2, p = lane & 3;
    return off_b(8 * g + 4 * t + q, 2 * c + (p >> 1)) + 8 * (p & 1);
}
__device__ __forceinline__ s16x4 tr_rd(const char* lds) { return __builtin_amdgcn_ds_read_tr16_b64_v4i16((LAS s16x4*)lds); }
__device__ __forceinline__ u32x2 tr_rdu(const char* lds) { return __builtin_bit_cast(u32x2, tr_rd(lds)); }
__device__ __forceinline__ bf16x8 cat8(s16x4 a, s16x4 b) {
    const u32x2 x = __builtin_bit_cast(u32x2, a), y = __builtin_bit_cast(u32x2, b);
    const u32x4 w = {x[0], x[1], y[0], y[1]}; return __builtin_bit_cast(bf16x8, w); }
__device__ __forceinline__ f32x4 smf(u32x4 a, u32x8 b, f32x4 c, int idx) {
    return __builtin_amdgcn_smfmac_f32_16x16x64_bf16(__builtin_bit_cast(bfv8, a), __builtin_bit_cast(bfv16, b), c, idx, 0, 0); }
__device__ __forceinline__ u32x8 ilv(u32x2 a0, u32x2 a1, u32x2 b0, u32x2 b1) {
    return u32x8{a0[0], a1[0], a0[1], a1[1], b0[0], b1[0], b0[1], b1[1]}; }
__device__ __forceinline__ float swadd(float a, float b) {
    auto r = __builtin_amdgcn_permlane32_swap(__float_as_uint(a), __float_as_uint(b), false, false);
    return __uint_as_float(r[0]) + __uint_as_float(r[1]); }
__device__ __forceinline__ u32x4 mk_rs(const void* p, unsigned bytes) {
    return u32x4{(unsigned)(uintptr_t)p, (unsigned)((uintptr_t)p >> 32) & 0xFFFFu, bytes, 0x00020000u}; }
__device__ __forceinline__ u32x4 ldw(const bf16* p, unsigned v, unsigned so) {
    return __builtin_amdgcn_raw_buffer_load_b128(__builtin_amdgcn_make_buffer_rsrc((void*)p, 0, DM * HD * 2, 0x00020000), v, so, 0); }
template <bool SC1>
__device__ __forceinline__ void dma16(u32x4 rs, unsigned voff, unsigned soff, unsigned ldsaddr) {
    unsigned keep;
    soff = __builtin_amdgcn_readfirstlane(soff); ldsaddr = __builtin_amdgcn_readfirstlane(ldsaddr);
    if (SC1) asm volatile("s_mov_b32 %0, m0\n\ts_mov_b32 m0, %4\n\ts_nop 0\n\tbuffer_load_dwordx4 %1, %2, %3 offen sc1 lds\n\ts_mov_b32 m0, %0"
                          : "=&s"(keep) : "v"(voff), "s"(rs), "s"(soff), "s"(ldsaddr) : "memory");
    else asm volatile("s_mov_b32 %0, m0\n\ts_mov_b32 m0, %4\n\ts_nop 0\n\tbuffer_load_dwordx4 %1, %2, %3 offen lds\n\ts_mov_b32 m0, %0"
                      : "=&s"(keep) : "v"(voff), "s"(rs), "s"(soff), "s"(ldsaddr) : "memory");
}
__device__ __forceinline__ void dma4(u32x4 rs, unsigned voff, unsigned ldsaddr) {
    unsigned keep;
    ldsaddr = __builtin_amdgcn_readfirstlane(ldsaddr);
    asm volatile("s_mov_b32 %0, m0\n\ts_mov_b32 m0, %3\n\ts_nop 0\n\tbuffer_load_dword %1, %2, 0 offen sc1 lds\n\ts_mov_b32 m0, %0"
                 : "=&s"(keep) : "v"(voff), "s"(rs), "s"(ldsaddr) : "memory");
}
#define LDS_BARRIER() do { asm volatile("s_waitcnt lgkmcnt(0)" ::: "memory"); __builtin_amdgcn_s_barrier(); asm volatile("" ::: "memory"); } while (0)
#define LRD(d, a, o) asm volatile("ds_read_b128 %0, %1 offset:%2" : "=v"(d) : "v"(a), "i"(o))
#define LWAIT1(n, a) asm volatile("s_waitcnt lgkmcnt(%1)" : "+v"(a) : "i"(n))
#define LWAIT2(n, a, b) asm volatile("s_waitcnt lgkmcnt(" #n ")" : "+v"(a), "+v"(b))
#define VMWAIT(n) asm volatile("s_waitcnt vmcnt(%0)" :: "i"(n) : "memory")

constexpr unsigned SPIN_CAP = 1u << 17;
__device__ __forceinline__ bool bail(unsigned spins, unsigned cap, unsigned* tmo, unsigned code) {
    if ((spins & 63u) == 63u && __hip_atomic_load(tmo, __ATOMIC_RELAXED, __HIP_MEMORY_SCOPE_AGENT) != 0u) return true;
    if (spins > cap) { __hip_atomic_store(tmo, code, __ATOMIC_RELAXED, __HIP_MEMORY_SCOPE_AGENT); return true; }
    return false; }
template <int SLP>
__device__ __forceinline__ unsigned wave_wait(unsigned* w, unsigned target, unsigned* tmo, unsigned code, unsigned mask = ~0u) {
    unsigned v;
    for (unsigned spins = 0;; ++spins) {
        v = __builtin_amdgcn_readfirstlane(__hip_atomic_load(w, __ATOMIC_RELAXED, __HIP_MEMORY_SCOPE_AGENT));
        if ((v & mask) >= target) break;
        if (bail(spins, SPIN_CAP << 1, tmo, code)) break;
        __builtin_amdgcn_s_sleep(SLP); asm volatile("" ::: "memory");
    }
    return v;
}
__device__ __forceinline__ unsigned pin_s(unsigned x) { x = __builtin_amdgcn_readfirstlane(x); asm volatile("" : "+s"(x)); return x; }
constexpr int NPA = 96;
struct Hello {
    unsigned long long* rep; unsigned long long num;
#define PRB(k) do {} while (0)
    __device__ __forceinline__ unsigned long long peek() const { return __hip_atomic_load(rep, __ATOMIC_RELAXED, __HIP_MEMORY_SCOPE_AGENT); }
    __device__ __forceinline__ void wait(unsigned long long v) const {
        for (unsigned i = 0; v != num; ++i) {
            if (i > SPIN_CAP) { __hip_atomic_store((unsigned*)((char*)(rep - blockIdx.x) - 98304), 0xC0000u, __ATOMIC_RELAXED, __HIP_MEMORY_SCOPE_AGENT); break; }
            __builtin_amdgcn_s_sleep(4); v = peek(); } }
};

#define ASM_STAGE_PANEL \
    "s_mov_b32 s60, m0\n\tv_add_u32 v16, 0x10000, %[a0]\n\tv_add_u32 v17, 0x10000, %[a1]\n\ts_add_u32 m0, %[lb], 0\n\ts_mov_b32 s61, 0\n\tbuffer_load_dwordx4 %[wv], %[rw], s61 offen lds\n\t" \
    "s_add_u32 m0, %[lb], 1024\n\ts_mov_b32 s61, 65536\n\tbuffer_load_dwordx4 %[wv], %[rw], s61 offen lds\n\ts_add_u32 m0, %[lb], 2048\n\ts_mov_b32 s61, 131072\n\t" \
    "buffer_load_dwordx4 %[wv], %[rw], s61 offen lds\n\ts_add_u32 m0, %[lb], 3072\n\ts_mov_b32 s61, 196608\n\tbuffer_load_dwordx4 %[wv], %[rw], s61 offen lds\n\ts_add_u32 m0, %[lb], 16384\n\t" \
    "s_mov_b32 s61, 262144\n\tbuffer_load_dwordx4 %[wv], %[rw], s61 offen lds\n\ts_add_u32 m0, %[lb], 17408\n\ts_mov_b32 s61, 327680\n\tbuffer_load_dwordx4 %[wv], %[rw], s61 offen lds\n\t" \
    "s_add_u32 m0, %[lb], 18432\n\ts_mov_b32 s61, 393216\n\tbuffer_load_dwordx4 %[wv], %[rw], s61 offen lds\n\ts_add_u32 m0, %[lb], 19456\n\ts_mov_b32 s61, 458752\n\t" \
    "buffer_load_dwordx4 %[wv], %[rw], s61 offen lds\n\ts_add_u32 m0, %[lb], 32768\n\ts_mov_b32 s61, 524288\n\tbuffer_load_dwordx4 %[wv], %[rw], s61 offen lds\n\ts_add_u32 m0, %[lb], 33792\n\t" \
    "s_mov_b32 s61, 589824\n\tbuffer_load_dwordx4 %[wv], %[rw], s61 offen lds\n\ts_add_u32 m0, %[lb], 34816\n\ts_mov_b32 s61, 655360\n\tbuffer_load_dwordx4 %[wv], %[rw], s61 offen lds\n\t" \
    "s_add_u32 m0, %[lb], 35840\n\ts_mov_b32 s61, 720896\n\tbuffer_load_dwordx4 %[wv], %[rw], s61 offen lds\n\ts_add_u32 m0, %[lb], 49152\n\ts_mov_b32 s61, 786432\n\t" \
    "buffer_load_dwordx4 %[wv], %[rw], s61 offen lds\n\ts_add_u32 m0, %[lb], 50176\n\ts_mov_b32 s61, 851968\n\tbuffer_load_dwordx4 %[wv], %[rw], s61 offen lds\n\ts_add_u32 m0, %[lb], 51200\n\t" \
    "s_mov_b32 s61, 917504\n\tbuffer_load_dwordx4 %[wv], %[rw], s61 offen lds\n\ts_add_u32 m0, %[lb], 52224\n\ts_mov_b32 s61, 983040\n\tbuffer_load_dwordx4 %[wv], %[rw], s61 offen lds\n\t" \
    "s_add_u32 m0, %[lb], 65536\n\ts_mov_b32 s61, 1048576\n\tbuffer_load_dwordx4 %[wv], %[rw], s61 offen lds\n\ts_add_u32 m0, %[lb], 66560\n\ts_mov_b32 s61, 1114112\n\t" \
    "buffer_load_dwordx4 %[wv], %[rw], s61 offen lds\n\ts_add_u32 m0, %[lb], 67584\n\ts_mov_b32 s61, 1179648\n\tbuffer_load_dwordx4 %[wv], %[rw], s61 offen lds\n\ts_add_u32 m0, %[lb], 68608\n\t" \
    "s_mov_b32 s61, 1245184\n\tbuffer_load_dwordx4 %[wv], %[rw], s61 offen lds\n\ts_add_u32 m0, %[lb], 81920\n\ts_mov_b32 s61, 1310720\n\tbuffer_load_dwordx4 %[wv], %[rw], s61 offen lds\n\t" \
    "s_add_u32 m0, %[lb], 82944\n\ts_mov_b32 s61, 1376256\n\tbuffer_load_dwordx4 %[wv], %[rw], s61 offen lds\n\ts_add_u32 m0, %[lb], 83968\n\ts_mov_b32 s61, 1441792\n\t" \
    "buffer_load_dwordx4 %[wv], %[rw], s61 offen lds\n\ts_add_u32 m0, %[lb], 84992\n\ts_mov_b32 s61, 1507328\n\tbuffer_load_dwordx4 %[wv], %[rw], s61 offen lds\n\ts_add_u32 m0, %[lb], 98304\n\t" \
    "s_mov_b32 s61, 1572864\n\tbuffer_load_dwordx4 %[wv], %[rw], s61 offen lds\n\ts_add_u32 m0, %[lb], 99328\n\ts_mov_b32 s61, 1638400\n\tbuffer_load_dwordx4 %[wv], %[rw], s61 offen lds\n\t" \
    "s_add_u32 m0, %[lb], 100352\n\ts_mov_b32 s61, 1703936\n\tbuffer_load_dwordx4 %[wv], %[rw], s61 offen lds\n\ts_add_u32 m0, %[lb], 101376\n\ts_mov_b32 s61, 1769472\n\t" \
    "buffer_load_dwordx4 %[wv], %[rw], s61 offen lds\n\ts_add_u32 m0, %[lb], 114688\n\ts_mov_b32 s61, 1835008\n\tbuffer_load_dwordx4 %[wv], %[rw], s61 offen lds\n\ts_add_u32 m0, %[lb], 115712\n\t" \
    "s_mov_b32 s61, 1900544\n\tbuffer_load_dwordx4 %[wv], %[rw], s61 offen lds\n\ts_add_u32 m0, %[lb], 116736\n\ts_mov_b32 s61, 1966080\n\tbuffer_load_dwordx4 %[wv], %[rw], s61 offen lds\n\t" \
    "s_add_u32 m0, %[lb], 117760\n\ts_mov_b32 s61, 2031616\n\tbuffer_load_dwordx4 %[wv], %[rw], s61 offen lds\n\ts_waitcnt vmcnt(28)\n\tds_read_b64_tr_b16 a[0:1], %[a0]\n\t" \
    "ds_read_b64_tr_b16 a[2:3], %[a0] offset:512\n\tds_read_b64_tr_b16 a[64:65], %[a0] offset:8\n\tds_read_b64_tr_b16 a[66:67], %[a0] offset:520\n\tds_read_b64_tr_b16 a[128:129], %[a1]\n\t" \
    "ds_read_b64_tr_b16 a[130:131], %[a1] offset:512\n\tds_read_b64_tr_b16 a[192:193], %[a1] offset:8\n\tds_read_b64_tr_b16 a[194:195], %[a1] offset:520\n\ts_waitcnt lgkmcnt(0)\n\t" \
    "s_add_u32 m0, %[lb], 0\n\ts_mov_b32 s61, 2097152\n\tbuffer_load_dwordx4 %[wv], %[rw], s61 offen lds\n\ts_add_u32 m0, %[lb], 1024\n\ts_mov_b32 s61, 2162688\n\t" \
    "buffer_load_dwordx4 %[wv], %[rw], s61 offen lds\n\ts_add_u32 m0, %[lb], 2048\n\ts_mov_b32 s61, 2228224\n\tbuffer_load_dwordx4 %[wv], %[rw], s61 offen lds\n\ts_add_u32 m0, %[lb], 3072\n\t" \
    "s_mov_b32 s61, 2293760\n\tbuffer_load_dwordx4 %[wv], %[rw], s61 offen lds\n\ts_waitcnt vmcnt(28)\n\tds_read_b64_tr_b16 a[4:5], %[a0] offset:16384\n\tds_read_b64_tr_b16 a[6:7], %[a0] offset:16896\n\t" \
    "ds_read_b64_tr_b16 a[68:69], %[a0] offset:16392\n\tds_read_b64_tr_b16 a[70:71], %[a0] offset:16904\n\tds_read_b64_tr_b16 a[132:133], %[a1] offset:16384\n\t" \
    "ds_read_b64_tr_b16 a[134:135], %[a1] offset:16896\n\tds_read_b64_tr_b16 a[196:197], %[a1] offset:16392\n\tds_read_b64_tr_b16 a[198:199], %[a1] offset:16904\n\ts_waitcnt lgkmcnt(0)\n\t" \
    "s_add_u32 m0, %[lb], 16384\n\ts_mov_b32 s61, 2359296\n\tbuffer_load_dwordx4 %[wv], %[rw], s61 offen lds\n\ts_add_u32 m0, %[lb], 17408\n\ts_mov_b32 s61, 2424832\n\t" \
    "buffer_load_dwordx4 %[wv], %[rw], s61 offen lds\n\ts_add_u32 m0, %[lb], 18432\n\ts_mov_b32 s61, 2490368\n\tbuffer_load_dwordx4 %[wv], %[rw], s61 offen lds\n\ts_add_u32 m0, %[lb], 19456\n\t" \
    "s_mov_b32 s61, 2555904\n\tbuffer_load_dwordx4 %[wv], %[rw], s61 offen lds\n\ts_waitcnt vmcnt(28)\n\tds_read_b64_tr_b16 a[8:9], %[a0] offset:32768\n\tds_read_b64_tr_b16 a[10:11], %[a0] offset:33280\n\t" \
    "ds_read_b64_tr_b16 a[72:73], %[a0] offset:32776\n\tds_read_b64_tr_b16 a[74:75], %[a0] offset:33288\n\tds_read_b64_tr_b16 a[136:137], %[a1] offset:32768\n\t" \
    "ds_read_b64_tr_b16 a[138:139], %[a1] offset:33280\n\tds_read_b64_tr_b16 a[200:201], %[a1] offset:32776\n\tds_read_b64_tr_b16 a[202:203], %[a1] offset:33288\n\ts_waitcnt lgkmcnt(0)\n\t" \
    "s_add_u32 m0, %[lb], 32768\n\ts_mov_b32 s61, 2621440\n\tbuffer_load_dwordx4 %[wv], %[rw], s61 offen lds\n\ts_add_u32 m0, %[lb], 33792\n\ts_mov_b32 s61, 2686976\n\t" \
    "buffer_load_dwordx4 %[wv], %[rw], s61 offen lds\n\ts_add_u32 m0, %[lb], 34816\n\ts_mov_b32 s61, 2752512\n\tbuffer_load_dwordx4 %[wv], %[rw], s61 offen lds\n\ts_add_u32 m0, %[lb], 35840\n\t" \
    "s_mov_b32 s61, 2818048\n\tbuffer_load_dwordx4 %[wv], %[rw], s61 offen lds\n\ts_waitcnt vmcnt(28)\n\tds_read_b64_tr_b16 a[12:13], %[a0] offset:49152\n\t" \
    "ds_read_b64_tr_b16 a[14:15], %[a0] offset:49664\n\tds_read_b64_tr_b16 a[76:77], %[a0] offset:49160\n\tds_read_b64_tr_b16 a[78:79], %[a0] offset:49672\n\t" \
    "ds_read_b64_tr_b16 a[140:141], %[a1] offset:49152\n\tds_read_b64_tr_b16 a[142:143], %[a1] offset:49664\n\tds_read_b64_tr_b16 a[204:205], %[a1] offset:49160\n\t" \
    "ds_read_b64_tr_b16 a[206:207], %[a1] offset:49672\n\ts_waitcnt lgkmcnt(0)\n\ts_add_u32 m0, %[lb], 49152\n\ts_mov_b32 s61, 2883584\n\tbuffer_load_dwordx4 %[wv], %[rw], s61 offen lds\n\t" \
    "s_add_u32 m0, %[lb], 50176\n\ts_mov_b32 s61, 2949120\n\tbuffer_load_dwordx4 %[wv], %[rw], s61 offen lds\n\ts_add_u32 m0, %[lb], 51200\n\ts_mov_b32 s61, 3014656\n\t" \
    "buffer_load_dwordx4 %[wv], %[rw], s61 offen lds\n\ts_add_u32 m0, %[lb], 52224\n\ts_mov_b32 s61, 3080192\n\tbuffer_load_dwordx4 %[wv], %[rw], s61 offen lds\n\ts_waitcnt vmcnt(28)\n\t" \
    "ds_read_b64_tr_b16 a[16:17], v16\n\tds_read_b64_tr_b16 a[18:19], v16 offset:512\n\tds_read_b64_tr_b16 a[80:81], v16 offset:8\n\tds_read_b64_tr_b16 a[82:83], v16 offset:520\n\t" \
    "ds_read_b64_tr_b16 a[144:145], v17\n\tds_read_b64_tr_b16 a[146:147], v17 offset:512\n\tds_read_b64_tr_b16 a[208:209], v17 offset:8\n\tds_read_b64_tr_b16 a[210:211], v17 offset:520\n\t" \
    "s_waitcnt lgkmcnt(0)\n\ts_add_u32 m0, %[lb], 65536\n\ts_mov_b32 s61, 3145728\n\tbuffer_load_dwordx4 %[wv], %[rw], s61 offen lds\n\ts_add_u32 m0, %[lb], 66560\n\ts_mov_b32 s61, 3211264\n\t" \
    "buffer_load_dwordx4 %[wv], %[rw], s61 offen lds\n\ts_add_u32 m0, %[lb], 67584\n\ts_mov_b32 s61, 3276800\n\tbuffer_load_dwordx4 %[wv], %[rw], s61 offen lds\n\ts_add_u32 m0, %[lb], 68608\n\t" \
    "s_mov_b32 s61, 3342336\n\tbuffer_load_dwordx4 %[wv], %[rw], s61 offen lds\n\ts_waitcnt vmcnt(28)\n\tds_read_b64_tr_b16 a[20:21], v16 offset:16384\n\tds_read_b64_tr_b16 a[22:23], v16 offset:16896\n\t" \
    "ds_read_b64_tr_b16 a[84:85], v16 offset:16392\n\tds_read_b64_tr_b16 a[86:87], v16 offset:16904\n\tds_read_b64_tr_b16 a[148:149], v17 offset:16384\n\tds_read_b64_tr_b16 a[150:151], v17 offset:16896\n\t" \
    "ds_read_b64_tr_b16 a[212:213], v17 offset:16392\n\tds_read_b64_tr_b16 a[214:215], v17 offset:16904\n\ts_waitcnt lgkmcnt(0)\n\ts_add_u32 m0, %[lb], 81920\n\ts_mov_b32 s61, 3407872\n\t" \
    "buffer_load_dwordx4 %[wv], %[rw], s61 offen lds\n\ts_add_u32 m0, %[lb], 82944\n\ts_mov_b32 s61, 3473408\n\tbuffer_load_dwordx4 %[wv], %[rw], s61 offen lds\n\ts_add_u32 m0, %[lb], 83968\n\t" \
    "s_mov_b32 s61, 3538944\n\tbuffer_load_dwordx4 %[wv], %[rw], s61 offen lds\n\ts_add_u32 m0, %[lb], 84992\n\ts_mov_b32 s61, 3604480\n\tbuffer_load_dwordx4 %[wv], %[rw], s61 offen lds\n\t" \
    "s_waitcnt vmcnt(28)\n\tds_read_b64_tr_b16 a[24:25], v16 offset:32768\n\tds_read_b64_tr_b16 a[26:27], v16 offset:33280\n\tds_read_b64_tr_b16 a[88:89], v16 offset:32776\n\t" \
    "ds_read_b64_tr_b16 a[90:91], v16 offset:33288\n\tds_read_b64_tr_b16 a[152:153], v17 offset:32768\n\tds_read_b64_tr_b16 a[154:155], v17 offset:33280\n\t" \
    "ds_read_b64_tr_b16 a[216:217], v17 offset:32776\n\tds_read_b64_tr_b16 a[218:219], v17 offset:33288\n\ts_waitcnt lgkmcnt(0)\n\ts_add_u32 m0, %[lb], 98304\n\ts_mov_b32 s61, 3670016\n\t" \
    "buffer_load_dwordx4 %[wv], %[rw], s61 offen lds\n\ts_add_u32 m0, %[lb], 99328\n\ts_mov_b32 s61, 3735552\n\tbuffer_load_dwordx4 %[wv], %[rw], s61 offen lds\n\ts_add_u32 m0, %[lb], 100352\n\t" \
    "s_mov_b32 s61, 3801088\n\tbuffer_load_dwordx4 %[wv], %[rw], s61 offen lds\n\ts_add_u32 m0, %[lb], 101376\n\ts_mov_b32 s61, 3866624\n\tbuffer_load_dwordx4 %[wv], %[rw], s61 offen lds\n\t" \
    "s_waitcnt vmcnt(28)\n\tds_read_b64_tr_b16 a[28:29], v16 offset:49152\n\tds_read_b64_tr_b16 a[30:31], v16 offset:49664\n\tds_read_b64_tr_b16 a[92:93], v16 offset:49160\n\t" \
    "ds_read_b64_tr_b16 a[94:95], v16 offset:49672\n\tds_read_b64_tr_b16 a[156:157], v17 offset:49152\n\tds_read_b64_tr_b16 a[158:159], v17 offset:49664\n\t" \
    "ds_read_b64_tr_b16 a[220:221], v17 offset:49160\n\tds_read_b64_tr_b16 a[222:223], v17 offset:49672\n\ts_waitcnt lgkmcnt(0)\n\ts_add_u32 m0, %[lb], 114688\n\ts_mov_b32 s61, 3932160\n\t" \
    "buffer_load_dwordx4 %[wv], %[rw], s61 offen lds\n\ts_add_u32 m0, %[lb], 115712\n\ts_mov_b32 s61, 3997696\n\tbuffer_load_dwordx4 %[wv], %[rw], s61 offen lds\n\ts_add_u32 m0, %[lb], 116736\n\t" \
    "s_mov_b32 s61, 4063232\n\tbuffer_load_dwordx4 %[wv], %[rw], s61 offen lds\n\ts_add_u32 m0, %[lb], 117760\n\ts_mov_b32 s61, 4128768\n\tbuffer_load_dwordx4 %[wv], %[rw], s61 offen lds\n\t" \
    "s_waitcnt vmcnt(28)\n\tds_read_b64_tr_b16 a[32:33], %[a0]\n\tds_read_b64_tr_b16 a[34:35], %[a0] offset:512\n\tds_read_b64_tr_b16 a[96:97], %[a0] offset:8\n\t" \
    "ds_read_b64_tr_b16 a[98:99], %[a0] offset:520\n\tds_read_b64_tr_b16 a[160:161], %[a1]\n\tds_read_b64_tr_b16 a[162:163], %[a1] offset:512\n\tds_read_b64_tr_b16 a[224:225], %[a1] offset:8\n\t" \
    "ds_read_b64_tr_b16 a[226:227], %[a1] offset:520\n\ts_waitcnt lgkmcnt(0)\n\ts_waitcnt vmcnt(24)\n\tds_read_b64_tr_b16 a[36:37], %[a0] offset:16384\n\tds_read_b64_tr_b16 a[38:39], %[a0] offset:16896\n\t" \
    "ds_read_b64_tr_b16 a[100:101], %[a0] offset:16392\n\tds_read_b64_tr_b16 a[102:103], %[a0] offset:16904\n\tds_read_b64_tr_b16 a[164:165], %[a1] offset:16384\n\t" \
    "ds_read_b64_tr_b16 a[166:167], %[a1] offset:16896\n\tds_read_b64_tr_b16 a[228:229], %[a1] offset:16392\n\tds_read_b64_tr_b16 a[230:231], %[a1] offset:16904\n\ts_waitcnt lgkmcnt(0)\n\t" \
    "s_waitcnt vmcnt(20)\n\tds_read_b64_tr_b16 a[40:41], %[a0] offset:32768\n\tds_read_b64_tr_b16 a[42:43], %[a0] offset:33280\n\tds_read_b64_tr_b16 a[104:105], %[a0] offset:32776\n\t" \
    "ds_read_b64_tr_b16 a[106:107], %[a0] offset:33288\n\tds_read_b64_tr_b16 a[168:169], %[a1] offset:32768\n\tds_read_b64_tr_b16 a[170:171], %[a1] offset:33280\n\t" \
    "ds_read_b64_tr_b16 a[232:233], %[a1] offset:32776\n\tds_read_b64_tr_b16 a[234:235], %[a1] offset:33288\n\ts_waitcnt lgkmcnt(0)\n\ts_waitcnt vmcnt(16)\n\t" \
    "ds_read_b64_tr_b16 a[44:45], %[a0] offset:49152\n\tds_read_b64_tr_b16 a[46:47], %[a0] offset:49664\n\tds_read_b64_tr_b16 a[108:109], %[a0] offset:49160\n\t" \
    "ds_read_b64_tr_b16 a[110:111], %[a0] offset:49672\n\tds_read_b64_tr_b16 a[172:173], %[a1] offset:49152\n\tds_read_b64_tr_b16 a[174:175], %[a1] offset:49664\n\t" \
    "ds_read_b64_tr_b16 a[236:237], %[a1] offset:49160\n\tds_read_b64_tr_b16 a[238:239], %[a1] offset:49672\n\ts_waitcnt lgkmcnt(0)\n\ts_waitcnt vmcnt(12)\n\tds_read_b64_tr_b16 a[48:49], v16\n\t" \
    "ds_read_b64_tr_b16 a[50:51], v16 offset:512\n\tds_read_b64_tr_b16 a[112:113], v16 offset:8\n\tds_read_b64_tr_b16 a[114:115], v16 offset:520\n\tds_read_b64_tr_b16 a[176:177], v17\n\t" \
    "ds_read_b64_tr_b16 a[178:179], v17 offset:512\n\tds_read_b64_tr_b16 a[240:241], v17 offset:8\n\tds_read_b64_tr_b16 a[242:243], v17 offset:520\n\ts_waitcnt lgkmcnt(0)\n\ts_waitcnt vmcnt(8)\n\t" \
    "ds_read_b64_tr_b16 a[52:53], v16 offset:16384\n\tds_read_b64_tr_b16 a[54:55], v16 offset:16896\n\tds_read_b64_tr_b16 a[116:117], v16 offset:16392\n\tds_read_b64_tr_b16 a[118:119], v16 offset:16904\n\t" \
    "ds_read_b64_tr_b16 a[180:181], v17 offset:16384\n\tds_read_b64_tr_b16 a[182:183], v17 offset:16896\n\tds_read_b64_tr_b16 a[244:245], v17 offset:16392\n\t" \
    "ds_read_b64_tr_b16 a[246:247], v17 offset:16904\n\ts_waitcnt lgkmcnt(0)\n\ts_waitcnt vmcnt(4)\n\tds_read_b64_tr_b16 a[56:57], v16 offset:32768\n\tds_read_b64_tr_b16 a[58:59], v16 offset:33280\n\t" \
    "ds_read_b64_tr_b16 a[120:121], v16 offset:32776\n\tds_read_b64_tr_b16 a[122:123], v16 offset:33288\n\tds_read_b64_tr_b16 a[184:185], v17 offset:32768\n\t" \
    "ds_read_b64_tr_b16 a[186:187], v17 offset:33280\n\tds_read_b64_tr_b16 a[248:249], v17 offset:32776\n\tds_read_b64_tr_b16 a[250:251], v17 offset:33288\n\ts_waitcnt lgkmcnt(0)\n\ts_waitcnt vmcnt(0)\n\t" \
    "ds_read_b64_tr_b16 a[60:61], v16 offset:49152\n\tds_read_b64_tr_b16 a[62:63], v16 offset:49664\n\tds_read_b64_tr_b16 a[124:125], v16 offset:49160\n\tds_read_b64_tr_b16 a[126:127], v16 offset:49672\n\t" \
    "ds_read_b64_tr_b16 a[188:189], v17 offset:49152\n\tds_read_b64_tr_b16 a[190:191], v17 offset:49664\n\tds_read_b64_tr_b16 a[252:253], v17 offset:49160\n\t" \
    "ds_read_b64_tr_b16 a[254:255], v17 offset:49672\n\ts_waitcnt lgkmcnt(0)\n\ts_mov_b32 m0, s60\n\t"
#define ASM_PROJ_Q \
    "s_mov_b32 s60, m0\n\ts_mov_b32 s61, 0\n\tv_add_u32 v96, 0x18000, %[xb]\n\tv_xor_b32 v97, 64, v96\n\tv_bfrev_b32 v98, 1\n\tds_read_b128 v[16:19], v96\n\tds_read_b128 v[20:23], v97\n\t" \
    "ds_read_b128 v[24:27], v96 offset:1024\n\ts_waitcnt lgkmcnt(2)\n\t1:\nv_mfma_f32_16x16x32_bf16 v[48:51], a[0:3], v[16:19], 0\n\tds_read_b128 v[28:31], v97 offset:1024\n\t" \
    "v_mfma_f32_16x16x32_bf16 v[52:55], a[64:67], v[16:19], 0\n\ts_lshl_b32 s62, s61, 15\n\tv_mfma_f32_16x16x32_bf16 v[56:59], a[128:131], v[16:19], 0\n\ts_and_b32 s62, s62, 0x18000\n\t" \
    "v_mul_f32 v64, %[sc], v64\n\tv_mul_f32 v65, %[sc], v65\n\tv_mfma_f32_16x16x32_bf16 v[60:63], a[192:195], v[16:19], 0\n\ts_waitcnt lgkmcnt(2)\n\tv_mul_f32 v66, %[sc], v66\n\t" \
    "v_mul_f32 v67, %[sc], v67\n\tv_mfma_f32_16x16x32_bf16 v[60:63], a[196:199], v[20:23], v[60:63]\n\tds_read_b128 v[32:35], v96 offset:2048\n\tv_cvt_pk_bf16_f32 v88, v64, v65\n\t" \
    "v_mfma_f32_16x16x32_bf16 v[56:59], a[132:135], v[20:23], v[56:59]\n\ts_xor_b32 s63, s62, 0x10000\n\tv_cvt_pk_bf16_f32 v89, v66, v67\n\tv_mul_f32 v68, %[sc], v68\n\t" \
    "v_mfma_f32_16x16x32_bf16 v[52:55], a[68:71], v[20:23], v[52:55]\n\ts_add_u32 s63, s63, %[lb]\n\tv_mul_f32 v69, %[sc], v69\n\tv_mul_f32 v70, %[sc], v70\n\t" \
    "v_mfma_f32_16x16x32_bf16 v[48:51], a[4:7], v[20:23], v[48:51]\n\ts_waitcnt lgkmcnt(2)\n\tv_mul_f32 v71, %[sc], v71\n\tv_cvt_pk_bf16_f32 v90, v68, v69\n\t" \
    "v_mfma_f32_16x16x32_bf16 v[48:51], a[8:11], v[24:27], v[48:51]\n\tds_read_b128 v[36:39], v97 offset:2048\n\tv_cvt_pk_bf16_f32 v91, v70, v71\n\t" \
    "v_mfma_f32_16x16x32_bf16 v[52:55], a[72:75], v[24:27], v[52:55]\n\tbuffer_store_dwordx4 v[88:91], v98, %[rc], %[sp] offen offset:1024 sc1\n\tv_mul_f32 v72, %[sc], v72\n\t" \
    "v_mfma_f32_16x16x32_bf16 v[56:59], a[136:139], v[24:27], v[56:59]\n\tv_mul_f32 v73, %[sc], v73\n\tv_mul_f32 v74, %[sc], v74\n\tv_mfma_f32_16x16x32_bf16 v[60:63], a[200:203], v[24:27], v[60:63]\n\t" \
    "s_waitcnt lgkmcnt(2)\n\tv_mul_f32 v75, %[sc], v75\n\tv_cvt_pk_bf16_f32 v92, v72, v73\n\tv_mfma_f32_16x16x32_bf16 v[60:63], a[204:207], v[28:31], v[60:63]\n\tds_read_b128 v[40:43], v96 offset:3072\n\t" \
    "v_cvt_pk_bf16_f32 v93, v74, v75\n\tv_mfma_f32_16x16x32_bf16 v[56:59], a[140:143], v[28:31], v[56:59]\n\tv_mul_f32 v76, %[sc], v76\n\tv_mul_f32 v77, %[sc], v77\n\t" \
    "v_mfma_f32_16x16x32_bf16 v[52:55], a[76:79], v[28:31], v[52:55]\n\tv_mul_f32 v78, %[sc], v78\n\tv_mul_f32 v79, %[sc], v79\n\tv_mfma_f32_16x16x32_bf16 v[48:51], a[12:15], v[28:31], v[48:51]\n\t" \
    "s_waitcnt lgkmcnt(2)\n\tv_cvt_pk_bf16_f32 v94, v76, v77\n\tv_cvt_pk_bf16_f32 v95, v78, v79\n\tv_mfma_f32_16x16x32_bf16 v[48:51], a[16:19], v[32:35], v[48:51]\n\t" \
    "ds_read_b128 v[44:47], v97 offset:3072\n\tbuffer_store_dwordx4 v[92:95], v98, %[rc], %[sp] offen offset:3072 sc1\n\tv_mfma_f32_16x16x32_bf16 v[52:55], a[80:83], v[32:35], v[52:55]\n\t" \
    "v_mov_b32 v98, %[co]\n\ts_add_u32 %[sp], %[sp], %[c18]\n\tv_mfma_f32_16x16x32_bf16 v[56:59], a[144:147], v[32:35], v[56:59]\n\tv_mfma_f32_16x16x32_bf16 v[60:63], a[208:211], v[32:35], v[60:63]\n\t" \
    "s_waitcnt lgkmcnt(2)\n\tv_mfma_f32_16x16x32_bf16 v[60:63], a[212:215], v[36:39], v[60:63]\n\tds_read_b128 v[16:19], v96 offset:4096\n\t" \
    "v_mfma_f32_16x16x32_bf16 v[56:59], a[148:151], v[36:39], v[56:59]\n\tv_mfma_f32_16x16x32_bf16 v[52:55], a[84:87], v[36:39], v[52:55]\n\t" \
    "v_mfma_f32_16x16x32_bf16 v[48:51], a[20:23], v[36:39], v[48:51]\n\ts_waitcnt lgkmcnt(2)\n\tv_mfma_f32_16x16x32_bf16 v[48:51], a[24:27], v[40:43], v[48:51]\n\tds_read_b128 v[20:23], v97 offset:4096\n\t" \
    "v_mfma_f32_16x16x32_bf16 v[52:55], a[88:91], v[40:43], v[52:55]\n\tv_mfma_f32_16x16x32_bf16 v[56:59], a[152:155], v[40:43], v[56:59]\n\t" \
    "v_mfma_f32_16x16x32_bf16 v[60:63], a[216:219], v[40:43], v[60:63]\n\ts_waitcnt lgkmcnt(2)\n\tv_mfma_f32_16x16x32_bf16 v[60:63], a[220:223], v[44:47], v[60:63]\n\t" \
    "ds_read_b128 v[24:27], v96 offset:5120\n\tv_mfma_f32_16x16x32_bf16 v[56:59], a[156:159], v[44:47], v[56:59]\n\tv_mfma_f32_16x16x32_bf16 v[52:55], a[92:95], v[44:47], v[52:55]\n\t" \
    "v_mfma_f32_16x16x32_bf16 v[48:51], a[28:31], v[44:47], v[48:51]\n\ts_waitcnt lgkmcnt(2)\n\tv_mfma_f32_16x16x32_bf16 v[48:51], a[32:35], v[16:19], v[48:51]\n\tds_read_b128 v[28:31], v97 offset:5120\n\t" \
    "v_mfma_f32_16x16x32_bf16 v[52:55], a[96:99], v[16:19], v[52:55]\n\tv_mfma_f32_16x16x32_bf16 v[56:59], a[160:163], v[16:19], v[56:59]\n\t" \
    "v_mfma_f32_16x16x32_bf16 v[60:63], a[224:227], v[16:19], v[60:63]\n\ts_waitcnt lgkmcnt(2)\n\tv_mfma_f32_16x16x32_bf16 v[60:63], a[228:231], v[20:23], v[60:63]\n\t" \
    "ds_read_b128 v[32:35], v96 offset:6144\n\tv_mfma_f32_16x16x32_bf16 v[56:59], a[164:167], v[20:23], v[56:59]\n\tv_mfma_f32_16x16x32_bf16 v[52:55], a[100:103], v[20:23], v[52:55]\n\t" \
    "v_mfma_f32_16x16x32_bf16 v[48:51], a[36:39], v[20:23], v[48:51]\n\ts_waitcnt lgkmcnt(2)\n\tv_mfma_f32_16x16x32_bf16 v[48:51], a[40:43], v[24:27], v[48:51]\n\tds_read_b128 v[36:39], v97 offset:6144\n\t" \
    "v_mfma_f32_16x16x32_bf16 v[52:55], a[104:107], v[24:27], v[52:55]\n\tv_mfma_f32_16x16x32_bf16 v[56:59], a[168:171], v[24:27], v[56:59]\n\t" \
    "v_mfma_f32_16x16x32_bf16 v[60:63], a[232:235], v[24:27], v[60:63]\n\ts_waitcnt lgkmcnt(2)\n\tv_mfma_f32_16x16x32_bf16 v[60:63], a[236:239], v[28:31], v[60:63]\n\t" \
    "ds_read_b128 v[40:43], v96 offset:7168\n\tv_mfma_f32_16x16x32_bf16 v[56:59], a[172:175], v[28:31], v[56:59]\n\tv_mfma_f32_16x16x32_bf16 v[52:55], a[108:111], v[28:31], v[52:55]\n\t" \
    "v_mfma_f32_16x16x32_bf16 v[48:51], a[44:47], v[28:31], v[48:51]\n\ts_waitcnt lgkmcnt(2)\n\tv_mfma_f32_16x16x32_bf16 v[48:51], a[48:51], v[32:35], v[48:51]\n\ts_waitcnt vmcnt(10)\n\ts_barrier\n\t" \
    "s_mov_b32 m0, s63\n\tds_read_b128 v[44:47], v97 offset:7168\n\tv_mfma_f32_16x16x32_bf16 v[52:55], a[112:115], v[32:35], v[52:55]\n\t" \
    "v_mfma_f32_16x16x32_bf16 v[56:59], a[176:179], v[32:35], v[56:59]\n\tbuffer_load_dwordx4 %[dv], %[ra], %[so] offen lds\n\tv_mfma_f32_16x16x32_bf16 v[60:63], a[240:243], v[32:35], v[60:63]\n\t" \
    "s_waitcnt lgkmcnt(2)\n\ts_add_u32 m0, m0, 896\n\tv_mfma_f32_16x16x32_bf16 v[60:63], a[244:247], v[36:39], v[60:63]\n\tds_read_b128 v[16:19], v96 offset:16384\n\t" \
    "v_mfma_f32_16x16x32_bf16 v[56:59], a[180:183], v[36:39], v[56:59]\n\tv_mfma_f32_16x16x32_bf16 v[52:55], a[116:119], v[36:39], v[52:55]\n\t" \
    "buffer_load_dwordx4 %[dv], %[ra], %[so] offen offset:128 lds\n\tv_mfma_f32_16x16x32_bf16 v[48:51], a[52:55], v[36:39], v[48:51]\n\ts_waitcnt lgkmcnt(2)\n\ts_add_u32 m0, m0, 896\n\t" \
    "v_mfma_f32_16x16x32_bf16 v[48:51], a[56:59], v[40:43], v[48:51]\n\tds_read_b128 v[20:23], v97 offset:16384\n\tv_mfma_f32_16x16x32_bf16 v[52:55], a[120:123], v[40:43], v[52:55]\n\t" \
    "v_mfma_f32_16x16x32_bf16 v[56:59], a[184:187], v[40:43], v[56:59]\n\tbuffer_load_dwordx4 %[dv], %[ra], %[so] offen offset:256 lds\n\t" \
    "v_mfma_f32_16x16x32_bf16 v[60:63], a[248:251], v[40:43], v[60:63]\n\ts_waitcnt lgkmcnt(2)\n\ts_add_u32 m0, m0, 896\n\tv_mfma_f32_16x16x32_bf16 v[60:63], a[252:255], v[44:47], v[60:63]\n\t" \
    "ds_read_b128 v[24:27], v96 offset:17408\n\tv_mfma_f32_16x16x32_bf16 v[56:59], a[188:191], v[44:47], v[56:59]\n\tv_mfma_f32_16x16x32_bf16 v[52:55], a[124:127], v[44:47], v[52:55]\n\t" \
    "buffer_load_dwordx4 %[dv], %[ra], %[so] offen offset:384 lds\n\tv_mfma_f32_16x16x32_bf16 v[48:51], a[60:63], v[44:47], v[48:51]\n\ts_waitcnt lgkmcnt(2)\n\ts_add_u32 m0, m0, 896\n\t" \
    "v_mfma_f32_16x16x32_bf16 v[64:67], a[0:3], v[16:19], 0\n\tds_read_b128 v[28:31], v97 offset:17408\n\tv_mfma_f32_16x16x32_bf16 v[68:71], a[64:67], v[16:19], 0\n\t" \
    "v_mfma_f32_16x16x32_bf16 v[72:75], a[128:131], v[16:19], 0\n\tbuffer_load_dwordx4 %[dv], %[ra], %[so] offen offset:512 lds\n\tv_mul_f32 v48, %[sc], v48\n\tv_mul_f32 v49, %[sc], v49\n\t" \
    "v_mfma_f32_16x16x32_bf16 v[76:79], a[192:195], v[16:19], 0\n\ts_waitcnt lgkmcnt(2)\n\ts_add_u32 m0, m0, 896\n\tv_mul_f32 v50, %[sc], v50\n\tv_mul_f32 v51, %[sc], v51\n\t" \
    "v_mfma_f32_16x16x32_bf16 v[76:79], a[196:199], v[20:23], v[76:79]\n\tds_read_b128 v[32:35], v96 offset:18432\n\tv_cvt_pk_bf16_f32 v80, v48, v49\n\t" \
    "v_mfma_f32_16x16x32_bf16 v[72:75], a[132:135], v[20:23], v[72:75]\n\tv_cvt_pk_bf16_f32 v81, v50, v51\n\tv_mul_f32 v52, %[sc], v52\n\tv_mfma_f32_16x16x32_bf16 v[68:71], a[68:71], v[20:23], v[68:71]\n\t" \
    "buffer_load_dwordx4 %[dv], %[ra], %[so] offen offset:640 lds\n\tv_mul_f32 v53, %[sc], v53\n\tv_mul_f32 v54, %[sc], v54\n\tv_mfma_f32_16x16x32_bf16 v[64:67], a[4:7], v[20:23], v[64:67]\n\t" \
    "s_waitcnt lgkmcnt(2)\n\ts_add_u32 m0, m0, 896\n\tv_mul_f32 v55, %[sc], v55\n\tv_cvt_pk_bf16_f32 v82, v52, v53\n\tv_mfma_f32_16x16x32_bf16 v[64:67], a[8:11], v[24:27], v[64:67]\n\t" \
    "ds_read_b128 v[36:39], v97 offset:18432\n\tv_cvt_pk_bf16_f32 v83, v54, v55\n\tv_mfma_f32_16x16x32_bf16 v[68:71], a[72:75], v[24:27], v[68:71]\n\t" \
    "buffer_store_dwordx4 v[80:83], v98, %[rc], %[sp] offen sc1\n\tv_mul_f32 v56, %[sc], v56\n\tv_mfma_f32_16x16x32_bf16 v[72:75], a[136:139], v[24:27], v[72:75]\n\t" \
    "buffer_load_dwordx4 %[dv], %[ra], %[so] offen offset:768 lds\n\tv_mul_f32 v57, %[sc], v57\n\tv_mul_f32 v58, %[sc], v58\n\tv_mfma_f32_16x16x32_bf16 v[76:79], a[200:203], v[24:27], v[76:79]\n\t" \
    "s_waitcnt lgkmcnt(2)\n\ts_add_u32 m0, m0, 896\n\tv_mul_f32 v59, %[sc], v59\n\tv_cvt_pk_bf16_f32 v84, v56, v57\n\tv_mfma_f32_16x16x32_bf16 v[76:79], a[204:207], v[28:31], v[76:79]\n\t" \
    "ds_read_b128 v[40:43], v96 offset:19456\n\tv_cvt_pk_bf16_f32 v85, v58, v59\n\tv_mfma_f32_16x16x32_bf16 v[72:75], a[140:143], v[28:31], v[72:75]\n\tv_mul_f32 v60, %[sc], v60\n\t" \
    "v_mul_f32 v61, %[sc], v61\n\tv_mfma_f32_16x16x32_bf16 v[68:71], a[76:79], v[28:31], v[68:71]\n\tbuffer_load_dwordx4 %[dv], %[ra], %[so] offen offset:896 lds\n\tv_mul_f32 v62, %[sc], v62\n\t" \
    "v_mul_f32 v63, %[sc], v63\n\tv_mfma_f32_16x16x32_bf16 v[64:67], a[12:15], v[28:31], v[64:67]\n\ts_waitcnt lgkmcnt(2)\n\ts_add_u32 %[so], %[so], %[c15]\n\tv_cvt_pk_bf16_f32 v86, v60, v61\n\t" \
    "v_cvt_pk_bf16_f32 v87, v62, v63\n\tv_mfma_f32_16x16x32_bf16 v[64:67], a[16:19], v[32:35], v[64:67]\n\tds_read_b128 v[44:47], v97 offset:19456\n\t" \
    "buffer_store_dwordx4 v[84:87], v98, %[rc], %[sp] offen offset:2048 sc1\n\tv_mfma_f32_16x16x32_bf16 v[68:71], a[80:83], v[32:35], v[68:71]\n\t" \
    "v_mfma_f32_16x16x32_bf16 v[72:75], a[144:147], v[32:35], v[72:75]\n\ts_min_u32 %[so], %[so], 0x1f8000\n\tv_mfma_f32_16x16x32_bf16 v[76:79], a[208:211], v[32:35], v[76:79]\n\ts_waitcnt lgkmcnt(2)\n\t" \
    "v_mfma_f32_16x16x32_bf16 v[76:79], a[212:215], v[36:39], v[76:79]\n\tds_read_b128 v[16:19], v96 offset:20480\n\tv_mfma_f32_16x16x32_bf16 v[72:75], a[148:151], v[36:39], v[72:75]\n\t" \
    "v_mfma_f32_16x16x32_bf16 v[68:71], a[84:87], v[36:39], v[68:71]\n\tv_mfma_f32_16x16x32_bf16 v[64:67], a[20:23], v[36:39], v[64:67]\n\ts_waitcnt lgkmcnt(2)\n\t" \
    "v_mfma_f32_16x16x32_bf16 v[64:67], a[24:27], v[40:43], v[64:67]\n\tds_read_b128 v[20:23], v97 offset:20480\n\tv_mfma_f32_16x16x32_bf16 v[68:71], a[88:91], v[40:43], v[68:71]\n\t" \
    "v_mfma_f32_16x16x32_bf16 v[72:75], a[152:155], v[40:43], v[72:75]\n\tv_mfma_f32_16x16x32_bf16 v[76:79], a[216:219], v[40:43], v[76:79]\n\ts_waitcnt lgkmcnt(2)\n\t" \
    "v_mfma_f32_16x16x32_bf16 v[76:79], a[220:223], v[44:47], v[76:79]\n\tds_read_b128 v[24:27], v96 offset:21504\n\tv_mfma_f32_16x16x32_bf16 v[72:75], a[156:159], v[44:47], v[72:75]\n\t" \
    "v_mfma_f32_16x16x32_bf16 v[68:71], a[92:95], v[44:47], v[68:71]\n\tv_mfma_f32_16x16x32_bf16 v[64:67], a[28:31], v[44:47], v[64:67]\n\ts_waitcnt lgkmcnt(2)\n\t" \
    "v_mfma_f32_16x16x32_bf16 v[64:67], a[32:35], v[16:19], v[64:67]\n\tds_read_b128 v[28:31], v97 offset:21504\n\tv_mfma_f32_16x16x32_bf16 v[68:71], a[96:99], v[16:19], v[68:71]\n\t" \
    "v_mfma_f32_16x16x32_bf16 v[72:75], a[160:163], v[16:19], v[72:75]\n\tv_mfma_f32_16x16x32_bf16 v[76:79], a[224:227], v[16:19], v[76:79]\n\ts_waitcnt lgkmcnt(2)\n\t" \
    "v_mfma_f32_16x16x32_bf16 v[76:79], a[228:231], v[20:23], v[76:79]\n\tds_read_b128 v[32:35], v96 offset:22528\n\tv_mfma_f32_16x16x32_bf16 v[72:75], a[164:167], v[20:23], v[72:75]\n\t" \
    "v_mfma_f32_16x16x32_bf16 v[68:71], a[100:103], v[20:23], v[68:71]\n\tv_mfma_f32_16x16x32_bf16 v[64:67], a[36:39], v[20:23], v[64:67]\n\ts_waitcnt lgkmcnt(2)\n\t" \
    "v_mfma_f32_16x16x32_bf16 v[64:67], a[40:43], v[24:27], v[64:67]\n\tds_read_b128 v[36:39], v97 offset:22528\n\tv_mfma_f32_16x16x32_bf16 v[68:71], a[104:107], v[24:27], v[68:71]\n\t" \
    "v_mfma_f32_16x16x32_bf16 v[72:75], a[168:171], v[24:27], v[72:75]\n\tv_mfma_f32_16x16x32_bf16 v[76:79], a[232:235], v[24:27], v[76:79]\n\ts_waitcnt lgkmcnt(2)\n\t" \
    "v_mfma_f32_16x16x32_bf16 v[76:79], a[236:239], v[28:31], v[76:79]\n\tds_read_b128 v[40:43], v96 offset:23552\n\tv_mfma_f32_16x16x32_bf16 v[72:75], a[172:175], v[28:31], v[72:75]\n\t" \
    "v_add_u32 v96, s62, %[xb]\n\tv_mfma_f32_16x16x32_bf16 v[68:71], a[108:111], v[28:31], v[68:71]\n\tv_mfma_f32_16x16x32_bf16 v[64:67], a[44:47], v[28:31], v[64:67]\n\ts_waitcnt lgkmcnt(2)\n\t" \
    "v_mfma_f32_16x16x32_bf16 v[64:67], a[48:51], v[32:35], v[64:67]\n\tds_read_b128 v[44:47], v97 offset:23552\n\tv_mfma_f32_16x16x32_bf16 v[68:71], a[112:115], v[32:35], v[68:71]\n\t" \
    "v_xor_b32 v97, 64, v96\n\tv_mfma_f32_16x16x32_bf16 v[72:75], a[176:179], v[32:35], v[72:75]\n\tv_mfma_f32_16x16x32_bf16 v[76:79], a[240:243], v[32:35], v[76:79]\n\ts_waitcnt lgkmcnt(2)\n\t" \
    "v_mfma_f32_16x16x32_bf16 v[76:79], a[244:247], v[36:39], v[76:79]\n\tds_read_b128 v[16:19], v96\n\tv_mfma_f32_16x16x32_bf16 v[72:75], a[180:183], v[36:39], v[72:75]\n\t" \
    "v_mfma_f32_16x16x32_bf16 v[68:71], a[116:119], v[36:39], v[68:71]\n\tv_mfma_f32_16x16x32_bf16 v[64:67], a[52:55], v[36:39], v[64:67]\n\ts_waitcnt lgkmcnt(2)\n\t" \
    "v_mfma_f32_16x16x32_bf16 v[64:67], a[56:59], v[40:43], v[64:67]\n\tds_read_b128 v[20:23], v97\n\tv_mfma_f32_16x16x32_bf16 v[68:71], a[120:123], v[40:43], v[68:71]\n\t" \
    "v_mfma_f32_16x16x32_bf16 v[72:75], a[184:187], v[40:43], v[72:75]\n\tv_mfma_f32_16x16x32_bf16 v[76:79], a[248:251], v[40:43], v[76:79]\n\ts_waitcnt lgkmcnt(2)\n\t" \
    "v_mfma_f32_16x16x32_bf16 v[76:79], a[252:255], v[44:47], v[76:79]\n\tds_read_b128 v[24:27], v96 offset:1024\n\tv_mfma_f32_16x16x32_bf16 v[72:75], a[188:191], v[44:47], v[72:75]\n\t" \
    "s_add_u32 s61, s61, 1\n\tv_mfma_f32_16x16x32_bf16 v[68:71], a[124:127], v[44:47], v[68:71]\n\ts_cmp_lt_u32 s61, %[nn]\n\tv_mfma_f32_16x16x32_bf16 v[64:67], a[60:63], v[44:47], v[64:67]\n\t" \
    "s_waitcnt lgkmcnt(2)\n\ts_cbranch_scc1 1b\n\ts_mov_b32 m0, s60\n\ts_waitcnt lgkmcnt(0)\n\ts_nop 7\n\tv_mul_f32 v64, %[sc], v64\n\tv_mul_f32 v65, %[sc], v65\n\tv_mul_f32 v66, %[sc], v66\n\t" \
    "v_mul_f32 v67, %[sc], v67\n\tv_cvt_pk_bf16_f32 v88, v64, v65\n\tv_cvt_pk_bf16_f32 v89, v66, v67\n\tv_mul_f32 v68, %[sc], v68\n\tv_mul_f32 v69, %[sc], v69\n\tv_mul_f32 v70, %[sc], v70\n\t" \
    "v_mul_f32 v71, %[sc], v71\n\tv_cvt_pk_bf16_f32 v90, v68, v69\n\tv_cvt_pk_bf16_f32 v91, v70, v71\n\tbuffer_store_dwordx4 v[88:91], v98, %[rc], %[sp] offen offset:1024 sc1\n\t" \
    "v_mul_f32 v72, %[sc], v72\n\tv_mul_f32 v73, %[sc], v73\n\tv_mul_f32 v74, %[sc], v74\n\tv_mul_f32 v75, %[sc], v75\n\tv_cvt_pk_bf16_f32 v92, v72, v73\n\tv_cvt_pk_bf16_f32 v93, v74, v75\n\t" \
    "v_mul_f32 v76, %[sc], v76\n\tv_mul_f32 v77, %[sc], v77\n\tv_mul_f32 v78, %[sc], v78\n\tv_mul_f32 v79, %[sc], v79\n\tv_cvt_pk_bf16_f32 v94, v76, v77\n\tv_cvt_pk_bf16_f32 v95, v78, v79\n\t" \
    "buffer_store_dwordx4 v[92:95], v98, %[rc], %[sp] offen offset:3072 sc1\n\ts_nop 1\n\t"
#define ASM_PROJ_K \
    "s_mov_b32 s60, m0\n\ts_mov_b32 s61, 0\n\tv_add_u32 v96, 0x18000, %[xb]\n\tv_xor_b32 v97, 64, v96\n\tv_bfrev_b32 v98, 1\n\tds_read_b128 v[16:19], v96\n\tds_read_b128 v[20:23], v97\n\t" \
    "ds_read_b128 v[24:27], v96 offset:1024\n\ts_waitcnt lgkmcnt(2)\n\t1:\nv_mfma_f32_16x16x32_bf16 v[48:51], a[0:3], v[16:19], 0\n\tds_read_b128 v[28:31], v97 offset:1024\n\t" \
    "v_mfma_f32_16x16x32_bf16 v[52:55], a[64:67], v[16:19], 0\n\ts_lshl_b32 s62, s61, 15\n\tv_mfma_f32_16x16x32_bf16 v[56:59], a[128:131], v[16:19], 0\n\ts_and_b32 s62, s62, 0x18000\n\t" \
    "v_cvt_pk_bf16_f32 v88, v64, v65\n\tv_cvt_pk_bf16_f32 v89, v66, v67\n\tv_mfma_f32_16x16x32_bf16 v[60:63], a[192:195], v[16:19], 0\n\ts_waitcnt lgkmcnt(2)\n\tv_cvt_pk_bf16_f32 v90, v68, v69\n\t" \
    "v_cvt_pk_bf16_f32 v91, v70, v71\n\tv_mfma_f32_16x16x32_bf16 v[60:63], a[196:199], v[20:23], v[60:63]\n\tds_read_b128 v[32:35], v96 offset:2048\n\t" \
    "buffer_store_dwordx4 v[88:91], v98, %[rc], %[sp] offen offset:1024 sc1\n\tv_mfma_f32_16x16x32_bf16 v[56:59], a[132:135], v[20:23], v[56:59]\n\ts_xor_b32 s63, s62, 0x10000\n\t" \
    "v_cvt_pk_bf16_f32 v92, v72, v73\n\tv_cvt_pk_bf16_f32 v93, v74, v75\n\tv_mfma_f32_16x16x32_bf16 v[52:55], a[68:71], v[20:23], v[52:55]\n\ts_add_u32 s63, s63, %[lb]\n\t" \
    "v_cvt_pk_bf16_f32 v94, v76, v77\n\tv_cvt_pk_bf16_f32 v95, v78, v79\n\tv_mfma_f32_16x16x32_bf16 v[48:51], a[4:7], v[20:23], v[48:51]\n\ts_waitcnt lgkmcnt(2)\n\t" \
    "buffer_store_dwordx4 v[92:95], v98, %[rc], %[sp] offen offset:3072 sc1\n\tv_mov_b32 v98, %[co]\n\tv_mfma_f32_16x16x32_bf16 v[48:51], a[8:11], v[24:27], v[48:51]\n\t" \
    "ds_read_b128 v[36:39], v97 offset:2048\n\ts_add_u32 %[sp], %[sp], %[c18]\n\tv_mfma_f32_16x16x32_bf16 v[52:55], a[72:75], v[24:27], v[52:55]\n\t" \
    "v_mfma_f32_16x16x32_bf16 v[56:59], a[136:139], v[24:27], v[56:59]\n\tv_mfma_f32_16x16x32_bf16 v[60:63], a[200:203], v[24:27], v[60:63]\n\ts_waitcnt lgkmcnt(2)\n\t" \
    "v_mfma_f32_16x16x32_bf16 v[60:63], a[204:207], v[28:31], v[60:63]\n\tds_read_b128 v[40:43], v96 offset:3072\n\tv_mfma_f32_16x16x32_bf16 v[56:59], a[140:143], v[28:31], v[56:59]\n\t" \
    "v_mfma_f32_16x16x32_bf16 v[52:55], a[76:79], v[28:31], v[52:55]\n\tv_mfma_f32_16x16x32_bf16 v[48:51], a[12:15], v[28:31], v[48:51]\n\ts_waitcnt lgkmcnt(2)\n\t" \
    "v_mfma_f32_16x16x32_bf16 v[48:51], a[16:19], v[32:35], v[48:51]\n\tds_read_b128 v[44:47], v97 offset:3072\n\tv_mfma_f32_16x16x32_bf16 v[52:55], a[80:83], v[32:35], v[52:55]\n\t" \
    "v_mfma_f32_16x16x32_bf16 v[56:59], a[144:147], v[32:35], v[56:59]\n\tv_mfma_f32_16x16x32_bf16 v[60:63], a[208:211], v[32:35], v[60:63]\n\ts_waitcnt lgkmcnt(2)\n\t" \
    "v_mfma_f32_16x16x32_bf16 v[60:63], a[212:215], v[36:39], v[60:63]\n\tds_read_b128 v[16:19], v96 offset:4096\n\tv_mfma_f32_16x16x32_bf16 v[56:59], a[148:151], v[36:39], v[56:59]\n\t" \
    "v_mfma_f32_16x16x32_bf16 v[52:55], a[84:87], v[36:39], v[52:55]\n\tv_mfma_f32_16x16x32_bf16 v[48:51], a[20:23], v[36:39], v[48:51]\n\ts_waitcnt lgkmcnt(2)\n\t" \
    "v_mfma_f32_16x16x32_bf16 v[48:51], a[24:27], v[40:43], v[48:51]\n\tds_read_b128 v[20:23], v97 offset:4096\n\tv_mfma_f32_16x16x32_bf16 v[52:55], a[88:91], v[40:43], v[52:55]\n\t" \
    "v_mfma_f32_16x16x32_bf16 v[56:59], a[152:155], v[40:43], v[56:59]\n\tv_mfma_f32_16x16x32_bf16 v[60:63], a[216:219], v[40:43], v[60:63]\n\ts_waitcnt lgkmcnt(2)\n\t" \
    "v_mfma_f32_16x16x32_bf16 v[60:63], a[220:223], v[44:47], v[60:63]\n\tds_read_b128 v[24:27], v96 offset:5120\n\tv_mfma_f32_16x16x32_bf16 v[56:59], a[156:159], v[44:47], v[56:59]\n\t" \
    "v_mfma_f32_16x16x32_bf16 v[52:55], a[92:95], v[44:47], v[52:55]\n\tv_mfma_f32_16x16x32_bf16 v[48:51], a[28:31], v[44:47], v[48:51]\n\ts_waitcnt lgkmcnt(2)\n\t" \
    "v_mfma_f32_16x16x32_bf16 v[48:51], a[32:35], v[16:19], v[48:51]\n\tds_read_b128 v[28:31], v97 offset:5120\n\tv_mfma_f32_16x16x32_bf16 v[52:55], a[96:99], v[16:19], v[52:55]\n\t" \
    "v_mfma_f32_16x16x32_bf16 v[56:59], a[160:163], v[16:19], v[56:59]\n\tv_mfma_f32_16x16x32_bf16 v[60:63], a[224:227], v[16:19], v[60:63]\n\ts_waitcnt lgkmcnt(2)\n\t" \
    "v_mfma_f32_16x16x32_bf16 v[60:63], a[228:231], v[20:23], v[60:63]\n\tds_read_b128 v[32:35], v96 offset:6144\n\tv_mfma_f32_16x16x32_bf16 v[56:59], a[164:167], v[20:23], v[56:59]\n\t" \
    "v_mfma_f32_16x16x32_bf16 v[52:55], a[100:103], v[20:23], v[52:55]\n\tv_mfma_f32_16x16x32_bf16 v[48:51], a[36:39], v[20:23], v[48:51]\n\ts_waitcnt lgkmcnt(2)\n\t" \
    "v_mfma_f32_16x16x32_bf16 v[48:51], a[40:43], v[24:27], v[48:51]\n\tds_read_b128 v[36:39], v97 offset:6144\n\tv_mfma_f32_16x16x32_bf16 v[52:55], a[104:107], v[24:27], v[52:55]\n\t" \
    "v_mfma_f32_16x16x32_bf16 v[56:59], a[168:171], v[24:27], v[56:59]\n\tv_mfma_f32_16x16x32_bf16 v[60:63], a[232:235], v[24:27], v[60:63]\n\ts_waitcnt lgkmcnt(2)\n\t" \
    "v_mfma_f32_16x16x32_bf16 v[60:63], a[236:239], v[28:31], v[60:63]\n\tds_read_b128 v[40:43], v96 offset:7168\n\tv_mfma_f32_16x16x32_bf16 v[56:59], a[172:175], v[28:31], v[56:59]\n\t" \
    "v_mfma_f32_16x16x32_bf16 v[52:55], a[108:111], v[28:31], v[52:55]\n\tv_mfma_f32_16x16x32_bf16 v[48:51], a[44:47], v[28:31], v[48:51]\n\ts_waitcnt lgkmcnt(2)\n\t" \
    "v_mfma_f32_16x16x32_bf16 v[48:51], a[48:51], v[32:35], v[48:51]\n\ts_waitcnt vmcnt(10)\n\ts_barrier\n\ts_mov_b32 m0, s63\n\tds_read_b128 v[44:47], v97 offset:7168\n\t" \
    "v_mfma_f32_16x16x32_bf16 v[52:55], a[112:115], v[32:35], v[52:55]\n\tv_mfma_f32_16x16x32_bf16 v[56:59], a[176:179], v[32:35], v[56:59]\n\tbuffer_load_dwordx4 %[dv], %[ra], %[so] offen lds\n\t" \
    "v_mfma_f32_16x16x32_bf16 v[60:63], a[240:243], v[32:35], v[60:63]\n\ts_waitcnt lgkmcnt(2)\n\ts_add_u32 m0, m0, 896\n\tv_mfma_f32_16x16x32_bf16 v[60:63], a[244:247], v[36:39], v[60:63]\n\t" \
    "ds_read_b128 v[16:19], v96 offset:16384\n\tv_mfma_f32_16x16x32_bf16 v[56:59], a[180:183], v[36:39], v[56:59]\n\tv_mfma_f32_16x16x32_bf16 v[52:55], a[116:119], v[36:39], v[52:55]\n\t" \
    "buffer_load_dwordx4 %[dv], %[ra], %[so] offen offset:128 lds\n\tv_mfma_f32_16x16x32_bf16 v[48:51], a[52:55], v[36:39], v[48:51]\n\ts_waitcnt lgkmcnt(2)\n\ts_add_u32 m0, m0, 896\n\t" \
    "v_mfma_f32_16x16x32_bf16 v[48:51], a[56:59], v[40:43], v[48:51]\n\tds_read_b128 v[20:23], v97 offset:16384\n\tv_mfma_f32_16x16x32_bf16 v[52:55], a[120:123], v[40:43], v[52:55]\n\t" \
    "v_mfma_f32_16x16x32_bf16 v[56:59], a[184:187], v[40:43], v[56:59]\n\tbuffer_load_dwordx4 %[dv], %[ra], %[so] offen offset:256 lds\n\t" \
    "v_mfma_f32_16x16x32_bf16 v[60:63], a[248:251], v[40:43], v[60:63]\n\ts_waitcnt lgkmcnt(2)\n\ts_add_u32 m0, m0, 896\n\tv_mfma_f32_16x16x32_bf16 v[60:63], a[252:255], v[44:47], v[60:63]\n\t" \
    "ds_read_b128 v[24:27], v96 offset:17408\n\tv_mfma_f32_16x16x32_bf16 v[56:59], a[188:191], v[44:47], v[56:59]\n\tv_mfma_f32_16x16x32_bf16 v[52:55], a[124:127], v[44:47], v[52:55]\n\t" \
    "buffer_load_dwordx4 %[dv], %[ra], %[so] offen offset:384 lds\n\tv_mfma_f32_16x16x32_bf16 v[48:51], a[60:63], v[44:47], v[48:51]\n\ts_waitcnt lgkmcnt(2)\n\ts_add_u32 m0, m0, 896\n\t" \
    "v_mfma_f32_16x16x32_bf16 v[64:67], a[0:3], v[16:19], 0\n\tds_read_b128 v[28:31], v97 offset:17408\n\tv_mfma_f32_16x16x32_bf16 v[68:71], a[64:67], v[16:19], 0\n\t" \
    "v_mfma_f32_16x16x32_bf16 v[72:75], a[128:131], v[16:19], 0\n\tbuffer_load_dwordx4 %[dv], %[ra], %[so] offen offset:512 lds\n\tv_cvt_pk_bf16_f32 v80, v48, v49\n\tv_cvt_pk_bf16_f32 v81, v50, v51\n\t" \
    "v_mfma_f32_16x16x32_bf16 v[76:79], a[192:195], v[16:19], 0\n\ts_waitcnt lgkmcnt(2)\n\ts_add_u32 m0, m0, 896\n\tv_cvt_pk_bf16_f32 v82, v52, v53\n\tv_cvt_pk_bf16_f32 v83, v54, v55\n\t" \
    "v_mfma_f32_16x16x32_bf16 v[76:79], a[196:199], v[20:23], v[76:79]\n\tds_read_b128 v[32:35], v96 offset:18432\n\tbuffer_store_dwordx4 v[80:83], v98, %[rc], %[sp] offen sc1\n\t" \
    "v_mfma_f32_16x16x32_bf16 v[72:75], a[132:135], v[20:23], v[72:75]\n\tv_cvt_pk_bf16_f32 v84, v56, v57\n\tv_cvt_pk_bf16_f32 v85, v58, v59\n\t" \
    "v_mfma_f32_16x16x32_bf16 v[68:71], a[68:71], v[20:23], v[68:71]\n\tbuffer_load_dwordx4 %[dv], %[ra], %[so] offen offset:640 lds\n\tv_cvt_pk_bf16_f32 v86, v60, v61\n\t" \
    "v_cvt_pk_bf16_f32 v87, v62, v63\n\tv_mfma_f32_16x16x32_bf16 v[64:67], a[4:7], v[20:23], v[64:67]\n\ts_waitcnt lgkmcnt(2)\n\ts_add_u32 m0, m0, 896\n\t" \
    "buffer_store_dwordx4 v[84:87], v98, %[rc], %[sp] offen offset:2048 sc1\n\tv_mfma_f32_16x16x32_bf16 v[64:67], a[8:11], v[24:27], v[64:67]\n\tds_read_b128 v[36:39], v97 offset:18432\n\t" \
    "v_mfma_f32_16x16x32_bf16 v[68:71], a[72:75], v[24:27], v[68:71]\n\tv_mfma_f32_16x16x32_bf16 v[72:75], a[136:139], v[24:27], v[72:75]\n\tbuffer_load_dwordx4 %[dv], %[ra], %[so] offen offset:768 lds\n\t" \
    "v_mfma_f32_16x16x32_bf16 v[76:79], a[200:203], v[24:27], v[76:79]\n\ts_waitcnt lgkmcnt(2)\n\ts_add_u32 m0, m0, 896\n\tv_mfma_f32_16x16x32_bf16 v[76:79], a[204:207], v[28:31], v[76:79]\n\t" \
    "ds_read_b128 v[40:43], v96 offset:19456\n\tv_mfma_f32_16x16x32_bf16 v[72:75], a[140:143], v[28:31], v[72:75]\n\tv_mfma_f32_16x16x32_bf16 v[68:71], a[76:79], v[28:31], v[68:71]\n\t" \
    "buffer_load_dwordx4 %[dv], %[ra], %[so] offen offset:896 lds\n\tv_mfma_f32_16x16x32_bf16 v[64:67], a[12:15], v[28:31], v[64:67]\n\ts_waitcnt lgkmcnt(2)\n\ts_add_u32 %[so], %[so], %[c15]\n\t" \
    "v_mfma_f32_16x16x32_bf16 v[64:67], a[16:19], v[32:35], v[64:67]\n\tds_read_b128 v[44:47], v97 offset:19456\n\tv_mfma_f32_16x16x32_bf16 v[68:71], a[80:83], v[32:35], v[68:71]\n\t" \
    "v_mfma_f32_16x16x32_bf16 v[72:75], a[144:147], v[32:35], v[72:75]\n\ts_min_u32 %[so], %[so], 0x1f8000\n\tv_mfma_f32_16x16x32_bf16 v[76:79], a[208:211], v[32:35], v[76:79]\n\ts_waitcnt lgkmcnt(2)\n\t" \
    "v_mfma_f32_16x16x32_bf16 v[76:79], a[212:215], v[36:39], v[76:79]\n\tds_read_b128 v[16:19], v96 offset:20480\n\tv_mfma_f32_16x16x32_bf16 v[72:75], a[148:151], v[36:39], v[72:75]\n\t" \
    "v_mfma_f32_16x16x32_bf16 v[68:71], a[84:87], v[36:39], v[68:71]\n\tv_mfma_f32_16x16x32_bf16 v[64:67], a[20:23], v[36:39], v[64:67]\n\ts_waitcnt lgkmcnt(2)\n\t" \
    "v_mfma_f32_16x16x32_bf16 v[64:67], a[24:27], v[40:43], v[64:67]\n\tds_read_b128 v[20:23], v97 offset:20480\n\tv_mfma_f32_16x16x32_bf16 v[68:71], a[88:91], v[40:43], v[68:71]\n\t" \
    "v_mfma_f32_16x16x32_bf16 v[72:75], a[152:155], v[40:43], v[72:75]\n\tv_mfma_f32_16x16x32_bf16 v[76:79], a[216:219], v[40:43], v[76:79]\n\ts_waitcnt lgkmcnt(2)\n\t" \
    "v_mfma_f32_16x16x32_bf16 v[76:79], a[220:223], v[44:47], v[76:79]\n\tds_read_b128 v[24:27], v96 offset:21504\n\tv_mfma_f32_16x16x32_bf16 v[72:75], a[156:159], v[44:47], v[72:75]\n\t" \
    "v_mfma_f32_16x16x32_bf16 v[68:71], a[92:95], v[44:47], v[68:71]\n\tv_mfma_f32_16x16x32_bf16 v[64:67], a[28:31], v[44:47], v[64:67]\n\ts_waitcnt lgkmcnt(2)\n\t" \
    "v_mfma_f32_16x16x32_bf16 v[64:67], a[32:35], v[16:19], v[64:67]\n\tds_read_b128 v[28:31], v97 offset:21504\n\tv_mfma_f32_16x16x32_bf16 v[68:71], a[96:99], v[16:19], v[68:71]\n\t" \
    "v_mfma_f32_16x16x32_bf16 v[72:75], a[160:163], v[16:19], v[72:75]\n\tv_mfma_f32_16x16x32_bf16 v[76:79], a[224:227], v[16:19], v[76:79]\n\ts_waitcnt lgkmcnt(2)\n\t" \
    "v_mfma_f32_16x16x32_bf16 v[76:79], a[228:231], v[20:23], v[76:79]\n\tds_read_b128 v[32:35], v96 offset:22528\n\tv_mfma_f32_16x16x32_bf16 v[72:75], a[164:167], v[20:23], v[72:75]\n\t" \
    "v_mfma_f32_16x16x32_bf16 v[68:71], a[100:103], v[20:23], v[68:71]\n\tv_mfma_f32_16x16x32_bf16 v[64:67], a[36:39], v[20:23], v[64:67]\n\ts_waitcnt lgkmcnt(2)\n\t" \
    "v_mfma_f32_16x16x32_bf16 v[64:67], a[40:43], v[24:27], v[64:67]\n\tds_read_b128 v[36:39], v97 offset:22528\n\tv_mfma_f32_16x16x32_bf16 v[68:71], a[104:107], v[24:27], v[68:71]\n\t" \
    "v_mfma_f32_16x16x32_bf16 v[72:75], a[168:171], v[24:27], v[72:75]\n\tv_mfma_f32_16x16x32_bf16 v[76:79], a[232:235], v[24:27], v[76:79]\n\ts_waitcnt lgkmcnt(2)\n\t" \
    "v_mfma_f32_16x16x32_bf16 v[76:79], a[236:239], v[28:31], v[76:79]\n\tds_read_b128 v[40:43], v96 offset:23552\n\tv_mfma_f32_16x16x32_bf16 v[72:75], a[172:175], v[28:31], v[72:75]\n\t" \
    "v_add_u32 v96, s62, %[xb]\n\tv_mfma_f32_16x16x32_bf16 v[68:71], a[108:111], v[28:31], v[68:71]\n\tv_mfma_f32_16x16x32_bf16 v[64:67], a[44:47], v[28:31], v[64:67]\n\ts_waitcnt lgkmcnt(2)\n\t" \
    "v_mfma_f32_16x16x32_bf16 v[64:67], a[48:51], v[32:35], v[64:67]\n\tds_read_b128 v[44:47], v97 offset:23552\n\tv_mfma_f32_16x16x32_bf16 v[68:71], a[112:115], v[32:35], v[68:71]\n\t" \
    "v_xor_b32 v97, 64, v96\n\tv_mfma_f32_16x16x32_bf16 v[72:75], a[176:179], v[32:35], v[72:75]\n\tv_mfma_f32_16x16x32_bf16 v[76:79], a[240:243], v[32:35], v[76:79]\n\ts_waitcnt lgkmcnt(2)\n\t" \
    "v_mfma_f32_16x16x32_bf16 v[76:79], a[244:247], v[36:39], v[76:79]\n\tds_read_b128 v[16:19], v96\n\tv_mfma_f32_16x16x32_bf16 v[72:75], a[180:183], v[36:39], v[72:75]\n\t" \
    "v_mfma_f32_16x16x32_bf16 v[68:71], a[116:119], v[36:39], v[68:71]\n\tv_mfma_f32_16x16x32_bf16 v[64:67], a[52:55], v[36:39], v[64:67]\n\ts_waitcnt lgkmcnt(2)\n\t" \
    "v_mfma_f32_16x16x32_bf16 v[64:67], a[56:59], v[40:43], v[64:67]\n\tds_read_b128 v[20:23], v97\n\tv_mfma_f32_16x16x32_bf16 v[68:71], a[120:123], v[40:43], v[68:71]\n\t" \
    "v_mfma_f32_16x16x32_bf16 v[72:75], a[184:187], v[40:43], v[72:75]\n\tv_mfma_f32_16x16x32_bf16 v[76:79], a[248:251], v[40:43], v[76:79]\n\ts_waitcnt lgkmcnt(2)\n\t" \
    "v_mfma_f32_16x16x32_bf16 v[76:79], a[252:255], v[44:47], v[76:79]\n\tds_read_b128 v[24:27], v96 offset:1024\n\tv_mfma_f32_16x16x32_bf16 v[72:75], a[188:191], v[44:47], v[72:75]\n\t" \
    "s_add_u32 s61, s61, 1\n\tv_mfma_f32_16x16x32_bf16 v[68:71], a[124:127], v[44:47], v[68:71]\n\ts_cmp_lt_u32 s61, %[nn]\n\tv_mfma_f32_16x16x32_bf16 v[64:67], a[60:63], v[44:47], v[64:67]\n\t" \
    "s_waitcnt lgkmcnt(2)\n\ts_cbranch_scc1 1b\n\ts_mov_b32 m0, s60\n\ts_waitcnt lgkmcnt(0)\n\ts_nop 7\n\tv_cvt_pk_bf16_f32 v88, v64, v65\n\tv_cvt_pk_bf16_f32 v89, v66, v67\n\t" \
    "v_cvt_pk_bf16_f32 v90, v68, v69\n\tv_cvt_pk_bf16_f32 v91, v70, v71\n\tbuffer_store_dwordx4 v[88:91], v98, %[rc], %[sp] offen offset:1024 sc1\n\tv_cvt_pk_bf16_f32 v92, v72, v73\n\t" \
    "v_cvt_pk_bf16_f32 v93, v74, v75\n\tv_cvt_pk_bf16_f32 v94, v76, v77\n\tv_cvt_pk_bf16_f32 v95, v78, v79\n\tbuffer_store_dwordx4 v[92:95], v98, %[rc], %[sp] offen offset:3072 sc1\n\ts_nop 1\n\t"
#define ASM_PROJ_V \
    "s_mov_b32 s60, m0\n\ts_mov_b32 s61, 0\n\tv_add_u32 v96, 0x18000, %[xb]\n\tv_xor_b32 v97, 64, v96\n\tv_bfrev_b32 v98, 1\n\tds_read_b128 v[16:19], v96\n\tds_read_b128 v[20:23], v97\n\t" \
    "ds_read_b128 v[24:27], v96 offset:1024\n\ts_waitcnt lgkmcnt(2)\n\t1:\nv_mfma_f32_16x16x32_bf16 v[48:51], v[16:19], a[0:3], 0\n\tds_read_b128 v[28:31], v97 offset:1024\n\t" \
    "v_mfma_f32_16x16x32_bf16 v[52:55], v[16:19], a[64:67], 0\n\ts_lshl_b32 s62, s61, 15\n\tv_mfma_f32_16x16x32_bf16 v[56:59], v[16:19], a[128:131], 0\n\ts_and_b32 s62, s62, 0x18000\n\t" \
    "v_cvt_pk_bf16_f32 v82, v64, v65\n\tv_cvt_pk_bf16_f32 v83, v66, v67\n\tv_mfma_f32_16x16x32_bf16 v[60:63], v[16:19], a[192:195], 0\n\ts_waitcnt lgkmcnt(2)\n\t" \
    "buffer_store_dwordx4 v[80:83], v98, %[rc], %[sp] offen sc1\n\tv_cvt_pk_bf16_f32 v86, v68, v69\n\tv_mfma_f32_16x16x32_bf16 v[60:63], v[20:23], a[196:199], v[60:63]\n\t" \
    "ds_read_b128 v[32:35], v96 offset:2048\n\tv_cvt_pk_bf16_f32 v87, v70, v71\n\tv_mfma_f32_16x16x32_bf16 v[56:59], v[20:23], a[132:135], v[56:59]\n\ts_xor_b32 s63, s62, 0x10000\n\t" \
    "buffer_store_dwordx4 v[84:87], v98, %[rc], %[sp] offen offset:1024 sc1\n\tv_cvt_pk_bf16_f32 v90, v72, v73\n\tv_mfma_f32_16x16x32_bf16 v[52:55], v[20:23], a[68:71], v[52:55]\n\t" \
    "s_add_u32 s63, s63, %[lb]\n\tv_cvt_pk_bf16_f32 v91, v74, v75\n\tv_mfma_f32_16x16x32_bf16 v[48:51], v[20:23], a[4:7], v[48:51]\n\ts_waitcnt lgkmcnt(2)\n\t" \
    "buffer_store_dwordx4 v[88:91], v98, %[rc], %[sp] offen offset:2048 sc1\n\tv_cvt_pk_bf16_f32 v94, v76, v77\n\tv_mfma_f32_16x16x32_bf16 v[48:51], v[24:27], a[8:11], v[48:51]\n\t" \
    "ds_read_b128 v[36:39], v97 offset:2048\n\tv_cvt_pk_bf16_f32 v95, v78, v79\n\tv_mfma_f32_16x16x32_bf16 v[52:55], v[24:27], a[72:75], v[52:55]\n\t" \
    "buffer_store_dwordx4 v[92:95], v98, %[rc], %[sp] offen offset:3072 sc1\n\tv_mov_b32 v98, %[co]\n\tv_mfma_f32_16x16x32_bf16 v[56:59], v[24:27], a[136:139], v[56:59]\n\t" \
    "s_add_u32 %[sp], %[sp], %[c18]\n\tv_mfma_f32_16x16x32_bf16 v[60:63], v[24:27], a[200:203], v[60:63]\n\ts_waitcnt lgkmcnt(2)\n\tv_mfma_f32_16x16x32_bf16 v[60:63], v[28:31], a[204:207], v[60:63]\n\t" \
    "ds_read_b128 v[40:43], v96 offset:3072\n\tv_mfma_f32_16x16x32_bf16 v[56:59], v[28:31], a[140:143], v[56:59]\n\tv_mfma_f32_16x16x32_bf16 v[52:55], v[28:31], a[76:79], v[52:55]\n\t" \
    "v_mfma_f32_16x16x32_bf16 v[48:51], v[28:31], a[12:15], v[48:51]\n\ts_waitcnt lgkmcnt(2)\n\tv_mfma_f32_16x16x32_bf16 v[48:51], v[32:35], a[16:19], v[48:51]\n\tds_read_b128 v[44:47], v97 offset:3072\n\t" \
    "v_mfma_f32_16x16x32_bf16 v[52:55], v[32:35], a[80:83], v[52:55]\n\tv_mfma_f32_16x16x32_bf16 v[56:59], v[32:35], a[144:147], v[56:59]\n\t" \
    "v_mfma_f32_16x16x32_bf16 v[60:63], v[32:35], a[208:211], v[60:63]\n\ts_waitcnt lgkmcnt(2)\n\tv_mfma_f32_16x16x32_bf16 v[60:63], v[36:39], a[212:215], v[60:63]\n\t" \
    "ds_read_b128 v[16:19], v96 offset:4096\n\tv_mfma_f32_16x16x32_bf16 v[56:59], v[36:39], a[148:151], v[56:59]\n\tv_mfma_f32_16x16x32_bf16 v[52:55], v[36:39], a[84:87], v[52:55]\n\t" \
    "v_mfma_f32_16x16x32_bf16 v[48:51], v[36:39], a[20:23], v[48:51]\n\ts_waitcnt lgkmcnt(2)\n\tv_mfma_f32_16x16x32_bf16 v[48:51], v[40:43], a[24:27], v[48:51]\n\tds_read_b128 v[20:23], v97 offset:4096\n\t" \
    "v_mfma_f32_16x16x32_bf16 v[52:55], v[40:43], a[88:91], v[52:55]\n\tv_mfma_f32_16x16x32_bf16 v[56:59], v[40:43], a[152:155], v[56:59]\n\t" \
    "v_mfma_f32_16x16x32_bf16 v[60:63], v[40:43], a[216:219], v[60:63]\n\ts_waitcnt lgkmcnt(2)\n\tv_mfma_f32_16x16x32_bf16 v[60:63], v[44:47], a[220:223], v[60:63]\n\t" \
    "ds_read_b128 v[24:27], v96 offset:5120\n\tv_mfma_f32_16x16x32_bf16 v[56:59], v[44:47], a[156:159], v[56:59]\n\tv_mfma_f32_16x16x32_bf16 v[52:55], v[44:47], a[92:95], v[52:55]\n\t" \
    "v_mfma_f32_16x16x32_bf16 v[48:51], v[44:47], a[28:31], v[48:51]\n\ts_waitcnt lgkmcnt(2)\n\tv_mfma_f32_16x16x32_bf16 v[48:51], v[16:19], a[32:35], v[48:51]\n\tds_read_b128 v[28:31], v97 offset:5120\n\t" \
    "v_mfma_f32_16x16x32_bf16 v[52:55], v[16:19], a[96:99], v[52:55]\n\tv_mfma_f32_16x16x32_bf16 v[56:59], v[16:19], a[160:163], v[56:59]\n\t" \
    "v_mfma_f32_16x16x32_bf16 v[60:63], v[16:19], a[224:227], v[60:63]\n\ts_waitcnt lgkmcnt(2)\n\tv_mfma_f32_16x16x32_bf16 v[60:63], v[20:23], a[228:231], v[60:63]\n\t" \
    "ds_read_b128 v[32:35], v96 offset:6144\n\tv_mfma_f32_16x16x32_bf16 v[56:59], v[20:23], a[164:167], v[56:59]\n\tv_mfma_f32_16x16x32_bf16 v[52:55], v[20:23], a[100:103], v[52:55]\n\t" \
    "v_mfma_f32_16x16x32_bf16 v[48:51], v[20:23], a[36:39], v[48:51]\n\ts_waitcnt lgkmcnt(2)\n\tv_mfma_f32_16x16x32_bf16 v[48:51], v[24:27], a[40:43], v[48:51]\n\tds_read_b128 v[36:39], v97 offset:6144\n\t" \
    "v_mfma_f32_16x16x32_bf16 v[52:55], v[24:27], a[104:107], v[52:55]\n\tv_mfma_f32_16x16x32_bf16 v[56:59], v[24:27], a[168:171], v[56:59]\n\t" \
    "v_mfma_f32_16x16x32_bf16 v[60:63], v[24:27], a[232:235], v[60:63]\n\ts_waitcnt lgkmcnt(2)\n\tv_mfma_f32_16x16x32_bf16 v[60:63], v[28:31], a[236:239], v[60:63]\n\t" \
    "ds_read_b128 v[40:43], v96 offset:7168\n\tv_mfma_f32_16x16x32_bf16 v[56:59], v[28:31], a[172:175], v[56:59]\n\tv_mfma_f32_16x16x32_bf16 v[52:55], v[28:31], a[108:111], v[52:55]\n\t" \
    "v_mfma_f32_16x16x32_bf16 v[48:51], v[28:31], a[44:47], v[48:51]\n\ts_waitcnt lgkmcnt(2)\n\tv_mfma_f32_16x16x32_bf16 v[48:51], v[32:35], a[48:51], v[48:51]\n\ts_waitcnt vmcnt(12)\n\ts_barrier\n\t" \
    "s_mov_b32 m0, s63\n\tds_read_b128 v[44:47], v97 offset:7168\n\tv_mfma_f32_16x16x32_bf16 v[52:55], v[32:35], a[112:115], v[52:55]\n\t" \
    "v_mfma_f32_16x16x32_bf16 v[56:59], v[32:35], a[176:179], v[56:59]\n\tbuffer_load_dwordx4 %[dv], %[ra], %[so] offen lds\n\tv_mfma_f32_16x16x32_bf16 v[60:63], v[32:35], a[240:243], v[60:63]\n\t" \
    "s_waitcnt lgkmcnt(2)\n\ts_add_u32 m0, m0, 896\n\tv_mfma_f32_16x16x32_bf16 v[60:63], v[36:39], a[244:247], v[60:63]\n\tds_read_b128 v[16:19], v96 offset:16384\n\t" \
    "v_mfma_f32_16x16x32_bf16 v[56:59], v[36:39], a[180:183], v[56:59]\n\tv_mfma_f32_16x16x32_bf16 v[52:55], v[36:39], a[116:119], v[52:55]\n\t" \
    "buffer_load_dwordx4 %[dv], %[ra], %[so] offen offset:128 lds\n\tv_mfma_f32_16x16x32_bf16 v[48:51], v[36:39], a[52:55], v[48:51]\n\ts_waitcnt lgkmcnt(2)\n\ts_add_u32 m0, m0, 896\n\t" \
    "v_mfma_f32_16x16x32_bf16 v[48:51], v[40:43], a[56:59], v[48:51]\n\tds_read_b128 v[20:23], v97 offset:16384\n\tv_mfma_f32_16x16x32_bf16 v[52:55], v[40:43], a[120:123], v[52:55]\n\t" \
    "v_mfma_f32_16x16x32_bf16 v[56:59], v[40:43], a[184:187], v[56:59]\n\tbuffer_load_dwordx4 %[dv], %[ra], %[so] offen offset:256 lds\n\t" \
    "v_mfma_f32_16x16x32_bf16 v[60:63], v[40:43], a[248:251], v[60:63]\n\ts_waitcnt lgkmcnt(2)\n\ts_add_u32 m0, m0, 896\n\tv_mfma_f32_16x16x32_bf16 v[60:63], v[44:47], a[252:255], v[60:63]\n\t" \
    "ds_read_b128 v[24:27], v96 offset:17408\n\tv_mfma_f32_16x16x32_bf16 v[56:59], v[44:47], a[188:191], v[56:59]\n\tv_mfma_f32_16x16x32_bf16 v[52:55], v[44:47], a[124:127], v[52:55]\n\t" \
    "buffer_load_dwordx4 %[dv], %[ra], %[so] offen offset:384 lds\n\tv_mfma_f32_16x16x32_bf16 v[48:51], v[44:47], a[60:63], v[48:51]\n\ts_waitcnt lgkmcnt(2)\n\ts_add_u32 m0, m0, 896\n\t" \
    "v_mfma_f32_16x16x32_bf16 v[64:67], v[16:19], a[0:3], 0\n\tds_read_b128 v[28:31], v97 offset:17408\n\tv_mfma_f32_16x16x32_bf16 v[68:71], v[16:19], a[64:67], 0\n\t" \
    "v_mfma_f32_16x16x32_bf16 v[72:75], v[16:19], a[128:131], 0\n\tbuffer_load_dwordx4 %[dv], %[ra], %[so] offen offset:512 lds\n\tv_cvt_pk_bf16_f32 v80, v48, v49\n\tv_cvt_pk_bf16_f32 v81, v50, v51\n\t" \
    "v_mfma_f32_16x16x32_bf16 v[76:79], v[16:19], a[192:195], 0\n\ts_waitcnt lgkmcnt(2)\n\ts_add_u32 m0, m0, 896\n\tv_cvt_pk_bf16_f32 v84, v52, v53\n\tv_cvt_pk_bf16_f32 v85, v54, v55\n\t" \
    "v_mfma_f32_16x16x32_bf16 v[76:79], v[20:23], a[196:199], v[76:79]\n\tds_read_b128 v[32:35], v96 offset:18432\n\tv_cvt_pk_bf16_f32 v88, v56, v57\n\t" \
    "v_mfma_f32_16x16x32_bf16 v[72:75], v[20:23], a[132:135], v[72:75]\n\tv_cvt_pk_bf16_f32 v89, v58, v59\n\tv_cvt_pk_bf16_f32 v92, v60, v61\n\t" \
    "v_mfma_f32_16x16x32_bf16 v[68:71], v[20:23], a[68:71], v[68:71]\n\tbuffer_load_dwordx4 %[dv], %[ra], %[so] offen offset:640 lds\n\tv_cvt_pk_bf16_f32 v93, v62, v63\n\t" \
    "v_mfma_f32_16x16x32_bf16 v[64:67], v[20:23], a[4:7], v[64:67]\n\ts_waitcnt lgkmcnt(2)\n\ts_add_u32 m0, m0, 896\n\tv_mfma_f32_16x16x32_bf16 v[64:67], v[24:27], a[8:11], v[64:67]\n\t" \
    "ds_read_b128 v[36:39], v97 offset:18432\n\tv_mfma_f32_16x16x32_bf16 v[68:71], v[24:27], a[72:75], v[68:71]\n\tv_mfma_f32_16x16x32_bf16 v[72:75], v[24:27], a[136:139], v[72:75]\n\t" \
    "buffer_load_dwordx4 %[dv], %[ra], %[so] offen offset:768 lds\n\tv_mfma_f32_16x16x32_bf16 v[76:79], v[24:27], a[200:203], v[76:79]\n\ts_waitcnt lgkmcnt(2)\n\ts_add_u32 m0, m0, 896\n\t" \
    "v_mfma_f32_16x16x32_bf16 v[76:79], v[28:31], a[204:207], v[76:79]\n\tds_read_b128 v[40:43], v96 offset:19456\n\tv_mfma_f32_16x16x32_bf16 v[72:75], v[28:31], a[140:143], v[72:75]\n\t" \
    "v_mfma_f32_16x16x32_bf16 v[68:71], v[28:31], a[76:79], v[68:71]\n\tbuffer_load_dwordx4 %[dv], %[ra], %[so] offen offset:896 lds\n\tv_mfma_f32_16x16x32_bf16 v[64:67], v[28:31], a[12:15], v[64:67]\n\t" \
    "s_waitcnt lgkmcnt(2)\n\ts_add_u32 %[so], %[so], %[c15]\n\tv_mfma_f32_16x16x32_bf16 v[64:67], v[32:35], a[16:19], v[64:67]\n\tds_read_b128 v[44:47], v97 offset:19456\n\t" \
    "v_mfma_f32_16x16x32_bf16 v[68:71], v[32:35], a[80:83], v[68:71]\n\tv_mfma_f32_16x16x32_bf16 v[72:75], v[32:35], a[144:147], v[72:75]\n\ts_min_u32 %[so], %[so], 0x1f8000\n\t" \
    "v_mfma_f32_16x16x32_bf16 v[76:79], v[32:35], a[208:211], v[76:79]\n\ts_waitcnt lgkmcnt(2)\n\tv_mfma_f32_16x16x32_bf16 v[76:79], v[36:39], a[212:215], v[76:79]\n\t" \
    "ds_read_b128 v[16:19], v96 offset:20480\n\tv_mfma_f32_16x16x32_bf16 v[72:75], v[36:39], a[148:151], v[72:75]\n\tv_mfma_f32_16x16x32_bf16 v[68:71], v[36:39], a[84:87], v[68:71]\n\t" \
    "v_mfma_f32_16x16x32_bf16 v[64:67], v[36:39], a[20:23], v[64:67]\n\ts_waitcnt lgkmcnt(2)\n\tv_mfma_f32_16x16x32_bf16 v[64:67], v[40:43], a[24:27], v[64:67]\n\t" \
    "ds_read_b128 v[20:23], v97 offset:20480\n\tv_mfma_f32_16x16x32_bf16 v[68:71], v[40:43], a[88:91], v[68:71]\n\tv_mfma_f32_16x16x32_bf16 v[72:75], v[40:43], a[152:155], v[72:75]\n\t" \
    "v_mfma_f32_16x16x32_bf16 v[76:79], v[40:43], a[216:219], v[76:79]\n\ts_waitcnt lgkmcnt(2)\n\tv_mfma_f32_16x16x32_bf16 v[76:79], v[44:47], a[220:223], v[76:79]\n\t" \
    "ds_read_b128 v[24:27], v96 offset:21504\n\tv_mfma_f32_16x16x32_bf16 v[72:75], v[44:47], a[156:159], v[72:75]\n\tv_mfma_f32_16x16x32_bf16 v[68:71], v[44:47], a[92:95], v[68:71]\n\t" \
    "v_mfma_f32_16x16x32_bf16 v[64:67], v[44:47], a[28:31], v[64:67]\n\ts_waitcnt lgkmcnt(2)\n\tv_mfma_f32_16x16x32_bf16 v[64:67], v[16:19], a[32:35], v[64:67]\n\t" \
    "ds_read_b128 v[28:31], v97 offset:21504\n\tv_mfma_f32_16x16x32_bf16 v[68:71], v[16:19], a[96:99], v[68:71]\n\tv_mfma_f32_16x16x32_bf16 v[72:75], v[16:19], a[160:163], v[72:75]\n\t" \
    "v_mfma_f32_16x16x32_bf16 v[76:79], v[16:19], a[224:227], v[76:79]\n\ts_waitcnt lgkmcnt(2)\n\tv_mfma_f32_16x16x32_bf16 v[76:79], v[20:23], a[228:231], v[76:79]\n\t" \
    "ds_read_b128 v[32:35], v96 offset:22528\n\tv_mfma_f32_16x16x32_bf16 v[72:75], v[20:23], a[164:167], v[72:75]\n\tv_mfma_f32_16x16x32_bf16 v[68:71], v[20:23], a[100:103], v[68:71]\n\t" \
    "v_mfma_f32_16x16x32_bf16 v[64:67], v[20:23], a[36:39], v[64:67]\n\ts_waitcnt lgkmcnt(2)\n\tv_mfma_f32_16x16x32_bf16 v[64:67], v[24:27], a[40:43], v[64:67]\n\t" \
    "ds_read_b128 v[36:39], v97 offset:22528\n\tv_mfma_f32_16x16x32_bf16 v[68:71], v[24:27], a[104:107], v[68:71]\n\tv_mfma_f32_16x16x32_bf16 v[72:75], v[24:27], a[168:171], v[72:75]\n\t" \
    "v_mfma_f32_16x16x32_bf16 v[76:79], v[24:27], a[232:235], v[76:79]\n\ts_waitcnt lgkmcnt(2)\n\tv_mfma_f32_16x16x32_bf16 v[76:79], v[28:31], a[236:239], v[76:79]\n\t" \
    "ds_read_b128 v[40:43], v96 offset:23552\n\tv_mfma_f32_16x16x32_bf16 v[72:75], v[28:31], a[172:175], v[72:75]\n\tv_add_u32 v96, s62, %[xb]\n\t" \
    "v_mfma_f32_16x16x32_bf16 v[68:71], v[28:31], a[108:111], v[68:71]\n\tv_mfma_f32_16x16x32_bf16 v[64:67], v[28:31], a[44:47], v[64:67]\n\ts_waitcnt lgkmcnt(2)\n\t" \
    "v_mfma_f32_16x16x32_bf16 v[64:67], v[32:35], a[48:51], v[64:67]\n\tds_read_b128 v[44:47], v97 offset:23552\n\tv_mfma_f32_16x16x32_bf16 v[68:71], v[32:35], a[112:115], v[68:71]\n\t" \
    "v_xor_b32 v97, 64, v96\n\tv_mfma_f32_16x16x32_bf16 v[72:75], v[32:35], a[176:179], v[72:75]\n\tv_mfma_f32_16x16x32_bf16 v[76:79], v[32:35], a[240:243], v[76:79]\n\ts_waitcnt lgkmcnt(2)\n\t" \
    "v_mfma_f32_16x16x32_bf16 v[76:79], v[36:39], a[244:247], v[76:79]\n\tds_read_b128 v[16:19], v96\n\tv_mfma_f32_16x16x32_bf16 v[72:75], v[36:39], a[180:183], v[72:75]\n\t" \
    "v_mfma_f32_16x16x32_bf16 v[68:71], v[36:39], a[116:119], v[68:71]\n\tv_mfma_f32_16x16x32_bf16 v[64:67], v[36:39], a[52:55], v[64:67]\n\ts_waitcnt lgkmcnt(2)\n\t" \
    "v_mfma_f32_16x16x32_bf16 v[64:67], v[40:43], a[56:59], v[64:67]\n\tds_read_b128 v[20:23], v97\n\tv_mfma_f32_16x16x32_bf16 v[68:71], v[40:43], a[120:123], v[68:71]\n\t" \
    "v_mfma_f32_16x16x32_bf16 v[72:75], v[40:43], a[184:187], v[72:75]\n\tv_mfma_f32_16x16x32_bf16 v[76:79], v[40:43], a[248:251], v[76:79]\n\ts_waitcnt lgkmcnt(2)\n\t" \
    "v_mfma_f32_16x16x32_bf16 v[76:79], v[44:47], a[252:255], v[76:79]\n\tds_read_b128 v[24:27], v96 offset:1024\n\tv_mfma_f32_16x16x32_bf16 v[72:75], v[44:47], a[188:191], v[72:75]\n\t" \
    "s_add_u32 s61, s61, 1\n\tv_mfma_f32_16x16x32_bf16 v[68:71], v[44:47], a[124:127], v[68:71]\n\ts_cmp_lt_u32 s61, %[nn]\n\tv_mfma_f32_16x16x32_bf16 v[64:67], v[44:47], a[60:63], v[64:67]\n\t" \
    "s_waitcnt lgkmcnt(2)\n\ts_cbranch_scc1 1b\n\ts_mov_b32 m0, s60\n\ts_waitcnt lgkmcnt(0)\n\ts_nop 7\n\tv_cvt_pk_bf16_f32 v82, v64, v65\n\tv_cvt_pk_bf16_f32 v83, v66, v67\n\t" \
    "buffer_store_dwordx4 v[80:83], v98, %[rc], %[sp] offen sc1\n\tv_cvt_pk_bf16_f32 v86, v68, v69\n\tv_cvt_pk_bf16_f32 v87, v70, v71\n\t" \
    "buffer_store_dwordx4 v[84:87], v98, %[rc], %[sp] offen offset:1024 sc1\n\tv_cvt_pk_bf16_f32 v90, v72, v73\n\tv_cvt_pk_bf16_f32 v91, v74, v75\n\t" \
    "buffer_store_dwordx4 v[88:91], v98, %[rc], %[sp] offen offset:2048 sc1\n\tv_cvt_pk_bf16_f32 v94, v76, v77\n\tv_cvt_pk_bf16_f32 v95, v78, v79\n\t" \
    "buffer_store_dwordx4 v[92:95], v98, %[rc], %[sp] offen offset:3072 sc1\n\ts_nop 1\n\t"
#define ASM_PROJP_Q \
    "s_mov_b32 s60, m0\n\ts_mov_b32 s61, 0\n\tv_add_u32 v96, 0x18000, %[xb]\n\tv_xor_b32 v97, 64, v96\n\tv_bfrev_b32 v98, 1\n\tds_read_b128 v[16:19], v96\n\tds_read_b128 v[20:23], v97\n\t" \
    "ds_read_b128 v[24:27], v96 offset:1024\n\ts_waitcnt lgkmcnt(2)\n\t1:\nv_mfma_f32_16x16x32_bf16 v[48:51], a[0:3], v[16:19], 0\n\tds_read_b128 v[28:31], v97 offset:1024\n\t" \
    "v_mfma_f32_16x16x32_bf16 v[52:55], a[64:67], v[16:19], 0\n\ts_lshl_b32 s62, s61, 15\n\tv_mfma_f32_16x16x32_bf16 v[56:59], a[128:131], v[16:19], 0\n\ts_and_b32 s62, s62, 0x18000\n\t" \
    "v_mul_f32 v64, %[sc], v64\n\tv_mul_f32 v65, %[sc], v65\n\tv_mfma_f32_16x16x32_bf16 v[60:63], a[192:195], v[16:19], 0\n\ts_waitcnt lgkmcnt(2)\n\tv_mul_f32 v66, %[sc], v66\n\t" \
    "v_mul_f32 v67, %[sc], v67\n\tv_mfma_f32_16x16x32_bf16 v[60:63], a[196:199], v[20:23], v[60:63]\n\tds_read_b128 v[32:35], v96 offset:2048\n\tv_cvt_pk_bf16_f32 v88, v64, v65\n\t" \
    "v_mfma_f32_16x16x32_bf16 v[56:59], a[132:135], v[20:23], v[56:59]\n\ts_xor_b32 s63, s62, 0x10000\n\tv_cvt_pk_bf16_f32 v89, v66, v67\n\tv_mul_f32 v68, %[sc], v68\n\t" \
    "v_mfma_f32_16x16x32_bf16 v[52:55], a[68:71], v[20:23], v[52:55]\n\ts_add_u32 s63, s63, %[lb]\n\tv_mul_f32 v69, %[sc], v69\n\tv_mul_f32 v70, %[sc], v70\n\t" \
    "v_mfma_f32_16x16x32_bf16 v[48:51], a[4:7], v[20:23], v[48:51]\n\ts_waitcnt lgkmcnt(2)\n\tv_mul_f32 v71, %[sc], v71\n\tv_cvt_pk_bf16_f32 v90, v68, v69\n\t" \
    "v_mfma_f32_16x16x32_bf16 v[48:51], a[8:11], v[24:27], v[48:51]\n\tds_read_b128 v[36:39], v97 offset:2048\n\tv_cvt_pk_bf16_f32 v91, v70, v71\n\t" \
    "v_mfma_f32_16x16x32_bf16 v[52:55], a[72:75], v[24:27], v[52:55]\n\tbuffer_store_dwordx4 v[88:91], v98, %[rc], %[sp] offen offset:1024 sc1\n\tv_mul_f32 v72, %[sc], v72\n\t" \
    "v_mfma_f32_16x16x32_bf16 v[56:59], a[136:139], v[24:27], v[56:59]\n\tv_mul_f32 v73, %[sc], v73\n\tv_mul_f32 v74, %[sc], v74\n\tv_mfma_f32_16x16x32_bf16 v[60:63], a[200:203], v[24:27], v[60:63]\n\t" \
    "s_waitcnt lgkmcnt(2)\n\tv_mul_f32 v75, %[sc], v75\n\tv_cvt_pk_bf16_f32 v92, v72, v73\n\tv_mfma_f32_16x16x32_bf16 v[60:63], a[204:207], v[28:31], v[60:63]\n\tds_read_b128 v[40:43], v96 offset:3072\n\t" \
    "v_cvt_pk_bf16_f32 v93, v74, v75\n\tv_mfma_f32_16x16x32_bf16 v[56:59], a[140:143], v[28:31], v[56:59]\n\tv_mul_f32 v76, %[sc], v76\n\tv_mul_f32 v77, %[sc], v77\n\t" \
    "v_mfma_f32_16x16x32_bf16 v[52:55], a[76:79], v[28:31], v[52:55]\n\tv_mul_f32 v78, %[sc], v78\n\tv_mul_f32 v79, %[sc], v79\n\tv_mfma_f32_16x16x32_bf16 v[48:51], a[12:15], v[28:31], v[48:51]\n\t" \
    "s_waitcnt lgkmcnt(2)\n\tv_cvt_pk_bf16_f32 v94, v76, v77\n\tv_cvt_pk_bf16_f32 v95, v78, v79\n\tv_mfma_f32_16x16x32_bf16 v[48:51], a[16:19], v[32:35], v[48:51]\n\t" \
    "ds_read_b128 v[44:47], v97 offset:3072\n\tbuffer_store_dwordx4 v[92:95], v98, %[rc], %[sp] offen offset:3072 sc1\n\tv_mfma_f32_16x16x32_bf16 v[52:55], a[80:83], v[32:35], v[52:55]\n\t" \
    "v_mov_b32 v98, %[co]\n\ts_add_u32 %[sp], %[sp], %[c18]\n\tv_mfma_f32_16x16x32_bf16 v[56:59], a[144:147], v[32:35], v[56:59]\n\tv_mov_b32 v99, s61\n\t" \
    "v_mfma_f32_16x16x32_bf16 v[60:63], a[208:211], v[32:35], v[60:63]\n\ts_waitcnt lgkmcnt(2)\n\tv_mfma_f32_16x16x32_bf16 v[60:63], a[212:215], v[36:39], v[60:63]\n\t" \
    "ds_read_b128 v[16:19], v96 offset:4096\n\tv_mfma_f32_16x16x32_bf16 v[56:59], a[148:151], v[36:39], v[56:59]\n\tv_mfma_f32_16x16x32_bf16 v[52:55], a[84:87], v[36:39], v[52:55]\n\t" \
    "v_mfma_f32_16x16x32_bf16 v[48:51], a[20:23], v[36:39], v[48:51]\n\ts_waitcnt lgkmcnt(2)\n\tv_mfma_f32_16x16x32_bf16 v[48:51], a[24:27], v[40:43], v[48:51]\n\tds_read_b128 v[20:23], v97 offset:4096\n\t" \
    "v_mfma_f32_16x16x32_bf16 v[52:55], a[88:91], v[40:43], v[52:55]\n\tv_mfma_f32_16x16x32_bf16 v[56:59], a[152:155], v[40:43], v[56:59]\n\t" \
    "v_mfma_f32_16x16x32_bf16 v[60:63], a[216:219], v[40:43], v[60:63]\n\ts_waitcnt lgkmcnt(2)\n\tv_mfma_f32_16x16x32_bf16 v[60:63], a[220:223], v[44:47], v[60:63]\n\t" \
    "ds_read_b128 v[24:27], v96 offset:5120\n\tv_mfma_f32_16x16x32_bf16 v[56:59], a[156:159], v[44:47], v[56:59]\n\tv_mfma_f32_16x16x32_bf16 v[52:55], a[92:95], v[44:47], v[52:55]\n\t" \
    "v_mfma_f32_16x16x32_bf16 v[48:51], a[28:31], v[44:47], v[48:51]\n\ts_waitcnt lgkmcnt(2)\n\tv_mfma_f32_16x16x32_bf16 v[48:51], a[32:35], v[16:19], v[48:51]\n\tds_read_b128 v[28:31], v97 offset:5120\n\t" \
    "v_mfma_f32_16x16x32_bf16 v[52:55], a[96:99], v[16:19], v[52:55]\n\tv_mfma_f32_16x16x32_bf16 v[56:59], a[160:163], v[16:19], v[56:59]\n\t" \
    "v_mfma_f32_16x16x32_bf16 v[60:63], a[224:227], v[16:19], v[60:63]\n\ts_waitcnt lgkmcnt(2)\n\tv_mfma_f32_16x16x32_bf16 v[60:63], a[228:231], v[20:23], v[60:63]\n\t" \
    "ds_read_b128 v[32:35], v96 offset:6144\n\tv_mfma_f32_16x16x32_bf16 v[56:59], a[164:167], v[20:23], v[56:59]\n\tv_mfma_f32_16x16x32_bf16 v[52:55], a[100:103], v[20:23], v[52:55]\n\t" \
    "v_mfma_f32_16x16x32_bf16 v[48:51], a[36:39], v[20:23], v[48:51]\n\ts_waitcnt lgkmcnt(2)\n\tv_mfma_f32_16x16x32_bf16 v[48:51], a[40:43], v[24:27], v[48:51]\n\tds_read_b128 v[36:39], v97 offset:6144\n\t" \
    "v_mfma_f32_16x16x32_bf16 v[52:55], a[104:107], v[24:27], v[52:55]\n\tv_mfma_f32_16x16x32_bf16 v[56:59], a[168:171], v[24:27], v[56:59]\n\t" \
    "v_mfma_f32_16x16x32_bf16 v[60:63], a[232:235], v[24:27], v[60:63]\n\ts_waitcnt lgkmcnt(2)\n\tv_mfma_f32_16x16x32_bf16 v[60:63], a[236:239], v[28:31], v[60:63]\n\t" \
    "ds_read_b128 v[40:43], v96 offset:7168\n\tv_mfma_f32_16x16x32_bf16 v[56:59], a[172:175], v[28:31], v[56:59]\n\tv_mfma_f32_16x16x32_bf16 v[52:55], a[108:111], v[28:31], v[52:55]\n\t" \
    "v_mfma_f32_16x16x32_bf16 v[48:51], a[44:47], v[28:31], v[48:51]\n\ts_waitcnt lgkmcnt(2)\n\tv_mfma_f32_16x16x32_bf16 v[48:51], a[48:51], v[32:35], v[48:51]\n\tds_read_b128 v[44:47], v97 offset:7168\n\t" \
    "v_mfma_f32_16x16x32_bf16 v[52:55], a[112:115], v[32:35], v[52:55]\n\tv_mfma_f32_16x16x32_bf16 v[56:59], a[176:179], v[32:35], v[56:59]\n\t" \
    "v_mfma_f32_16x16x32_bf16 v[60:63], a[240:243], v[32:35], v[60:63]\n\ts_waitcnt lgkmcnt(2)\n\tv_mfma_f32_16x16x32_bf16 v[60:63], a[244:247], v[36:39], v[60:63]\n\t" \
    "ds_read_b128 v[16:19], v96 offset:16384\n\tv_mfma_f32_16x16x32_bf16 v[56:59], a[180:183], v[36:39], v[56:59]\n\tv_mfma_f32_16x16x32_bf16 v[52:55], a[116:119], v[36:39], v[52:55]\n\t" \
    "v_mfma_f32_16x16x32_bf16 v[48:51], a[52:55], v[36:39], v[48:51]\n\ts_waitcnt lgkmcnt(2)\n\tv_mfma_f32_16x16x32_bf16 v[48:51], a[56:59], v[40:43], v[48:51]\n\ts_waitcnt vmcnt(0)\n\ts_barrier\n\t" \
    "s_mov_b32 m0, s63\n\tds_read_b128 v[20:23], v97 offset:16384\n\tv_mfma_f32_16x16x32_bf16 v[52:55], a[120:123], v[40:43], v[52:55]\n\ts_mov_b64 exec, %[em]\n\t" \
    "global_store_dword %[co], v99, %[pg] sc1\n\ts_mov_b64 exec, -1\n\tv_mfma_f32_16x16x32_bf16 v[56:59], a[184:187], v[40:43], v[56:59]\n\tbuffer_load_dwordx4 %[dv], %[ra], %[so] offen lds\n\t" \
    "v_mfma_f32_16x16x32_bf16 v[60:63], a[248:251], v[40:43], v[60:63]\n\ts_waitcnt lgkmcnt(2)\n\ts_add_u32 m0, m0, 896\n\tv_mfma_f32_16x16x32_bf16 v[60:63], a[252:255], v[44:47], v[60:63]\n\t" \
    "ds_read_b128 v[24:27], v96 offset:17408\n\tv_mfma_f32_16x16x32_bf16 v[56:59], a[188:191], v[44:47], v[56:59]\n\tv_mfma_f32_16x16x32_bf16 v[52:55], a[124:127], v[44:47], v[52:55]\n\t" \
    "buffer_load_dwordx4 %[dv], %[ra], %[so] offen offset:128 lds\n\tv_mfma_f32_16x16x32_bf16 v[48:51], a[60:63], v[44:47], v[48:51]\n\ts_waitcnt lgkmcnt(2)\n\ts_add_u32 m0, m0, 896\n\t" \
    "v_mfma_f32_16x16x32_bf16 v[64:67], a[0:3], v[16:19], 0\n\tds_read_b128 v[28:31], v97 offset:17408\n\tv_mfma_f32_16x16x32_bf16 v[68:71], a[64:67], v[16:19], 0\n\t" \
    "v_mfma_f32_16x16x32_bf16 v[72:75], a[128:131], v[16:19], 0\n\tbuffer_load_dwordx4 %[dv], %[ra], %[so] offen offset:256 lds\n\tv_mul_f32 v48, %[sc], v48\n\tv_mul_f32 v49, %[sc], v49\n\t" \
    "v_mfma_f32_16x16x32_bf16 v[76:79], a[192:195], v[16:19], 0\n\ts_waitcnt lgkmcnt(2)\n\ts_add_u32 m0, m0, 896\n\tv_mul_f32 v50, %[sc], v50\n\tv_mul_f32 v51, %[sc], v51\n\t" \
    "v_mfma_f32_16x16x32_bf16 v[76:79], a[196:199], v[20:23], v[76:79]\n\tds_read_b128 v[32:35], v96 offset:18432\n\tv_cvt_pk_bf16_f32 v80, v48, v49\n\t" \
    "v_mfma_f32_16x16x32_bf16 v[72:75], a[132:135], v[20:23], v[72:75]\n\tv_cvt_pk_bf16_f32 v81, v50, v51\n\tv_mul_f32 v52, %[sc], v52\n\tv_mfma_f32_16x16x32_bf16 v[68:71], a[68:71], v[20:23], v[68:71]\n\t" \
    "buffer_load_dwordx4 %[dv], %[ra], %[so] offen offset:384 lds\n\tv_mul_f32 v53, %[sc], v53\n\tv_mul_f32 v54, %[sc], v54\n\tv_mfma_f32_16x16x32_bf16 v[64:67], a[4:7], v[20:23], v[64:67]\n\t" \
    "s_waitcnt lgkmcnt(2)\n\ts_add_u32 m0, m0, 896\n\tv_mul_f32 v55, %[sc], v55\n\tv_cvt_pk_bf16_f32 v82, v52, v53\n\tv_mfma_f32_16x16x32_bf16 v[64:67], a[8:11], v[24:27], v[64:67]\n\t" \
    "ds_read_b128 v[36:39], v97 offset:18432\n\tv_cvt_pk_bf16_f32 v83, v54, v55\n\tv_mfma_f32_16x16x32_bf16 v[68:71], a[72:75], v[24:27], v[68:71]\n\t" \
    "buffer_store_dwordx4 v[80:83], v98, %[rc], %[sp] offen sc1\n\tv_mul_f32 v56, %[sc], v56\n\tv_mfma_f32_16x16x32_bf16 v[72:75], a[136:139], v[24:27], v[72:75]\n\t" \
    "buffer_load_dwordx4 %[dv], %[ra], %[so] offen offset:512 lds\n\tv_mul_f32 v57, %[sc], v57\n\tv_mul_f32 v58, %[sc], v58\n\tv_mfma_f32_16x16x32_bf16 v[76:79], a[200:203], v[24:27], v[76:79]\n\t" \
    "s_waitcnt lgkmcnt(2)\n\ts_add_u32 m0, m0, 896\n\tv_mul_f32 v59, %[sc], v59\n\tv_cvt_pk_bf16_f32 v84, v56, v57\n\tv_mfma_f32_16x16x32_bf16 v[76:79], a[204:207], v[28:31], v[76:79]\n\t" \
    "ds_read_b128 v[40:43], v96 offset:19456\n\tv_cvt_pk_bf16_f32 v85, v58, v59\n\tv_mfma_f32_16x16x32_bf16 v[72:75], a[140:143], v[28:31], v[72:75]\n\tv_mul_f32 v60, %[sc], v60\n\t" \
    "v_mul_f32 v61, %[sc], v61\n\tv_mfma_f32_16x16x32_bf16 v[68:71], a[76:79], v[28:31], v[68:71]\n\tbuffer_load_dwordx4 %[dv], %[ra], %[so] offen offset:640 lds\n\tv_mul_f32 v62, %[sc], v62\n\t" \
    "v_mul_f32 v63, %[sc], v63\n\tv_mfma_f32_16x16x32_bf16 v[64:67], a[12:15], v[28:31], v[64:67]\n\ts_waitcnt lgkmcnt(2)\n\ts_add_u32 m0, m0, 896\n\tv_cvt_pk_bf16_f32 v86, v60, v61\n\t" \
    "v_cvt_pk_bf16_f32 v87, v62, v63\n\tv_mfma_f32_16x16x32_bf16 v[64:67], a[16:19], v[32:35], v[64:67]\n\tds_read_b128 v[44:47], v97 offset:19456\n\t" \
    "buffer_store_dwordx4 v[84:87], v98, %[rc], %[sp] offen offset:2048 sc1\n\tv_mfma_f32_16x16x32_bf16 v[68:71], a[80:83], v[32:35], v[68:71]\n\t" \
    "v_mfma_f32_16x16x32_bf16 v[72:75], a[144:147], v[32:35], v[72:75]\n\tbuffer_load_dwordx4 %[dv], %[ra], %[so] offen offset:768 lds\n\t" \
    "v_mfma_f32_16x16x32_bf16 v[76:79], a[208:211], v[32:35], v[76:79]\n\ts_waitcnt lgkmcnt(2)\n\ts_add_u32 m0, m0, 896\n\tv_mfma_f32_16x16x32_bf16 v[76:79], a[212:215], v[36:39], v[76:79]\n\t" \
    "ds_read_b128 v[16:19], v96 offset:20480\n\tv_mfma_f32_16x16x32_bf16 v[72:75], a[148:151], v[36:39], v[72:75]\n\tv_mfma_f32_16x16x32_bf16 v[68:71], a[84:87], v[36:39], v[68:71]\n\t" \
    "buffer_load_dwordx4 %[dv], %[ra], %[so] offen offset:896 lds\n\tv_mfma_f32_16x16x32_bf16 v[64:67], a[20:23], v[36:39], v[64:67]\n\ts_waitcnt lgkmcnt(2)\n\ts_add_u32 %[so], %[so], %[c15]\n\t" \
    "v_mfma_f32_16x16x32_bf16 v[64:67], a[24:27], v[40:43], v[64:67]\n\tds_read_b128 v[20:23], v97 offset:20480\n\tv_mfma_f32_16x16x32_bf16 v[68:71], a[88:91], v[40:43], v[68:71]\n\t" \
    "v_mfma_f32_16x16x32_bf16 v[72:75], a[152:155], v[40:43], v[72:75]\n\ts_min_u32 %[so], %[so], 0x1f8000\n\tv_mfma_f32_16x16x32_bf16 v[76:79], a[216:219], v[40:43], v[76:79]\n\ts_waitcnt lgkmcnt(2)\n\t" \
    "v_mfma_f32_16x16x32_bf16 v[76:79], a[220:223], v[44:47], v[76:79]\n\tds_read_b128 v[24:27], v96 offset:21504\n\tv_mfma_f32_16x16x32_bf16 v[72:75], a[156:159], v[44:47], v[72:75]\n\t" \
    "v_mfma_f32_16x16x32_bf16 v[68:71], a[92:95], v[44:47], v[68:71]\n\tv_mfma_f32_16x16x32_bf16 v[64:67], a[28:31], v[44:47], v[64:67]\n\ts_waitcnt lgkmcnt(2)\n\t" \
    "v_mfma_f32_16x16x32_bf16 v[64:67], a[32:35], v[16:19], v[64:67]\n\tds_read_b128 v[28:31], v97 offset:21504\n\tv_mfma_f32_16x16x32_bf16 v[68:71], a[96:99], v[16:19], v[68:71]\n\t" \
    "v_mfma_f32_16x16x32_bf16 v[72:75], a[160:163], v[16:19], v[72:75]\n\tv_mfma_f32_16x16x32_bf16 v[76:79], a[224:227], v[16:19], v[76:79]\n\ts_waitcnt lgkmcnt(2)\n\t" \
    "v_mfma_f32_16x16x32_bf16 v[76:79], a[228:231], v[20:23], v[76:79]\n\tds_read_b128 v[32:35], v96 offset:22528\n\tv_mfma_f32_16x16x32_bf16 v[72:75], a[164:167], v[20:23], v[72:75]\n\t" \
    "v_mfma_f32_16x16x32_bf16 v[68:71], a[100:103], v[20:23], v[68:71]\n\tv_mfma_f32_16x16x32_bf16 v[64:67], a[36:39], v[20:23], v[64:67]\n\ts_waitcnt lgkmcnt(2)\n\t" \
    "v_mfma_f32_16x16x32_bf16 v[64:67], a[40:43], v[24:27], v[64:67]\n\tds_read_b128 v[36:39], v97 offset:22528\n\tv_mfma_f32_16x16x32_bf16 v[68:71], a[104:107], v[24:27], v[68:71]\n\t" \
    "v_mfma_f32_16x16x32_bf16 v[72:75], a[168:171], v[24:27], v[72:75]\n\tv_mfma_f32_16x16x32_bf16 v[76:79], a[232:235], v[24:27], v[76:79]\n\ts_waitcnt lgkmcnt(2)\n\t" \
    "v_mfma_f32_16x16x32_bf16 v[76:79], a[236:239], v[28:31], v[76:79]\n\tds_read_b128 v[40:43], v96 offset:23552\n\tv_mfma_f32_16x16x32_bf16 v[72:75], a[172:175], v[28:31], v[72:75]\n\t" \
    "v_add_u32 v96, s62, %[xb]\n\tv_mfma_f32_16x16x32_bf16 v[68:71], a[108:111], v[28:31], v[68:71]\n\tv_mfma_f32_16x16x32_bf16 v[64:67], a[44:47], v[28:31], v[64:67]\n\ts_waitcnt lgkmcnt(2)\n\t" \
    "v_mfma_f32_16x16x32_bf16 v[64:67], a[48:51], v[32:35], v[64:67]\n\tds_read_b128 v[44:47], v97 offset:23552\n\tv_mfma_f32_16x16x32_bf16 v[68:71], a[112:115], v[32:35], v[68:71]\n\t" \
    "v_xor_b32 v97, 64, v96\n\tv_mfma_f32_16x16x32_bf16 v[72:75], a[176:179], v[32:35], v[72:75]\n\tv_mfma_f32_16x16x32_bf16 v[76:79], a[240:243], v[32:35], v[76:79]\n\ts_waitcnt lgkmcnt(2)\n\t" \
    "v_mfma_f32_16x16x32_bf16 v[76:79], a[244:247], v[36:39], v[76:79]\n\tds_read_b128 v[16:19], v96\n\tv_mfma_f32_16x16x32_bf16 v[72:75], a[180:183], v[36:39], v[72:75]\n\t" \
    "v_mfma_f32_16x16x32_bf16 v[68:71], a[116:119], v[36:39], v[68:71]\n\tv_mfma_f32_16x16x32_bf16 v[64:67], a[52:55], v[36:39], v[64:67]\n\ts_waitcnt lgkmcnt(2)\n\t" \
    "v_mfma_f32_16x16x32_bf16 v[64:67], a[56:59], v[40:43], v[64:67]\n\tds_read_b128 v[20:23], v97\n\tv_mfma_f32_16x16x32_bf16 v[68:71], a[120:123], v[40:43], v[68:71]\n\t" \
    "v_mfma_f32_16x16x32_bf16 v[72:75], a[184:187], v[40:43], v[72:75]\n\tv_mfma_f32_16x16x32_bf16 v[76:79], a[248:251], v[40:43], v[76:79]\n\ts_waitcnt lgkmcnt(2)\n\t" \
    "v_mfma_f32_16x16x32_bf16 v[76:79], a[252:255], v[44:47], v[76:79]\n\tds_read_b128 v[24:27], v96 offset:1024\n\tv_mfma_f32_16x16x32_bf16 v[72:75], a[188:191], v[44:47], v[72:75]\n\t" \
    "s_add_u32 s61, s61, 1\n\tv_mfma_f32_16x16x32_bf16 v[68:71], a[124:127], v[44:47], v[68:71]\n\ts_cmp_lt_u32 s61, %[nn]\n\tv_mfma_f32_16x16x32_bf16 v[64:67], a[60:63], v[44:47], v[64:67]\n\t" \
    "s_waitcnt lgkmcnt(2)\n\ts_cbranch_scc1 1b\n\ts_mov_b32 m0, s60\n\ts_waitcnt lgkmcnt(0)\n\ts_nop 7\n\tv_mul_f32 v64, %[sc], v64\n\tv_mul_f32 v65, %[sc], v65\n\tv_mul_f32 v66, %[sc], v66\n\t" \
    "v_mul_f32 v67, %[sc], v67\n\tv_cvt_pk_bf16_f32 v88, v64, v65\n\tv_cvt_pk_bf16_f32 v89, v66, v67\n\tv_mul_f32 v68, %[sc], v68\n\tv_mul_f32 v69, %[sc], v69\n\tv_mul_f32 v70, %[sc], v70\n\t" \
    "v_mul_f32 v71, %[sc], v71\n\tv_cvt_pk_bf16_f32 v90, v68, v69\n\tv_cvt_pk_bf16_f32 v91, v70, v71\n\tbuffer_store_dwordx4 v[88:91], v98, %[rc], %[sp] offen offset:1024 sc1\n\t" \
    "v_mul_f32 v72, %[sc], v72\n\tv_mul_f32 v73, %[sc], v73\n\tv_mul_f32 v74, %[sc], v74\n\tv_mul_f32 v75, %[sc], v75\n\tv_cvt_pk_bf16_f32 v92, v72, v73\n\tv_cvt_pk_bf16_f32 v93, v74, v75\n\t" \
    "v_mul_f32 v76, %[sc], v76\n\tv_mul_f32 v77, %[sc], v77\n\tv_mul_f32 v78, %[sc], v78\n\tv_mul_f32 v79, %[sc], v79\n\tv_cvt_pk_bf16_f32 v94, v76, v77\n\tv_cvt_pk_bf16_f32 v95, v78, v79\n\t" \
    "buffer_store_dwordx4 v[92:95], v98, %[rc], %[sp] offen offset:3072 sc1\n\ts_nop 1\n\t"
#define ASM_PROJP_K \
    "s_mov_b32 s60, m0\n\ts_mov_b32 s61, 0\n\tv_add_u32 v96, 0x18000, %[xb]\n\tv_xor_b32 v97, 64, v96\n\tv_bfrev_b32 v98, 1\n\tds_read_b128 v[16:19], v96\n\tds_read_b128 v[20:23], v97\n\t" \
    "ds_read_b128 v[24:27], v96 offset:1024\n\ts_waitcnt lgkmcnt(2)\n\t1:\nv_mfma_f32_16x16x32_bf16 v[48:51], a[0:3], v[16:19], 0\n\tds_read_b128 v[28:31], v97 offset:1024\n\t" \
    "v_mfma_f32_16x16x32_bf16 v[52:55], a[64:67], v[16:19], 0\n\ts_lshl_b32 s62, s61, 15\n\tv_mfma_f32_16x16x32_bf16 v[56:59], a[128:131], v[16:19], 0\n\ts_and_b32 s62, s62, 0x18000\n\t" \
    "v_cvt_pk_bf16_f32 v88, v64, v65\n\tv_cvt_pk_bf16_f32 v89, v66, v67\n\tv_mfma_f32_16x16x32_bf16 v[60:63], a[192:195], v[16:19], 0\n\ts_waitcnt lgkmcnt(2)\n\tv_cvt_pk_bf16_f32 v90, v68, v69\n\t" \
    "v_cvt_pk_bf16_f32 v91, v70, v71\n\tv_mfma_f32_16x16x32_bf16 v[60:63], a[196:199], v[20:23], v[60:63]\n\tds_read_b128 v[32:35], v96 offset:2048\n\t" \
    "buffer_store_dwordx4 v[88:91], v98, %[rc], %[sp] offen offset:1024 sc1\n\tv_mfma_f32_16x16x32_bf16 v[56:59], a[132:135], v[20:23], v[56:59]\n\ts_xor_b32 s63, s62, 0x10000\n\t" \
    "v_cvt_pk_bf16_f32 v92, v72, v73\n\tv_cvt_pk_bf16_f32 v93, v74, v75\n\tv_mfma_f32_16x16x32_bf16 v[52:55], a[68:71], v[20:23], v[52:55]\n\ts_add_u32 s63, s63, %[lb]\n\t" \
    "v_cvt_pk_bf16_f32 v94, v76, v77\n\tv_cvt_pk_bf16_f32 v95, v78, v79\n\tv_mfma_f32_16x16x32_bf16 v[48:51], a[4:7], v[20:23], v[48:51]\n\ts_waitcnt lgkmcnt(2)\n\t" \
    "buffer_store_dwordx4 v[92:95], v98, %[rc], %[sp] offen offset:3072 sc1\n\tv_mov_b32 v98, %[co]\n\tv_mfma_f32_16x16x32_bf16 v[48:51], a[8:11], v[24:27], v[48:51]\n\t" \
    "ds_read_b128 v[36:39], v97 offset:2048\n\ts_add_u32 %[sp], %[sp], %[c18]\n\tv_mfma_f32_16x16x32_bf16 v[52:55], a[72:75], v[24:27], v[52:55]\n\tv_mov_b32 v99, s61\n\t" \
    "v_mfma_f32_16x16x32_bf16 v[56:59], a[136:139], v[24:27], v[56:59]\n\tv_mfma_f32_16x16x32_bf16 v[60:63], a[200:203], v[24:27], v[60:63]\n\ts_waitcnt lgkmcnt(2)\n\t" \
    "v_mfma_f32_16x16x32_bf16 v[60:63], a[204:207], v[28:31], v[60:63]\n\tds_read_b128 v[40:43], v96 offset:3072\n\tv_mfma_f32_16x16x32_bf16 v[56:59], a[140:143], v[28:31], v[56:59]\n\t" \
    "v_mfma_f32_16x16x32_bf16 v[52:55], a[76:79], v[28:31], v[52:55]\n\tv_mfma_f32_16x16x32_bf16 v[48:51], a[12:15], v[28:31], v[48:51]\n\ts_waitcnt lgkmcnt(2)\n\t" \
    "v_mfma_f32_16x16x32_bf16 v[48:51], a[16:19], v[32:35], v[48:51]\n\tds_read_b128 v[44:47], v97 offset:3072\n\tv_mfma_f32_16x16x32_bf16 v[52:55], a[80:83], v[32:35], v[52:55]\n\t" \
    "v_mfma_f32_16x16x32_bf16 v[56:59], a[144:147], v[32:35], v[56:59]\n\tv_mfma_f32_16x16x32_bf16 v[60:63], a[208:211], v[32:35], v[60:63]\n\ts_waitcnt lgkmcnt(2)\n\t" \
    "v_mfma_f32_16x16x32_bf16 v[60:63], a[212:215], v[36:39], v[60:63]\n\tds_read_b128 v[16:19], v96 offset:4096\n\tv_mfma_f32_16x16x32_bf16 v[56:59], a[148:151], v[36:39], v[56:59]\n\t" \
    "v_mfma_f32_16x16x32_bf16 v[52:55], a[84:87], v[36:39], v[52:55]\n\tv_mfma_f32_16x16x32_bf16 v[48:51], a[20:23], v[36:39], v[48:51]\n\ts_waitcnt lgkmcnt(2)\n\t" \
    "v_mfma_f32_16x16x32_bf16 v[48:51], a[24:27], v[40:43], v[48:51]\n\tds_read_b128 v[20:23], v97 offset:4096\n\tv_mfma_f32_16x16x32_bf16 v[52:55], a[88:91], v[40:43], v[52:55]\n\t" \
    "v_mfma_f32_16x16x32_bf16 v[56:59], a[152:155], v[40:43], v[56:59]\n\tv_mfma_f32_16x16x32_bf16 v[60:63], a[216:219], v[40:43], v[60:63]\n\ts_waitcnt lgkmcnt(2)\n\t" \
    "v_mfma_f32_16x16x32_bf16 v[60:63], a[220:223], v[44:47], v[60:63]\n\tds_read_b128 v[24:27], v96 offset:5120\n\tv_mfma_f32_16x16x32_bf16 v[56:59], a[156:159], v[44:47], v[56:59]\n\t" \
    "v_mfma_f32_16x16x32_bf16 v[52:55], a[92:95], v[44:47], v[52:55]\n\tv_mfma_f32_16x16x32_bf16 v[48:51], a[28:31], v[44:47], v[48:51]\n\ts_waitcnt lgkmcnt(2)\n\t" \
    "v_mfma_f32_16x16x32_bf16 v[48:51], a[32:35], v[16:19], v[48:51]\n\tds_read_b128 v[28:31], v97 offset:5120\n\tv_mfma_f32_16x16x32_bf16 v[52:55], a[96:99], v[16:19], v[52:55]\n\t" \
    "v_mfma_f32_16x16x32_bf16 v[56:59], a[160:163], v[16:19], v[56:59]\n\tv_mfma_f32_16x16x32_bf16 v[60:63], a[224:227], v[16:19], v[60:63]\n\ts_waitcnt lgkmcnt(2)\n\t" \
    "v_mfma_f32_16x16x32_bf16 v[60:63], a[228:231], v[20:23], v[60:63]\n\tds_read_b128 v[32:35], v96 offset:6144\n\tv_mfma_f32_16x16x32_bf16 v[56:59], a[164:167], v[20:23], v[56:59]\n\t" \
    "v_mfma_f32_16x16x32_bf16 v[52:55], a[100:103], v[20:23], v[52:55]\n\tv_mfma_f32_16x16x32_bf16 v[48:51], a[36:39], v[20:23], v[48:51]\n\ts_waitcnt lgkmcnt(2)\n\t" \
    "v_mfma_f32_16x16x32_bf16 v[48:51], a[40:43], v[24:27], v[48:51]\n\tds_read_b128 v[36:39], v97 offset:6144\n\tv_mfma_f32_16x16x32_bf16 v[52:55], a[104:107], v[24:27], v[52:55]\n\t" \
    "v_mfma_f32_16x16x32_bf16 v[56:59], a[168:171], v[24:27], v[56:59]\n\tv_mfma_f32_16x16x32_bf16 v[60:63], a[232:235], v[24:27], v[60:63]\n\ts_waitcnt lgkmcnt(2)\n\t" \
    "v_mfma_f32_16x16x32_bf16 v[60:63], a[236:239], v[28:31], v[60:63]\n\tds_read_b128 v[40:43], v96 offset:7168\n\tv_mfma_f32_16x16x32_bf16 v[56:59], a[172:175], v[28:31], v[56:59]\n\t" \
    "v_mfma_f32_16x16x32_bf16 v[52:55], a[108:111], v[28:31], v[52:55]\n\tv_mfma_f32_16x16x32_bf16 v[48:51], a[44:47], v[28:31], v[48:51]\n\ts_waitcnt lgkmcnt(2)\n\t" \
    "v_mfma_f32_16x16x32_bf16 v[48:51], a[48:51], v[32:35], v[48:51]\n\tds_read_b128 v[44:47], v97 offset:7168\n\tv_mfma_f32_16x16x32_bf16 v[52:55], a[112:115], v[32:35], v[52:55]\n\t" \
    "v_mfma_f32_16x16x32_bf16 v[56:59], a[176:179], v[32:35], v[56:59]\n\tv_mfma_f32_16x16x32_bf16 v[60:63], a[240:243], v[32:35], v[60:63]\n\ts_waitcnt lgkmcnt(2)\n\t" \
    "v_mfma_f32_16x16x32_bf16 v[60:63], a[244:247], v[36:39], v[60:63]\n\tds_read_b128 v[16:19], v96 offset:16384\n\tv_mfma_f32_16x16x32_bf16 v[56:59], a[180:183], v[36:39], v[56:59]\n\t" \
    "v_mfma_f32_16x16x32_bf16 v[52:55], a[116:119], v[36:39], v[52:55]\n\tv_mfma_f32_16x16x32_bf16 v[48:51], a[52:55], v[36:39], v[48:51]\n\ts_waitcnt lgkmcnt(2)\n\t" \
    "v_mfma_f32_16x16x32_bf16 v[48:51], a[56:59], v[40:43], v[48:51]\n\ts_waitcnt vmcnt(0)\n\ts_barrier\n\ts_mov_b32 m0, s63\n\tds_read_b128 v[20:23], v97 offset:16384\n\t" \
    "v_mfma_f32_16x16x32_bf16 v[52:55], a[120:123], v[40:43], v[52:55]\n\ts_mov_b64 exec, %[em]\n\tglobal_store_dword %[co], v99, %[pg] sc1\n\ts_mov_b64 exec, -1\n\t" \
    "v_mfma_f32_16x16x32_bf16 v[56:59], a[184:187], v[40:43], v[56:59]\n\tbuffer_load_dwordx4 %[dv], %[ra], %[so] offen lds\n\tv_mfma_f32_16x16x32_bf16 v[60:63], a[248:251], v[40:43], v[60:63]\n\t" \
    "s_waitcnt lgkmcnt(2)\n\ts_add_u32 m0, m0, 896\n\tv_mfma_f32_16x16x32_bf16 v[60:63], a[252:255], v[44:47], v[60:63]\n\tds_read_b128 v[24:27], v96 offset:17408\n\t" \
    "v_mfma_f32_16x16x32_bf16 v[56:59], a[188:191], v[44:47], v[56:59]\n\tv_mfma_f32_16x16x32_bf16 v[52:55], a[124:127], v[44:47], v[52:55]\n\t" \
    "buffer_load_dwordx4 %[dv], %[ra], %[so] offen offset:128 lds\n\tv_mfma_f32_16x16x32_bf16 v[48:51], a[60:63], v[44:47], v[48:51]\n\ts_waitcnt lgkmcnt(2)\n\ts_add_u32 m0, m0, 896\n\t" \
    "v_mfma_f32_16x16x32_bf16 v[64:67], a[0:3], v[16:19], 0\n\tds_read_b128 v[28:31], v97 offset:17408\n\tv_mfma_f32_16x16x32_bf16 v[68:71], a[64:67], v[16:19], 0\n\t" \
    "v_mfma_f32_16x16x32_bf16 v[72:75], a[128:131], v[16:19], 0\n\tbuffer_load_dwordx4 %[dv], %[ra], %[so] offen offset:256 lds\n\tv_cvt_pk_bf16_f32 v80, v48, v49\n\tv_cvt_pk_bf16_f32 v81, v50, v51\n\t" \
    "v_mfma_f32_16x16x32_bf16 v[76:79], a[192:195], v[16:19], 0\n\ts_waitcnt lgkmcnt(2)\n\ts_add_u32 m0, m0, 896\n\tv_cvt_pk_bf16_f32 v82, v52, v53\n\tv_cvt_pk_bf16_f32 v83, v54, v55\n\t" \
    "v_mfma_f32_16x16x32_bf16 v[76:79], a[196:199], v[20:23], v[76:79]\n\tds_read_b128 v[32:35], v96 offset:18432\n\tbuffer_store_dwordx4 v[80:83], v98, %[rc], %[sp] offen sc1\n\t" \
    "v_mfma_f32_16x16x32_bf16 v[72:75], a[132:135], v[20:23], v[72:75]\n\tv_cvt_pk_bf16_f32 v84, v56, v57\n\tv_cvt_pk_bf16_f32 v85, v58, v59\n\t" \
    "v_mfma_f32_16x16x32_bf16 v[68:71], a[68:71], v[20:23], v[68:71]\n\tbuffer_load_dwordx4 %[dv], %[ra], %[so] offen offset:384 lds\n\tv_cvt_pk_bf16_f32 v86, v60, v61\n\t" \
    "v_cvt_pk_bf16_f32 v87, v62, v63\n\tv_mfma_f32_16x16x32_bf16 v[64:67], a[4:7], v[20:23], v[64:67]\n\ts_waitcnt lgkmcnt(2)\n\ts_add_u32 m0, m0, 896\n\t" \
    "buffer_store_dwordx4 v[84:87], v98, %[rc], %[sp] offen offset:2048 sc1\n\tv_mfma_f32_16x16x32_bf16 v[64:67], a[8:11], v[24:27], v[64:67]\n\tds_read_b128 v[36:39], v97 offset:18432\n\t" \
    "v_mfma_f32_16x16x32_bf16 v[68:71], a[72:75], v[24:27], v[68:71]\n\tv_mfma_f32_16x16x32_bf16 v[72:75], a[136:139], v[24:27], v[72:75]\n\tbuffer_load_dwordx4 %[dv], %[ra], %[so] offen offset:512 lds\n\t" \
    "v_mfma_f32_16x16x32_bf16 v[76:79], a[200:203], v[24:27], v[76:79]\n\ts_waitcnt lgkmcnt(2)\n\ts_add_u32 m0, m0, 896\n\tv_mfma_f32_16x16x32_bf16 v[76:79], a[204:207], v[28:31], v[76:79]\n\t" \
    "ds_read_b128 v[40:43], v96 offset:19456\n\tv_mfma_f32_16x16x32_bf16 v[72:75], a[140:143], v[28:31], v[72:75]\n\tv_mfma_f32_16x16x32_bf16 v[68:71], a[76:79], v[28:31], v[68:71]\n\t" \
    "buffer_load_dwordx4 %[dv], %[ra], %[so] offen offset:640 lds\n\tv_mfma_f32_16x16x32_bf16 v[64:67], a[12:15], v[28:31], v[64:67]\n\ts_waitcnt lgkmcnt(2)\n\ts_add_u32 m0, m0, 896\n\t" \
    "v_mfma_f32_16x16x32_bf16 v[64:67], a[16:19], v[32:35], v[64:67]\n\tds_read_b128 v[44:47], v97 offset:19456\n\tv_mfma_f32_16x16x32_bf16 v[68:71], a[80:83], v[32:35], v[68:71]\n\t" \
    "v_mfma_f32_16x16x32_bf16 v[72:75], a[144:147], v[32:35], v[72:75]\n\tbuffer_load_dwordx4 %[dv], %[ra], %[so] offen offset:768 lds\n\t" \
    "v_mfma_f32_16x16x32_bf16 v[76:79], a[208:211], v[32:35], v[76:79]\n\ts_waitcnt lgkmcnt(2)\n\ts_add_u32 m0, m0, 896\n\tv_mfma_f32_16x16x32_bf16 v[76:79], a[212:215], v[36:39], v[76:79]\n\t" \
    "ds_read_b128 v[16:19], v96 offset:20480\n\tv_mfma_f32_16x16x32_bf16 v[72:75], a[148:151], v[36:39], v[72:75]\n\tv_mfma_f32_16x16x32_bf16 v[68:71], a[84:87], v[36:39], v[68:71]\n\t" \
    "buffer_load_dwordx4 %[dv], %[ra], %[so] offen offset:896 lds\n\tv_mfma_f32_16x16x32_bf16 v[64:67], a[20:23], v[36:39], v[64:67]\n\ts_waitcnt lgkmcnt(2)\n\ts_add_u32 %[so], %[so], %[c15]\n\t" \
    "v_mfma_f32_16x16x32_bf16 v[64:67], a[24:27], v[40:43], v[64:67]\n\tds_read_b128 v[20:23], v97 offset:20480\n\tv_mfma_f32_16x16x32_bf16 v[68:71], a[88:91], v[40:43], v[68:71]\n\t" \
    "v_mfma_f32_16x16x32_bf16 v[72:75], a[152:155], v[40:43], v[72:75]\n\ts_min_u32 %[so], %[so], 0x1f8000\n\tv_mfma_f32_16x16x32_bf16 v[76:79], a[216:219], v[40:43], v[76:79]\n\ts_waitcnt lgkmcnt(2)\n\t" \
    "v_mfma_f32_16x16x32_bf16 v[76:79], a[220:223], v[44:47], v[76:79]\n\tds_read_b128 v[24:27], v96 offset:21504\n\tv_mfma_f32_16x16x32_bf16 v[72:75], a[156:159], v[44:47], v[72:75]\n\t" \
    "v_mfma_f32_16x16x32_bf16 v[68:71], a[92:95], v[44:47], v[68:71]\n\tv_mfma_f32_16x16x32_bf16 v[64:67], a[28:31], v[44:47], v[64:67]\n\ts_waitcnt lgkmcnt(2)\n\t" \
    "v_mfma_f32_16x16x32_bf16 v[64:67], a[32:35], v[16:19], v[64:67]\n\tds_read_b128 v[28:31], v97 offset:21504\n\tv_mfma_f32_16x16x32_bf16 v[68:71], a[96:99], v[16:19], v[68:71]\n\t" \
    "v_mfma_f32_16x16x32_bf16 v[72:75], a[160:163], v[16:19], v[72:75]\n\tv_mfma_f32_16x16x32_bf16 v[76:79], a[224:227], v[16:19], v[76:79]\n\ts_waitcnt lgkmcnt(2)\n\t" \
    "v_mfma_f32_16x16x32_bf16 v[76:79], a[228:231], v[20:23], v[76:79]\n\tds_read_b128 v[32:35], v96 offset:22528\n\tv_mfma_f32_16x16x32_bf16 v[72:75], a[164:167], v[20:23], v[72:75]\n\t" \
    "v_mfma_f32_16x16x32_bf16 v[68:71], a[100:103], v[20:23], v[68:71]\n\tv_mfma_f32_16x16x32_bf16 v[64:67], a[36:39], v[20:23], v[64:67]\n\ts_waitcnt lgkmcnt(2)\n\t" \
    "v_mfma_f32_16x16x32_bf16 v[64:67], a[40:43], v[24:27], v[64:67]\n\tds_read_b128 v[36:39], v97 offset:22528\n\tv_mfma_f32_16x16x32_bf16 v[68:71], a[104:107], v[24:27], v[68:71]\n\t" \
    "v_mfma_f32_16x16x32_bf16 v[72:75], a[168:171], v[24:27], v[72:75]\n\tv_mfma_f32_16x16x32_bf16 v[76:79], a[232:235], v[24:27], v[76:79]\n\ts_waitcnt lgkmcnt(2)\n\t" \
    "v_mfma_f32_16x16x32_bf16 v[76:79], a[236:239], v[28:31], v[76:79]\n\tds_read_b128 v[40:43], v96 offset:23552\n\tv_mfma_f32_16x16x32_bf16 v[72:75], a[172:175], v[28:31], v[72:75]\n\t" \
    "v_add_u32 v96, s62, %[xb]\n\tv_mfma_f32_16x16x32_bf16 v[68:71], a[108:111], v[28:31], v[68:71]\n\tv_mfma_f32_16x16x32_bf16 v[64:67], a[44:47], v[28:31], v[64:67]\n\ts_waitcnt lgkmcnt(2)\n\t" \
    "v_mfma_f32_16x16x32_bf16 v[64:67], a[48:51], v[32:35], v[64:67]\n\tds_read_b128 v[44:47], v97 offset:23552\n\tv_mfma_f32_16x16x32_bf16 v[68:71], a[112:115], v[32:35], v[68:71]\n\t" \
    "v_xor_b32 v97, 64, v96\n\tv_mfma_f32_16x16x32_bf16 v[72:75], a[176:179], v[32:35], v[72:75]\n\tv_mfma_f32_16x16x32_bf16 v[76:79], a[240:243], v[32:35], v[76:79]\n\ts_waitcnt lgkmcnt(2)\n\t" \
    "v_mfma_f32_16x16x32_bf16 v[76:79], a[244:247], v[36:39], v[76:79]\n\tds_read_b128 v[16:19], v96\n\tv_mfma_f32_16x16x32_bf16 v[72:75], a[180:183], v[36:39], v[72:75]\n\t" \
    "v_mfma_f32_16x16x32_bf16 v[68:71], a[116:119], v[36:39], v[68:71]\n\tv_mfma_f32_16x16x32_bf16 v[64:67], a[52:55], v[36:39], v[64:67]\n\ts_waitcnt lgkmcnt(2)\n\t" \
    "v_mfma_f32_16x16x32_bf16 v[64:67], a[56:59], v[40:43], v[64:67]\n\tds_read_b128 v[20:23], v97\n\tv_mfma_f32_16x16x32_bf16 v[68:71], a[120:123], v[40:43], v[68:71]\n\t" \
    "v_mfma_f32_16x16x32_bf16 v[72:75], a[184:187], v[40:43], v[72:75]\n\tv_mfma_f32_16x16x32_bf16 v[76:79], a[248:251], v[40:43], v[76:79]\n\ts_waitcnt lgkmcnt(2)\n\t" \
    "v_mfma_f32_16x16x32_bf16 v[76:79], a[252:255], v[44:47], v[76:79]\n\tds_read_b128 v[24:27], v96 offset:1024\n\tv_mfma_f32_16x16x32_bf16 v[72:75], a[188:191], v[44:47], v[72:75]\n\t" \
    "s_add_u32 s61, s61, 1\n\tv_mfma_f32_16x16x32_bf16 v[68:71], a[124:127], v[44:47], v[68:71]\n\ts_cmp_lt_u32 s61, %[nn]\n\tv_mfma_f32_16x16x32_bf16 v[64:67], a[60:63], v[44:47], v[64:67]\n\t" \
    "s_waitcnt lgkmcnt(2)\n\ts_cbranch_scc1 1b\n\ts_mov_b32 m0, s60\n\ts_waitcnt lgkmcnt(0)\n\ts_nop 7\n\tv_cvt_pk_bf16_f32 v88, v64, v65\n\tv_cvt_pk_bf16_f32 v89, v66, v67\n\t" \
    "v_cvt_pk_bf16_f32 v90, v68, v69\n\tv_cvt_pk_bf16_f32 v91, v70, v71\n\tbuffer_store_dwordx4 v[88:91], v98, %[rc], %[sp] offen offset:1024 sc1\n\tv_cvt_pk_bf16_f32 v92, v72, v73\n\t" \
    "v_cvt_pk_bf16_f32 v93, v74, v75\n\tv_cvt_pk_bf16_f32 v94, v76, v77\n\tv_cvt_pk_bf16_f32 v95, v78, v79\n\tbuffer_store_dwordx4 v[92:95], v98, %[rc], %[sp] offen offset:3072 sc1\n\ts_nop 1\n\t"
#define ASM_ATTN \
    "s_mov_b32 s60, m0\n\ts_mov_b32 s62, %[sk]\n\ts_add_u32 s63, %[sk], 0x1000000\n\ts_mov_b32 s61, %[sq]\n\tv_mov_b32 v208, 0\n\tv_mov_b32 v209, 1\n\tv_mov_b32 v210, -1\n\ts_mov_b64 exec, %[ep]\n\t" \
    "global_atomic_add v210, v208, v209, %[qc] sc0\n\ts_mov_b64 exec, -1\n\ts_add_u32 m0, %[lb], 0\n\ts_nop 0\n\tbuffer_load_dwordx4 %[dv], %[rs], s62 offen sc1 lds\n\t" \
    "buffer_load_dwordx4 %[dv], %[rs], s62 offen offset:1024 sc1 lds\n\ts_add_u32 s62, s62, 0x40000\n\tbuffer_load_dwordx4 a[128:131], %[dv], %[rs], s61 offen sc1\n\t" \
    "buffer_load_dwordx4 a[132:135], %[dv], %[rs], s61 offen offset:2048 sc1\n\tbuffer_load_dwordx4 a[136:139], %[dw], %[rs], s61 offen sc1\n\t" \
    "buffer_load_dwordx4 a[140:143], %[dw], %[rs], s61 offen offset:2048 sc1\n\ts_add_u32 s61, s61, 0x80000\n\tbuffer_load_dwordx4 a[144:147], %[dv], %[rs], s61 offen sc1\n\t" \
    "buffer_load_dwordx4 a[148:151], %[dv], %[rs], s61 offen offset:2048 sc1\n\tbuffer_load_dwordx4 a[152:155], %[dw], %[rs], s61 offen sc1\n\t" \
    "buffer_load_dwordx4 a[156:159], %[dw], %[rs], s61 offen offset:2048 sc1\n\ts_add_u32 s61, s61, 0x80000\n\tbuffer_load_dwordx4 a[160:163], %[dv], %[rs], s61 offen sc1\n\t" \
    "buffer_load_dwordx4 a[164:167], %[dv], %[rs], s61 offen offset:2048 sc1\n\tbuffer_load_dwordx4 a[168:171], %[dw], %[rs], s61 offen sc1\n\t" \
    "buffer_load_dwordx4 a[172:175], %[dw], %[rs], s61 offen offset:2048 sc1\n\ts_add_u32 s61, s61, 0x80000\n\tbuffer_load_dwordx4 a[176:179], %[dv], %[rs], s61 offen sc1\n\t" \
    "buffer_load_dwordx4 a[180:183], %[dv], %[rs], s61 offen offset:2048 sc1\n\tbuffer_load_dwordx4 a[184:187], %[dw], %[rs], s61 offen sc1\n\t" \
    "buffer_load_dwordx4 a[188:191], %[dw], %[rs], s61 offen offset:2048 sc1\n\ts_add_u32 s61, s61, 0x80000\n\ts_add_u32 m0, %[lb], 8192\n\ts_nop 0\n\t" \
    "buffer_load_dwordx4 %[dv], %[rs], s62 offen sc1 lds\n\tbuffer_load_dwordx4 %[dv], %[rs], s62 offen offset:1024 sc1 lds\n\ts_add_u32 s62, s62, 0x40000\n\ts_add_u32 m0, %[lb], 65536\n\ts_nop 0\n\t" \
    "buffer_load_dwordx4 %[dv], %[rs], s63 offen sc1 lds\n\tbuffer_load_dwordx4 %[dv], %[rs], s63 offen offset:1024 sc1 lds\n\ts_add_u32 s63, s63, 0x40000\n\ts_add_u32 m0, %[lb], 16384\n\ts_nop 0\n\t" \
    "buffer_load_dwordx4 %[dv], %[rs], s62 offen sc1 lds\n\tbuffer_load_dwordx4 %[dv], %[rs], s62 offen offset:1024 sc1 lds\n\ts_add_u32 s62, s62, 0x40000\n\ts_add_u32 m0, %[lb], 24576\n\ts_nop 0\n\t" \
    "buffer_load_dwordx4 %[dv], %[rs], s62 offen sc1 lds\n\tbuffer_load_dwordx4 %[dv], %[rs], s62 offen offset:1024 sc1 lds\n\ts_add_u32 s62, s62, 0x40000\n\ts_add_u32 m0, %[lb], 32768\n\ts_nop 0\n\t" \
    "buffer_load_dwordx4 %[dv], %[rs], s62 offen sc1 lds\n\tbuffer_load_dwordx4 %[dv], %[rs], s62 offen offset:1024 sc1 lds\n\ts_add_u32 s62, s62, 0x40000\n\ts_add_u32 m0, %[lb], 73728\n\ts_nop 0\n\t" \
    "buffer_load_dwordx4 %[dv], %[rs], s63 offen sc1 lds\n\tbuffer_load_dwordx4 %[dv], %[rs], s63 offen offset:1024 sc1 lds\n\ts_add_u32 s63, s63, 0x40000\n\ts_add_u32 m0, %[lb], 40960\n\ts_nop 0\n\t" \
    "buffer_load_dwordx4 %[dv], %[rs], s62 offen sc1 lds\n\tbuffer_load_dwordx4 %[dv], %[rs], s62 offen offset:1024 sc1 lds\n\ts_add_u32 s62, s62, 0x40000\n\ts_add_u32 m0, %[lb], 81920\n\ts_nop 0\n\t" \
    "buffer_load_dwordx4 %[dv], %[rs], s63 offen sc1 lds\n\tbuffer_load_dwordx4 %[dv], %[rs], s63 offen offset:1024 sc1 lds\n\ts_add_u32 s63, s63, 0x40000\n\tv_mov_b32 v176, %[ka]\n\t" \
    "v_add_u32 v177, 0x10000, %[ka]\n\tv_mov_b32 v180, 0xff800000\n\tv_mov_b32 v150, 0\n\tv_mov_b32 v151, 0\n\ts_mov_b64 s[66:67], 0\n\tv_cmp_le_i32 vcc, 0, %[md]\n\tv_mov_b32 v96, 0\n\t" \
    "v_accvgpr_write_b32 a0, 0\n\tv_cndmask_b32 v160, v180, v96, vcc\n\tv_cmp_le_i32 vcc, 1, %[md]\n\tv_mov_b32 v97, 0\n\tv_accvgpr_write_b32 a1, 0\n\tv_cndmask_b32 v161, v180, v97, vcc\n\t" \
    "v_cmp_le_i32 vcc, 2, %[md]\n\tv_mov_b32 v98, 0\n\tv_accvgpr_write_b32 a2, 0\n\tv_cndmask_b32 v162, v180, v98, vcc\n\tv_cmp_le_i32 vcc, 3, %[md]\n\tv_mov_b32 v99, 0\n\tv_accvgpr_write_b32 a3, 0\n\t" \
    "v_cndmask_b32 v163, v180, v99, vcc\n\tv_cmp_le_i32 vcc, 16, %[md]\n\tv_mov_b32 v100, 0\n\tv_accvgpr_write_b32 a4, 0\n\tv_cndmask_b32 v164, v180, v100, vcc\n\tv_cmp_le_i32 vcc, 17, %[md]\n\t" \
    "v_mov_b32 v101, 0\n\tv_accvgpr_write_b32 a5, 0\n\tv_cndmask_b32 v165, v180, v101, vcc\n\tv_cmp_le_i32 vcc, 18, %[md]\n\tv_mov_b32 v102, 0\n\tv_accvgpr_write_b32 a6, 0\n\t" \
    "v_cndmask_b32 v166, v180, v102, vcc\n\tv_cmp_le_i32 vcc, 19, %[md]\n\tv_mov_b32 v103, 0\n\tv_accvgpr_write_b32 a7, 0\n\tv_cndmask_b32 v167, v180, v103, vcc\n\tv_cmp_le_i32 vcc, 32, %[md]\n\t" \
    "v_mov_b32 v104, 0\n\tv_accvgpr_write_b32 a8, 0\n\tv_cndmask_b32 v168, v180, v104, vcc\n\tv_cmp_le_i32 vcc, 33, %[md]\n\tv_mov_b32 v105, 0\n\tv_accvgpr_write_b32 a9, 0\n\t" \
    "v_cndmask_b32 v169, v180, v105, vcc\n\tv_cmp_le_i32 vcc, 34, %[md]\n\tv_mov_b32 v106, 0\n\tv_accvgpr_write_b32 a10, 0\n\tv_cndmask_b32 v170, v180, v106, vcc\n\tv_cmp_le_i32 vcc, 35, %[md]\n\t" \
    "v_mov_b32 v107, 0\n\tv_accvgpr_write_b32 a11, 0\n\tv_cndmask_b32 v171, v180, v107, vcc\n\tv_cmp_le_i32 vcc, 48, %[md]\n\tv_mov_b32 v108, 0\n\tv_accvgpr_write_b32 a12, 0\n\t" \
    "v_cndmask_b32 v172, v180, v108, vcc\n\tv_cmp_le_i32 vcc, 49, %[md]\n\tv_mov_b32 v109, 0\n\tv_accvgpr_write_b32 a13, 0\n\tv_cndmask_b32 v173, v180, v109, vcc\n\tv_cmp_le_i32 vcc, 50, %[md]\n\t" \
    "v_mov_b32 v110, 0\n\tv_accvgpr_write_b32 a14, 0\n\tv_cndmask_b32 v174, v180, v110, vcc\n\tv_cmp_le_i32 vcc, 51, %[md]\n\tv_mov_b32 v111, 0\n\tv_accvgpr_write_b32 a15, 0\n\t" \
    "v_cndmask_b32 v175, v180, v111, vcc\n\tv_mov_b32 v152, 0\n\tv_mov_b32 v153, 0\n\tv_mov_b32 v154, 0\n\tv_mov_b32 v155, 0\n\tv_accvgpr_write_b32 a16, 0\n\tv_accvgpr_write_b32 a17, 0\n\t" \
    "v_accvgpr_write_b32 a18, 0\n\tv_accvgpr_write_b32 a19, 0\n\tv_accvgpr_write_b32 a20, 0\n\tv_accvgpr_write_b32 a21, 0\n\tv_accvgpr_write_b32 a22, 0\n\tv_accvgpr_write_b32 a23, 0\n\t" \
    "v_accvgpr_write_b32 a24, 0\n\tv_accvgpr_write_b32 a25, 0\n\tv_accvgpr_write_b32 a26, 0\n\tv_accvgpr_write_b32 a27, 0\n\tv_accvgpr_write_b32 a28, 0\n\tv_accvgpr_write_b32 a29, 0\n\t" \
    "v_accvgpr_write_b32 a30, 0\n\tv_accvgpr_write_b32 a31, 0\n\tv_accvgpr_write_b32 a32, 0\n\tv_accvgpr_write_b32 a33, 0\n\tv_accvgpr_write_b32 a34, 0\n\tv_accvgpr_write_b32 a35, 0\n\t" \
    "v_accvgpr_write_b32 a36, 0\n\tv_accvgpr_write_b32 a37, 0\n\tv_accvgpr_write_b32 a38, 0\n\tv_accvgpr_write_b32 a39, 0\n\tv_accvgpr_write_b32 a40, 0\n\tv_accvgpr_write_b32 a41, 0\n\t" \
    "v_accvgpr_write_b32 a42, 0\n\tv_accvgpr_write_b32 a43, 0\n\tv_accvgpr_write_b32 a44, 0\n\tv_accvgpr_write_b32 a45, 0\n\tv_accvgpr_write_b32 a46, 0\n\tv_accvgpr_write_b32 a47, 0\n\t" \
    "v_accvgpr_write_b32 a48, 0\n\tv_accvgpr_write_b32 a49, 0\n\tv_accvgpr_write_b32 a50, 0\n\tv_accvgpr_write_b32 a51, 0\n\tv_accvgpr_write_b32 a52, 0\n\tv_accvgpr_write_b32 a53, 0\n\t" \
    "v_accvgpr_write_b32 a54, 0\n\tv_accvgpr_write_b32 a55, 0\n\tv_accvgpr_write_b32 a56, 0\n\tv_accvgpr_write_b32 a57, 0\n\tv_accvgpr_write_b32 a58, 0\n\tv_accvgpr_write_b32 a59, 0\n\t" \
    "v_accvgpr_write_b32 a60, 0\n\tv_accvgpr_write_b32 a61, 0\n\tv_accvgpr_write_b32 a62, 0\n\tv_accvgpr_write_b32 a63, 0\n\tv_accvgpr_write_b32 a64, 0\n\tv_accvgpr_write_b32 a65, 0\n\t" \
    "v_accvgpr_write_b32 a66, 0\n\tv_accvgpr_write_b32 a67, 0\n\tv_accvgpr_write_b32 a68, 0\n\tv_accvgpr_write_b32 a69, 0\n\tv_accvgpr_write_b32 a70, 0\n\tv_accvgpr_write_b32 a71, 0\n\t" \
    "v_accvgpr_write_b32 a72, 0\n\tv_accvgpr_write_b32 a73, 0\n\tv_accvgpr_write_b32 a74, 0\n\tv_accvgpr_write_b32 a75, 0\n\tv_accvgpr_write_b32 a76, 0\n\tv_accvgpr_write_b32 a77, 0\n\t" \
    "v_accvgpr_write_b32 a78, 0\n\tv_accvgpr_write_b32 a79, 0\n\tv_accvgpr_write_b32 a80, 0\n\tv_accvgpr_write_b32 a81, 0\n\tv_accvgpr_write_b32 a82, 0\n\tv_accvgpr_write_b32 a83, 0\n\t" \
    "v_accvgpr_write_b32 a84, 0\n\tv_accvgpr_write_b32 a85, 0\n\tv_accvgpr_write_b32 a86, 0\n\tv_accvgpr_write_b32 a87, 0\n\tv_accvgpr_write_b32 a88, 0\n\tv_accvgpr_write_b32 a89, 0\n\t" \
    "v_accvgpr_write_b32 a90, 0\n\tv_accvgpr_write_b32 a91, 0\n\tv_accvgpr_write_b32 a92, 0\n\tv_accvgpr_write_b32 a93, 0\n\tv_accvgpr_write_b32 a94, 0\n\tv_accvgpr_write_b32 a95, 0\n\t" \
    "v_accvgpr_write_b32 a96, 0\n\tv_accvgpr_write_b32 a97, 0\n\tv_accvgpr_write_b32 a98, 0\n\tv_accvgpr_write_b32 a99, 0\n\tv_accvgpr_write_b32 a100, 0\n\tv_accvgpr_write_b32 a101, 0\n\t" \
    "v_accvgpr_write_b32 a102, 0\n\tv_accvgpr_write_b32 a103, 0\n\tv_accvgpr_write_b32 a104, 0\n\tv_accvgpr_write_b32 a105, 0\n\tv_accvgpr_write_b32 a106, 0\n\tv_accvgpr_write_b32 a107, 0\n\t" \
    "v_accvgpr_write_b32 a108, 0\n\tv_accvgpr_write_b32 a109, 0\n\tv_accvgpr_write_b32 a110, 0\n\tv_accvgpr_write_b32 a111, 0\n\tv_accvgpr_write_b32 a112, 0\n\tv_accvgpr_write_b32 a113, 0\n\t" \
    "v_accvgpr_write_b32 a114, 0\n\tv_accvgpr_write_b32 a115, 0\n\tv_accvgpr_write_b32 a116, 0\n\tv_accvgpr_write_b32 a117, 0\n\tv_accvgpr_write_b32 a118, 0\n\tv_accvgpr_write_b32 a119, 0\n\t" \
    "v_accvgpr_write_b32 a120, 0\n\tv_accvgpr_write_b32 a121, 0\n\tv_accvgpr_write_b32 a122, 0\n\tv_accvgpr_write_b32 a123, 0\n\tv_accvgpr_write_b32 a124, 0\n\tv_accvgpr_write_b32 a125, 0\n\t" \
    "v_accvgpr_write_b32 a126, 0\n\tv_accvgpr_write_b32 a127, 0\n\ts_waitcnt vmcnt(14)\n\tv_cmp_eq_u32 vcc, %[tc], v210\n\ts_cbranch_vccnz 6f\n\t7:\ns_barrier\n\tds_read_b128 v[112:115], v176\n\t" \
    "ds_read_b128 v[116:119], v176 offset:1024\n\tds_read_b128 v[120:123], v176 offset:2048\n\tds_read_b128 v[124:127], v176 offset:3072\n\tds_read_b128 v[128:131], v176 offset:4096\n\t" \
    "ds_read_b128 v[132:135], v176 offset:5120\n\ts_mov_b32 s64, %[qb]\n\ts_cmp_eq_u32 %[qb], 0\n\ts_cbranch_scc1 5f\n\tv_add_u32 v177, 8192, %[ka]\n\ts_waitcnt lgkmcnt(5)\n\t" \
    "v_mfma_f32_16x16x32_bf16 v[16:19], v[112:115], a[128:131], 0\n\tds_read_b128 v[136:139], v176 offset:6144\n\tv_mfma_f32_16x16x32_bf16 v[24:27], v[112:115], a[144:147], 0\n\t" \
    "v_mfma_f32_16x16x32_bf16 v[32:35], v[112:115], a[160:163], 0\n\tv_mfma_f32_16x16x32_bf16 v[40:43], v[112:115], a[176:179], 0\n\ts_waitcnt lgkmcnt(5)\n\t" \
    "v_mfma_f32_16x16x32_bf16 v[44:47], v[116:119], a[176:179], 0\n\tds_read_b128 v[140:143], v176 offset:7168\n\tv_mfma_f32_16x16x32_bf16 v[36:39], v[116:119], a[160:163], 0\n\t" \
    "v_mfma_f32_16x16x32_bf16 v[28:31], v[116:119], a[144:147], 0\n\tv_mfma_f32_16x16x32_bf16 v[20:23], v[116:119], a[128:131], 0\n\ts_waitcnt lgkmcnt(5)\n\t" \
    "v_mfma_f32_16x16x32_bf16 v[16:19], v[120:123], a[132:135], v[16:19]\n\tds_read_b128 v[112:115], v176 offset:8192\n\tv_mfma_f32_16x16x32_bf16 v[24:27], v[120:123], a[148:151], v[24:27]\n\t" \
    "v_mfma_f32_16x16x32_bf16 v[32:35], v[120:123], a[164:167], v[32:35]\n\tv_mfma_f32_16x16x32_bf16 v[40:43], v[120:123], a[180:183], v[40:43]\n\ts_waitcnt lgkmcnt(5)\n\t" \
    "v_mfma_f32_16x16x32_bf16 v[44:47], v[124:127], a[180:183], v[44:47]\n\tds_read_b128 v[116:119], v176 offset:9216\n\tv_mfma_f32_16x16x32_bf16 v[36:39], v[124:127], a[164:167], v[36:39]\n\t" \
    "v_mfma_f32_16x16x32_bf16 v[28:31], v[124:127], a[148:151], v[28:31]\n\tv_mfma_f32_16x16x32_bf16 v[20:23], v[124:127], a[132:135], v[20:23]\n\ts_waitcnt lgkmcnt(5)\n\t" \
    "v_mfma_f32_16x16x32_bf16 v[16:19], v[128:131], a[136:139], v[16:19]\n\tds_read_b128 v[120:123], v176 offset:10240\n\tv_mfma_f32_16x16x32_bf16 v[24:27], v[128:131], a[152:155], v[24:27]\n\t" \
    "v_mfma_f32_16x16x32_bf16 v[32:35], v[128:131], a[168:171], v[32:35]\n\tv_mfma_f32_16x16x32_bf16 v[40:43], v[128:131], a[184:187], v[40:43]\n\ts_waitcnt lgkmcnt(5)\n\t" \
    "v_mfma_f32_16x16x32_bf16 v[44:47], v[132:135], a[184:187], v[44:47]\n\tds_read_b128 v[124:127], v176 offset:11264\n\tv_mfma_f32_16x16x32_bf16 v[36:39], v[132:135], a[168:171], v[36:39]\n\t" \
    "v_mfma_f32_16x16x32_bf16 v[28:31], v[132:135], a[152:155], v[28:31]\n\tv_mfma_f32_16x16x32_bf16 v[20:23], v[132:135], a[136:139], v[20:23]\n\ts_waitcnt lgkmcnt(5)\n\t" \
    "v_mfma_f32_16x16x32_bf16 v[16:19], v[136:139], a[140:143], v[16:19]\n\tds_read_b128 v[128:131], v176 offset:12288\n\tv_mfma_f32_16x16x32_bf16 v[24:27], v[136:139], a[156:159], v[24:27]\n\t" \
    "v_mfma_f32_16x16x32_bf16 v[32:35], v[136:139], a[172:175], v[32:35]\n\tv_mfma_f32_16x16x32_bf16 v[40:43], v[136:139], a[188:191], v[40:43]\n\ts_waitcnt lgkmcnt(5)\n\t" \
    "v_mfma_f32_16x16x32_bf16 v[44:47], v[140:143], a[188:191], v[44:47]\n\tds_read_b128 v[132:135], v176 offset:13312\n\tv_mfma_f32_16x16x32_bf16 v[36:39], v[140:143], a[172:175], v[36:39]\n\t" \
    "v_mfma_f32_16x16x32_bf16 v[28:31], v[140:143], a[156:159], v[28:31]\n\tv_mfma_f32_16x16x32_bf16 v[20:23], v[140:143], a[140:143], v[20:23]\n\ts_waitcnt lgkmcnt(5)\n\t1:\n" \
    "v_mfma_f32_16x16x32_bf16 v[48:51], v[112:115], a[128:131], 0\n\ts_waitcnt vmcnt(8)\n\ts_barrier\n\tds_read_b128 v[136:139], v176 offset:14336\n\tv_add_f32 v155, v155, v151\n\t" \
    "v_mfma_f32_16x16x32_bf16 v[56:59], v[112:115], a[144:147], 0\n\tv_exp_f32 v144, v16\n\tv_mfma_f32_16x16x32_bf16 v[64:67], v[112:115], a[160:163], 0\n\tv_cvt_pk_bf16_f32 v111, v150, v151\n\t" \
    "v_add_f32 v152, v152, v144\n\tv_mfma_f32_16x16x32_bf16 v[72:75], v[112:115], a[176:179], 0\n\ts_waitcnt lgkmcnt(5)\n\tv_exp_f32 v145, v17\n\t" \
    "v_mfma_f32_16x16x32_bf16 v[76:79], v[116:119], a[176:179], 0\n\tds_read_b128 v[140:143], v176 offset:15360\n\tv_add_f32 v152, v152, v145\n\t" \
    "v_mfma_f32_16x16x32_bf16 v[68:71], v[116:119], a[160:163], 0\n\tv_exp_f32 v146, v18\n\tv_mfma_f32_16x16x32_bf16 v[60:63], v[116:119], a[144:147], 0\n\tv_cvt_pk_bf16_f32 v80, v144, v145\n\t" \
    "v_add_f32 v152, v152, v146\n\tv_mfma_f32_16x16x32_bf16 v[52:55], v[116:119], a[128:131], 0\n\ts_waitcnt lgkmcnt(5)\n\tv_exp_f32 v147, v19\n\t" \
    "v_mfma_f32_16x16x32_bf16 v[48:51], v[120:123], a[132:135], v[48:51]\n\tds_read_b128 v[112:115], v177 offset:57344\n\tv_add_f32 v152, v152, v147\n\t" \
    "v_mfma_f32_16x16x32_bf16 v[56:59], v[120:123], a[148:151], v[56:59]\n\ts_add_u32 m0, %[lb], 49152\n\tv_exp_f32 v148, v20\n\tv_mfma_f32_16x16x32_bf16 v[64:67], v[120:123], a[164:167], v[64:67]\n\t" \
    "v_cvt_pk_bf16_f32 v81, v146, v147\n\tv_add_f32 v152, v152, v148\n\tv_mfma_f32_16x16x32_bf16 v[72:75], v[120:123], a[180:183], v[72:75]\n\ts_waitcnt lgkmcnt(5)\n\tv_exp_f32 v149, v21\n\t" \
    "v_mfma_f32_16x16x32_bf16 v[76:79], v[124:127], a[180:183], v[76:79]\n\tds_read_b128 v[116:119], v177 offset:58368\n\tbuffer_load_dwordx4 %[dv], %[rs], s62 offen sc1 lds\n\t" \
    "v_add_f32 v152, v152, v149\n\tv_mfma_f32_16x16x32_bf16 v[68:71], v[124:127], a[164:167], v[68:71]\n\tv_exp_f32 v150, v22\n\tv_mfma_f32_16x16x32_bf16 v[60:63], v[124:127], a[148:151], v[60:63]\n\t" \
    "v_cvt_pk_bf16_f32 v82, v148, v149\n\tv_add_f32 v152, v152, v150\n\tv_mfma_f32_16x16x32_bf16 v[52:55], v[124:127], a[132:135], v[52:55]\n\ts_waitcnt lgkmcnt(5)\n\tv_exp_f32 v151, v23\n\t" \
    "v_mfma_f32_16x16x32_bf16 v[48:51], v[128:131], a[136:139], v[48:51]\n\tds_read_b128 v[120:123], v177 offset:59392\n\tbuffer_load_dwordx4 %[dv], %[rs], s62 offen offset:1024 sc1 lds\n\t" \
    "v_add_f32 v152, v152, v151\n\tv_mfma_f32_16x16x32_bf16 v[56:59], v[128:131], a[152:155], v[56:59]\n\ts_add_u32 m0, %[lb], 90112\n\tv_exp_f32 v144, v24\n\t" \
    "v_mfma_f32_16x16x32_bf16 v[64:67], v[128:131], a[168:171], v[64:67]\n\ts_add_u32 s62, s62, 0x40000\n\tv_cvt_pk_bf16_f32 v83, v150, v151\n\tv_add_f32 v153, v153, v144\n\t" \
    "v_mfma_f32_16x16x32_bf16 v[72:75], v[128:131], a[184:187], v[72:75]\n\ts_waitcnt lgkmcnt(5)\n\tv_exp_f32 v145, v25\n\tv_mfma_f32_16x16x32_bf16 v[76:79], v[132:135], a[184:187], v[76:79]\n\t" \
    "ds_read_b128 v[124:127], v177 offset:60416\n\tbuffer_load_dwordx4 %[dv], %[rs], s63 offen sc1 lds\n\tv_add_f32 v153, v153, v145\n\t" \
    "v_mfma_f32_16x16x32_bf16 v[68:71], v[132:135], a[168:171], v[68:71]\n\tv_exp_f32 v146, v26\n\tv_mfma_f32_16x16x32_bf16 v[60:63], v[132:135], a[152:155], v[60:63]\n\t" \
    "v_cvt_pk_bf16_f32 v84, v144, v145\n\tv_add_f32 v153, v153, v146\n\tv_mfma_f32_16x16x32_bf16 v[52:55], v[132:135], a[136:139], v[52:55]\n\ts_waitcnt lgkmcnt(5)\n\tv_exp_f32 v147, v27\n\t" \
    "v_mfma_f32_16x16x32_bf16 v[48:51], v[136:139], a[140:143], v[48:51]\n\tds_read_b128 v[128:131], v177 offset:61440\n\tbuffer_load_dwordx4 %[dv], %[rs], s63 offen offset:1024 sc1 lds\n\t" \
    "v_add_f32 v153, v153, v147\n\tv_mfma_f32_16x16x32_bf16 v[56:59], v[136:139], a[156:159], v[56:59]\n\tv_exp_f32 v148, v28\n\tv_mfma_f32_16x16x32_bf16 v[64:67], v[136:139], a[172:175], v[64:67]\n\t" \
    "s_add_u32 s63, s63, 0x40000\n\tv_cvt_pk_bf16_f32 v85, v146, v147\n\tv_add_f32 v153, v153, v148\n\tv_mfma_f32_16x16x32_bf16 v[72:75], v[136:139], a[188:191], v[72:75]\n\ts_waitcnt lgkmcnt(5)\n\t" \
    "v_exp_f32 v149, v29\n\tv_mfma_f32_16x16x32_bf16 v[76:79], v[140:143], a[188:191], v[76:79]\n\tds_read_b128 v[132:135], v177 offset:62464\n\tv_add_f32 v153, v153, v149\n\t" \
    "v_mfma_f32_16x16x32_bf16 v[68:71], v[140:143], a[172:175], v[68:71]\n\tv_exp_f32 v150, v30\n\tv_mfma_f32_16x16x32_bf16 v[60:63], v[140:143], a[156:159], v[60:63]\n\t" \
    "v_cvt_pk_bf16_f32 v86, v148, v149\n\tv_add_f32 v153, v153, v150\n\tv_mfma_f32_16x16x32_bf16 v[52:55], v[140:143], a[140:143], v[52:55]\n\ts_waitcnt lgkmcnt(5)\n\tv_exp_f32 v151, v31\n\t" \
    "v_mfma_f32_16x16x32_bf16 a[0:3], v[112:115], v[96:99], a[0:3]\n\tds_read_b128 v[136:139], v177 offset:63488\n\tv_add_f32 v153, v153, v151\n\t" \
    "v_mfma_f32_16x16x32_bf16 a[32:35], v[112:115], v[100:103], a[32:35]\n\tv_exp_f32 v144, v32\n\tv_mfma_f32_16x16x32_bf16 a[64:67], v[112:115], v[104:107], a[64:67]\n\t" \
    "v_cvt_pk_bf16_f32 v87, v150, v151\n\tv_add_f32 v154, v154, v144\n\tv_mfma_f32_16x16x32_bf16 a[96:99], v[112:115], v[108:111], a[96:99]\n\ts_waitcnt lgkmcnt(5)\n\tv_exp_f32 v145, v33\n\t" \
    "v_mfma_f32_16x16x32_bf16 a[100:103], v[116:119], v[108:111], a[100:103]\n\tds_read_b128 v[140:143], v177 offset:64512\n\tv_add_f32 v154, v154, v145\n\t" \
    "v_mfma_f32_16x16x32_bf16 a[68:71], v[116:119], v[104:107], a[68:71]\n\tv_exp_f32 v146, v34\n\tv_mfma_f32_16x16x32_bf16 a[36:39], v[116:119], v[100:103], a[36:39]\n\t" \
    "v_cvt_pk_bf16_f32 v88, v144, v145\n\tv_add_f32 v154, v154, v146\n\tv_mfma_f32_16x16x32_bf16 a[4:7], v[116:119], v[96:99], a[4:7]\n\ts_waitcnt lgkmcnt(5)\n\tv_exp_f32 v147, v35\n\t" \
    "v_mfma_f32_16x16x32_bf16 a[8:11], v[120:123], v[96:99], a[8:11]\n\tds_read_b128 v[112:115], v176 offset:16384\n\tv_add_f32 v154, v154, v147\n\t" \
    "v_mfma_f32_16x16x32_bf16 a[40:43], v[120:123], v[100:103], a[40:43]\n\tv_exp_f32 v148, v36\n\tv_mfma_f32_16x16x32_bf16 a[72:75], v[120:123], v[104:107], a[72:75]\n\t" \
    "v_cvt_pk_bf16_f32 v89, v146, v147\n\tv_add_f32 v154, v154, v148\n\tv_mfma_f32_16x16x32_bf16 a[104:107], v[120:123], v[108:111], a[104:107]\n\ts_waitcnt lgkmcnt(5)\n\tv_exp_f32 v149, v37\n\t" \
    "v_mfma_f32_16x16x32_bf16 a[108:111], v[124:127], v[108:111], a[108:111]\n\tds_read_b128 v[116:119], v176 offset:17408\n\tv_add_f32 v154, v154, v149\n\t" \
    "v_mfma_f32_16x16x32_bf16 a[76:79], v[124:127], v[104:107], a[76:79]\n\tv_exp_f32 v150, v38\n\tv_mfma_f32_16x16x32_bf16 a[44:47], v[124:127], v[100:103], a[44:47]\n\t" \
    "v_cvt_pk_bf16_f32 v90, v148, v149\n\tv_add_f32 v154, v154, v150\n\tv_mfma_f32_16x16x32_bf16 a[12:15], v[124:127], v[96:99], a[12:15]\n\ts_waitcnt lgkmcnt(5)\n\tv_exp_f32 v151, v39\n\t" \
    "v_mfma_f32_16x16x32_bf16 a[16:19], v[128:131], v[96:99], a[16:19]\n\tds_read_b128 v[120:123], v176 offset:18432\n\tv_add_f32 v154, v154, v151\n\t" \
    "v_mfma_f32_16x16x32_bf16 a[48:51], v[128:131], v[100:103], a[48:51]\n\tv_exp_f32 v144, v40\n\tv_mfma_f32_16x16x32_bf16 a[80:83], v[128:131], v[104:107], a[80:83]\n\t" \
    "v_cvt_pk_bf16_f32 v91, v150, v151\n\tv_add_f32 v155, v155, v144\n\tv_mfma_f32_16x16x32_bf16 a[112:115], v[128:131], v[108:111], a[112:115]\n\ts_waitcnt lgkmcnt(5)\n\tv_exp_f32 v145, v41\n\t" \
    "v_mfma_f32_16x16x32_bf16 a[116:119], v[132:135], v[108:111], a[116:119]\n\tds_read_b128 v[124:127], v176 offset:19456\n\tv_add_f32 v155, v155, v145\n\t" \
    "v_mfma_f32_16x16x32_bf16 a[84:87], v[132:135], v[104:107], a[84:87]\n\tv_exp_f32 v146, v42\n\tv_mfma_f32_16x16x32_bf16 a[52:55], v[132:135], v[100:103], a[52:55]\n\t" \
    "v_cvt_pk_bf16_f32 v92, v144, v145\n\tv_add_f32 v155, v155, v146\n\tv_mfma_f32_16x16x32_bf16 a[20:23], v[132:135], v[96:99], a[20:23]\n\ts_waitcnt lgkmcnt(5)\n\tv_exp_f32 v147, v43\n\t" \
    "v_mfma_f32_16x16x32_bf16 a[24:27], v[136:139], v[96:99], a[24:27]\n\tds_read_b128 v[128:131], v176 offset:20480\n\tv_add_f32 v155, v155, v147\n\t" \
    "v_mfma_f32_16x16x32_bf16 a[56:59], v[136:139], v[100:103], a[56:59]\n\tv_exp_f32 v148, v44\n\tv_mfma_f32_16x16x32_bf16 a[88:91], v[136:139], v[104:107], a[88:91]\n\t" \
    "v_cvt_pk_bf16_f32 v93, v146, v147\n\tv_add_f32 v155, v155, v148\n\tv_mfma_f32_16x16x32_bf16 a[120:123], v[136:139], v[108:111], a[120:123]\n\ts_waitcnt lgkmcnt(5)\n\tv_exp_f32 v149, v45\n\t" \
    "v_mfma_f32_16x16x32_bf16 a[124:127], v[140:143], v[108:111], a[124:127]\n\tds_read_b128 v[132:135], v176 offset:21504\n\tv_add_f32 v155, v155, v149\n\t" \
    "v_mfma_f32_16x16x32_bf16 a[92:95], v[140:143], v[104:107], a[92:95]\n\tv_exp_f32 v150, v46\n\tv_mfma_f32_16x16x32_bf16 a[60:63], v[140:143], v[100:103], a[60:63]\n\t" \
    "v_cvt_pk_bf16_f32 v94, v148, v149\n\tv_add_f32 v155, v155, v150\n\tv_mfma_f32_16x16x32_bf16 a[28:31], v[140:143], v[96:99], a[28:31]\n\ts_waitcnt lgkmcnt(5)\n\tv_exp_f32 v151, v47\n\t" \
    "v_add_u32 v177, 0x10000, %[ka]\n\tv_mfma_f32_16x16x32_bf16 v[16:19], v[112:115], a[128:131], 0\n\tds_read_b128 v[136:139], v176 offset:22528\n\tv_add_f32 v155, v155, v151\n\t" \
    "v_mfma_f32_16x16x32_bf16 v[24:27], v[112:115], a[144:147], 0\n\tv_exp_f32 v144, v48\n\tv_mfma_f32_16x16x32_bf16 v[32:35], v[112:115], a[160:163], 0\n\tv_cvt_pk_bf16_f32 v95, v150, v151\n\t" \
    "v_add_f32 v152, v152, v144\n\tv_mfma_f32_16x16x32_bf16 v[40:43], v[112:115], a[176:179], 0\n\ts_waitcnt lgkmcnt(5)\n\tv_exp_f32 v145, v49\n\t" \
    "v_mfma_f32_16x16x32_bf16 v[44:47], v[116:119], a[176:179], 0\n\tds_read_b128 v[140:143], v176 offset:23552\n\tv_add_f32 v152, v152, v145\n\t" \
    "v_mfma_f32_16x16x32_bf16 v[36:39], v[116:119], a[160:163], 0\n\tv_exp_f32 v146, v50\n\tv_mfma_f32_16x16x32_bf16 v[28:31], v[116:119], a[144:147], 0\n\tv_cvt_pk_bf16_f32 v96, v144, v145\n\t" \
    "v_add_f32 v152, v152, v146\n\tv_mfma_f32_16x16x32_bf16 v[20:23], v[116:119], a[128:131], 0\n\ts_waitcnt lgkmcnt(5)\n\tv_exp_f32 v147, v51\n\t" \
    "v_mfma_f32_16x16x32_bf16 v[16:19], v[120:123], a[132:135], v[16:19]\n\tds_read_b128 v[112:115], v177\n\tv_add_f32 v152, v152, v147\n\t" \
    "v_mfma_f32_16x16x32_bf16 v[24:27], v[120:123], a[148:151], v[24:27]\n\ts_add_u32 m0, %[lb], 57344\n\tv_exp_f32 v148, v52\n\tv_mfma_f32_16x16x32_bf16 v[32:35], v[120:123], a[164:167], v[32:35]\n\t" \
    "v_cvt_pk_bf16_f32 v97, v146, v147\n\tv_add_f32 v152, v152, v148\n\tv_mfma_f32_16x16x32_bf16 v[40:43], v[120:123], a[180:183], v[40:43]\n\ts_waitcnt lgkmcnt(5)\n\tv_exp_f32 v149, v53\n\t" \
    "v_mfma_f32_16x16x32_bf16 v[44:47], v[124:127], a[180:183], v[44:47]\n\tds_read_b128 v[116:119], v177 offset:1024\n\tbuffer_load_dwordx4 %[dv], %[rs], s62 offen sc1 lds\n\t" \
    "v_add_f32 v152, v152, v149\n\tv_mfma_f32_16x16x32_bf16 v[36:39], v[124:127], a[164:167], v[36:39]\n\tv_exp_f32 v150, v54\n\tv_mfma_f32_16x16x32_bf16 v[28:31], v[124:127], a[148:151], v[28:31]\n\t" \
    "v_cvt_pk_bf16_f32 v98, v148, v149\n\tv_add_f32 v152, v152, v150\n\tv_mfma_f32_16x16x32_bf16 v[20:23], v[124:127], a[132:135], v[20:23]\n\ts_waitcnt lgkmcnt(5)\n\tv_exp_f32 v151, v55\n\t" \
    "v_mfma_f32_16x16x32_bf16 v[16:19], v[128:131], a[136:139], v[16:19]\n\tds_read_b128 v[120:123], v177 offset:2048\n\tbuffer_load_dwordx4 %[dv], %[rs], s62 offen offset:1024 sc1 lds\n\t" \
    "v_add_f32 v152, v152, v151\n\tv_mfma_f32_16x16x32_bf16 v[24:27], v[128:131], a[152:155], v[24:27]\n\ts_add_u32 m0, %[lb], 98304\n\tv_exp_f32 v144, v56\n\t" \
    "v_mfma_f32_16x16x32_bf16 v[32:35], v[128:131], a[168:171], v[32:35]\n\ts_add_u32 s62, s62, 0x40000\n\tv_cvt_pk_bf16_f32 v99, v150, v151\n\tv_add_f32 v153, v153, v144\n\t" \
    "v_mfma_f32_16x16x32_bf16 v[40:43], v[128:131], a[184:187], v[40:43]\n\ts_waitcnt lgkmcnt(5)\n\tv_exp_f32 v145, v57\n\tv_mfma_f32_16x16x32_bf16 v[44:47], v[132:135], a[184:187], v[44:47]\n\t" \
    "ds_read_b128 v[124:127], v177 offset:3072\n\tbuffer_load_dwordx4 %[dv], %[rs], s63 offen sc1 lds\n\tv_add_f32 v153, v153, v145\n\t" \
    "v_mfma_f32_16x16x32_bf16 v[36:39], v[132:135], a[168:171], v[36:39]\n\tv_exp_f32 v146, v58\n\tv_mfma_f32_16x16x32_bf16 v[28:31], v[132:135], a[152:155], v[28:31]\n\t" \
    "v_cvt_pk_bf16_f32 v100, v144, v145\n\tv_add_f32 v153, v153, v146\n\tv_mfma_f32_16x16x32_bf16 v[20:23], v[132:135], a[136:139], v[20:23]\n\ts_waitcnt lgkmcnt(5)\n\tv_exp_f32 v147, v59\n\t" \
    "v_mfma_f32_16x16x32_bf16 v[16:19], v[136:139], a[140:143], v[16:19]\n\tds_read_b128 v[128:131], v177 offset:4096\n\tbuffer_load_dwordx4 %[dv], %[rs], s63 offen offset:1024 sc1 lds\n\t" \
    "v_add_f32 v153, v153, v147\n\tv_mfma_f32_16x16x32_bf16 v[24:27], v[136:139], a[156:159], v[24:27]\n\tv_exp_f32 v148, v60\n\tv_mfma_f32_16x16x32_bf16 v[32:35], v[136:139], a[172:175], v[32:35]\n\t" \
    "s_add_u32 s63, s63, 0x40000\n\tv_cvt_pk_bf16_f32 v101, v146, v147\n\tv_add_f32 v153, v153, v148\n\tv_mfma_f32_16x16x32_bf16 v[40:43], v[136:139], a[188:191], v[40:43]\n\ts_waitcnt lgkmcnt(5)\n\t" \
    "v_exp_f32 v149, v61\n\tv_mfma_f32_16x16x32_bf16 v[44:47], v[140:143], a[188:191], v[44:47]\n\tds_read_b128 v[132:135], v177 offset:5120\n\tv_add_f32 v153, v153, v149\n\t" \
    "v_mfma_f32_16x16x32_bf16 v[36:39], v[140:143], a[172:175], v[36:39]\n\tv_exp_f32 v150, v62\n\tv_mfma_f32_16x16x32_bf16 v[28:31], v[140:143], a[156:159], v[28:31]\n\t" \
    "v_cvt_pk_bf16_f32 v102, v148, v149\n\tv_add_f32 v153, v153, v150\n\tv_mfma_f32_16x16x32_bf16 v[20:23], v[140:143], a[140:143], v[20:23]\n\ts_waitcnt lgkmcnt(5)\n\tv_exp_f32 v151, v63\n\t" \
    "v_mfma_f32_16x16x32_bf16 a[0:3], v[112:115], v[80:83], a[0:3]\n\tds_read_b128 v[136:139], v177 offset:6144\n\tv_add_f32 v153, v153, v151\n\t" \
    "v_mfma_f32_16x16x32_bf16 a[32:35], v[112:115], v[84:87], a[32:35]\n\tv_exp_f32 v144, v64\n\tv_mfma_f32_16x16x32_bf16 a[64:67], v[112:115], v[88:91], a[64:67]\n\tv_cvt_pk_bf16_f32 v103, v150, v151\n\t" \
    "v_add_f32 v154, v154, v144\n\tv_mfma_f32_16x16x32_bf16 a[96:99], v[112:115], v[92:95], a[96:99]\n\ts_waitcnt lgkmcnt(5)\n\tv_exp_f32 v145, v65\n\t" \
    "v_mfma_f32_16x16x32_bf16 a[100:103], v[116:119], v[92:95], a[100:103]\n\tds_read_b128 v[140:143], v177 offset:7168\n\tv_add_f32 v154, v154, v145\n\t" \
    "v_mfma_f32_16x16x32_bf16 a[68:71], v[116:119], v[88:91], a[68:71]\n\tv_exp_f32 v146, v66\n\tv_mfma_f32_16x16x32_bf16 a[36:39], v[116:119], v[84:87], a[36:39]\n\tv_cvt_pk_bf16_f32 v104, v144, v145\n\t" \
    "v_add_f32 v154, v154, v146\n\tv_mfma_f32_16x16x32_bf16 a[4:7], v[116:119], v[80:83], a[4:7]\n\ts_waitcnt lgkmcnt(5)\n\tv_exp_f32 v147, v67\n\t" \
    "v_mfma_f32_16x16x32_bf16 a[8:11], v[120:123], v[80:83], a[8:11]\n\tds_read_b128 v[112:115], v176 offset:24576\n\tv_add_f32 v154, v154, v147\n\t" \
    "v_mfma_f32_16x16x32_bf16 a[40:43], v[120:123], v[84:87], a[40:43]\n\tv_exp_f32 v148, v68\n\tv_mfma_f32_16x16x32_bf16 a[72:75], v[120:123], v[88:91], a[72:75]\n\tv_cvt_pk_bf16_f32 v105, v146, v147\n\t" \
    "v_add_f32 v154, v154, v148\n\tv_mfma_f32_16x16x32_bf16 a[104:107], v[120:123], v[92:95], a[104:107]\n\ts_waitcnt lgkmcnt(5)\n\tv_exp_f32 v149, v69\n\t" \
    "v_mfma_f32_16x16x32_bf16 a[108:111], v[124:127], v[92:95], a[108:111]\n\tds_read_b128 v[116:119], v176 offset:25600\n\tv_add_f32 v154, v154, v149\n\t" \
    "v_mfma_f32_16x16x32_bf16 a[76:79], v[124:127], v[88:91], a[76:79]\n\tv_exp_f32 v150, v70\n\tv_mfma_f32_16x16x32_bf16 a[44:47], v[124:127], v[84:87], a[44:47]\n\tv_cvt_pk_bf16_f32 v106, v148, v149\n\t" \
    "v_add_f32 v154, v154, v150\n\tv_mfma_f32_16x16x32_bf16 a[12:15], v[124:127], v[80:83], a[12:15]\n\ts_waitcnt lgkmcnt(5)\n\tv_exp_f32 v151, v71\n\t" \
    "v_mfma_f32_16x16x32_bf16 a[16:19], v[128:131], v[80:83], a[16:19]\n\tds_read_b128 v[120:123], v176 offset:26624\n\tv_add_f32 v154, v154, v151\n\t" \
    "v_mfma_f32_16x16x32_bf16 a[48:51], v[128:131], v[84:87], a[48:51]\n\tv_exp_f32 v144, v72\n\tv_mfma_f32_16x16x32_bf16 a[80:83], v[128:131], v[88:91], a[80:83]\n\tv_cvt_pk_bf16_f32 v107, v150, v151\n\t" \
    "v_add_f32 v155, v155, v144\n\tv_mfma_f32_16x16x32_bf16 a[112:115], v[128:131], v[92:95], a[112:115]\n\ts_waitcnt lgkmcnt(5)\n\tv_exp_f32 v145, v73\n\t" \
    "v_mfma_f32_16x16x32_bf16 a[116:119], v[132:135], v[92:95], a[116:119]\n\tds_read_b128 v[124:127], v176 offset:27648\n\tv_add_f32 v155, v155, v145\n\t" \
    "v_mfma_f32_16x16x32_bf16 a[84:87], v[132:135], v[88:91], a[84:87]\n\tv_exp_f32 v146, v74\n\tv_mfma_f32_16x16x32_bf16 a[52:55], v[132:135], v[84:87], a[52:55]\n\tv_cvt_pk_bf16_f32 v108, v144, v145\n\t" \
    "v_add_f32 v155, v155, v146\n\tv_mfma_f32_16x16x32_bf16 a[20:23], v[132:135], v[80:83], a[20:23]\n\ts_waitcnt lgkmcnt(5)\n\tv_exp_f32 v147, v75\n\t" \
    "v_mfma_f32_16x16x32_bf16 a[24:27], v[136:139], v[80:83], a[24:27]\n\tds_read_b128 v[128:131], v176 offset:28672\n\tv_add_f32 v155, v155, v147\n\t" \
    "v_mfma_f32_16x16x32_bf16 a[56:59], v[136:139], v[84:87], a[56:59]\n\tv_exp_f32 v148, v76\n\tv_mfma_f32_16x16x32_bf16 a[88:91], v[136:139], v[88:91], a[88:91]\n\tv_cvt_pk_bf16_f32 v109, v146, v147\n\t" \
    "v_add_f32 v155, v155, v148\n\tv_mfma_f32_16x16x32_bf16 a[120:123], v[136:139], v[92:95], a[120:123]\n\ts_waitcnt lgkmcnt(5)\n\tv_exp_f32 v149, v77\n\t" \
    "v_mfma_f32_16x16x32_bf16 a[124:127], v[140:143], v[92:95], a[124:127]\n\tds_read_b128 v[132:135], v176 offset:29696\n\tv_add_f32 v155, v155, v149\n\t" \
    "v_mfma_f32_16x16x32_bf16 a[92:95], v[140:143], v[88:91], a[92:95]\n\tv_exp_f32 v150, v78\n\tv_mfma_f32_16x16x32_bf16 a[60:63], v[140:143], v[84:87], a[60:63]\n\tv_cvt_pk_bf16_f32 v110, v148, v149\n\t" \
    "v_add_f32 v155, v155, v150\n\tv_mfma_f32_16x16x32_bf16 a[28:31], v[140:143], v[80:83], a[28:31]\n\ts_waitcnt lgkmcnt(5)\n\tv_exp_f32 v151, v79\n\t" \
    "v_mfma_f32_16x16x32_bf16 v[48:51], v[112:115], a[128:131], 0\n\ts_waitcnt vmcnt(8)\n\ts_barrier\n\tds_read_b128 v[136:139], v176 offset:30720\n\tv_add_f32 v155, v155, v151\n\t" \
    "v_mfma_f32_16x16x32_bf16 v[56:59], v[112:115], a[144:147], 0\n\tv_exp_f32 v144, v16\n\tv_mfma_f32_16x16x32_bf16 v[64:67], v[112:115], a[160:163], 0\n\tv_cvt_pk_bf16_f32 v111, v150, v151\n\t" \
    "v_add_f32 v152, v152, v144\n\tv_mfma_f32_16x16x32_bf16 v[72:75], v[112:115], a[176:179], 0\n\ts_waitcnt lgkmcnt(5)\n\tv_exp_f32 v145, v17\n\t" \
    "v_mfma_f32_16x16x32_bf16 v[76:79], v[116:119], a[176:179], 0\n\tds_read_b128 v[140:143], v176 offset:31744\n\tv_add_f32 v152, v152, v145\n\t" \
    "v_mfma_f32_16x16x32_bf16 v[68:71], v[116:119], a[160:163], 0\n\tv_exp_f32 v146, v18\n\tv_mfma_f32_16x16x32_bf16 v[60:63], v[116:119], a[144:147], 0\n\tv_cvt_pk_bf16_f32 v80, v144, v145\n\t" \
    "v_add_f32 v152, v152, v146\n\tv_mfma_f32_16x16x32_bf16 v[52:55], v[116:119], a[128:131], 0\n\ts_waitcnt lgkmcnt(5)\n\tv_exp_f32 v147, v19\n\t" \
    "v_mfma_f32_16x16x32_bf16 v[48:51], v[120:123], a[132:135], v[48:51]\n\tds_read_b128 v[112:115], v177 offset:8192\n\tv_add_f32 v152, v152, v147\n\t" \
    "v_mfma_f32_16x16x32_bf16 v[56:59], v[120:123], a[148:151], v[56:59]\n\ts_add_u32 m0, %[lb], 0\n\tv_exp_f32 v148, v20\n\tv_mfma_f32_16x16x32_bf16 v[64:67], v[120:123], a[164:167], v[64:67]\n\t" \
    "v_cvt_pk_bf16_f32 v81, v146, v147\n\tv_add_f32 v152, v152, v148\n\tv_mfma_f32_16x16x32_bf16 v[72:75], v[120:123], a[180:183], v[72:75]\n\ts_waitcnt lgkmcnt(5)\n\tv_exp_f32 v149, v21\n\t" \
    "v_mfma_f32_16x16x32_bf16 v[76:79], v[124:127], a[180:183], v[76:79]\n\tds_read_b128 v[116:119], v177 offset:9216\n\tbuffer_load_dwordx4 %[dv], %[rs], s62 offen sc1 lds\n\t" \
    "v_add_f32 v152, v152, v149\n\tv_mfma_f32_16x16x32_bf16 v[68:71], v[124:127], a[164:167], v[68:71]\n\tv_exp_f32 v150, v22\n\tv_mfma_f32_16x16x32_bf16 v[60:63], v[124:127], a[148:151], v[60:63]\n\t" \
    "v_cvt_pk_bf16_f32 v82, v148, v149\n\tv_add_f32 v152, v152, v150\n\tv_mfma_f32_16x16x32_bf16 v[52:55], v[124:127], a[132:135], v[52:55]\n\ts_waitcnt lgkmcnt(5)\n\tv_exp_f32 v151, v23\n\t" \
    "v_mfma_f32_16x16x32_bf16 v[48:51], v[128:131], a[136:139], v[48:51]\n\tds_read_b128 v[120:123], v177 offset:10240\n\tbuffer_load_dwordx4 %[dv], %[rs], s62 offen offset:1024 sc1 lds\n\t" \
    "v_add_f32 v152, v152, v151\n\tv_mfma_f32_16x16x32_bf16 v[56:59], v[128:131], a[152:155], v[56:59]\n\ts_add_u32 m0, %[lb], 106496\n\tv_exp_f32 v144, v24\n\t" \
    "v_mfma_f32_16x16x32_bf16 v[64:67], v[128:131], a[168:171], v[64:67]\n\ts_add_u32 s62, s62, 0x40000\n\tv_cvt_pk_bf16_f32 v83, v150, v151\n\tv_add_f32 v153, v153, v144\n\t" \
    "v_mfma_f32_16x16x32_bf16 v[72:75], v[128:131], a[184:187], v[72:75]\n\ts_waitcnt lgkmcnt(5)\n\tv_exp_f32 v145, v25\n\tv_mfma_f32_16x16x32_bf16 v[76:79], v[132:135], a[184:187], v[76:79]\n\t" \
    "ds_read_b128 v[124:127], v177 offset:11264\n\tbuffer_load_dwordx4 %[dv], %[rs], s63 offen sc1 lds\n\tv_add_f32 v153, v153, v145\n\t" \
    "v_mfma_f32_16x16x32_bf16 v[68:71], v[132:135], a[168:171], v[68:71]\n\tv_exp_f32 v146, v26\n\tv_mfma_f32_16x16x32_bf16 v[60:63], v[132:135], a[152:155], v[60:63]\n\t" \
    "v_cvt_pk_bf16_f32 v84, v144, v145\n\tv_add_f32 v153, v153, v146\n\tv_mfma_f32_16x16x32_bf16 v[52:55], v[132:135], a[136:139], v[52:55]\n\ts_waitcnt lgkmcnt(5)\n\tv_exp_f32 v147, v27\n\t" \
    "v_mfma_f32_16x16x32_bf16 v[48:51], v[136:139], a[140:143], v[48:51]\n\tds_read_b128 v[128:131], v177 offset:12288\n\tbuffer_load_dwordx4 %[dv], %[rs], s63 offen offset:1024 sc1 lds\n\t" \
    "v_add_f32 v153, v153, v147\n\tv_mfma_f32_16x16x32_bf16 v[56:59], v[136:139], a[156:159], v[56:59]\n\tv_exp_f32 v148, v28\n\tv_mfma_f32_16x16x32_bf16 v[64:67], v[136:139], a[172:175], v[64:67]\n\t" \
    "s_add_u32 s63, s63, 0x40000\n\tv_cvt_pk_bf16_f32 v85, v146, v147\n\tv_add_f32 v153, v153, v148\n\tv_mfma_f32_16x16x32_bf16 v[72:75], v[136:139], a[188:191], v[72:75]\n\ts_waitcnt lgkmcnt(5)\n\t" \
    "v_exp_f32 v149, v29\n\tv_mfma_f32_16x16x32_bf16 v[76:79], v[140:143], a[188:191], v[76:79]\n\tds_read_b128 v[132:135], v177 offset:13312\n\tv_add_f32 v153, v153, v149\n\t" \
    "v_mfma_f32_16x16x32_bf16 v[68:71], v[140:143], a[172:175], v[68:71]\n\tv_exp_f32 v150, v30\n\tv_mfma_f32_16x16x32_bf16 v[60:63], v[140:143], a[156:159], v[60:63]\n\t" \
    "v_cvt_pk_bf16_f32 v86, v148, v149\n\tv_add_f32 v153, v153, v150\n\tv_mfma_f32_16x16x32_bf16 v[52:55], v[140:143], a[140:143], v[52:55]\n\ts_waitcnt lgkmcnt(5)\n\tv_exp_f32 v151, v31\n\t" \
    "v_mfma_f32_16x16x32_bf16 a[0:3], v[112:115], v[96:99], a[0:3]\n\tds_read_b128 v[136:139], v177 offset:14336\n\tv_add_f32 v153, v153, v151\n\t" \
    "v_mfma_f32_16x16x32_bf16 a[32:35], v[112:115], v[100:103], a[32:35]\n\tv_exp_f32 v144, v32\n\tv_mfma_f32_16x16x32_bf16 a[64:67], v[112:115], v[104:107], a[64:67]\n\t" \
    "v_cvt_pk_bf16_f32 v87, v150, v151\n\tv_add_f32 v154, v154, v144\n\tv_mfma_f32_16x16x32_bf16 a[96:99], v[112:115], v[108:111], a[96:99]\n\ts_waitcnt lgkmcnt(5)\n\tv_exp_f32 v145, v33\n\t" \
    "v_mfma_f32_16x16x32_bf16 a[100:103], v[116:119], v[108:111], a[100:103]\n\tds_read_b128 v[140:143], v177 offset:15360\n\tv_add_f32 v154, v154, v145\n\t" \
    "v_mfma_f32_16x16x32_bf16 a[68:71], v[116:119], v[104:107], a[68:71]\n\tv_exp_f32 v146, v34\n\tv_mfma_f32_16x16x32_bf16 a[36:39], v[116:119], v[100:103], a[36:39]\n\t" \
    "v_cvt_pk_bf16_f32 v88, v144, v145\n\tv_add_f32 v154, v154, v146\n\tv_mfma_f32_16x16x32_bf16 a[4:7], v[116:119], v[96:99], a[4:7]\n\ts_waitcnt lgkmcnt(5)\n\tv_exp_f32 v147, v35\n\t" \
    "v_mfma_f32_16x16x32_bf16 a[8:11], v[120:123], v[96:99], a[8:11]\n\tds_read_b128 v[112:115], v176 offset:32768\n\tv_add_f32 v154, v154, v147\n\t" \
    "v_mfma_f32_16x16x32_bf16 a[40:43], v[120:123], v[100:103], a[40:43]\n\tv_exp_f32 v148, v36\n\tv_mfma_f32_16x16x32_bf16 a[72:75], v[120:123], v[104:107], a[72:75]\n\t" \
    "v_cvt_pk_bf16_f32 v89, v146, v147\n\tv_add_f32 v154, v154, v148\n\tv_mfma_f32_16x16x32_bf16 a[104:107], v[120:123], v[108:111], a[104:107]\n\ts_waitcnt lgkmcnt(5)\n\tv_exp_f32 v149, v37\n\t" \
    "v_mfma_f32_16x16x32_bf16 a[108:111], v[124:127], v[108:111], a[108:111]\n\tds_read_b128 v[116:119], v176 offset:33792\n\tv_add_f32 v154, v154, v149\n\t" \
    "v_mfma_f32_16x16x32_bf16 a[76:79], v[124:127], v[104:107], a[76:79]\n\tv_exp_f32 v150, v38\n\tv_mfma_f32_16x16x32_bf16 a[44:47], v[124:127], v[100:103], a[44:47]\n\t" \
    "v_cvt_pk_bf16_f32 v90, v148, v149\n\tv_add_f32 v154, v154, v150\n\tv_mfma_f32_16x16x32_bf16 a[12:15], v[124:127], v[96:99], a[12:15]\n\ts_waitcnt lgkmcnt(5)\n\tv_exp_f32 v151, v39\n\t" \
    "v_mfma_f32_16x16x32_bf16 a[16:19], v[128:131], v[96:99], a[16:19]\n\tds_read_b128 v[120:123], v176 offset:34816\n\tv_add_f32 v154, v154, v151\n\t" \
    "v_mfma_f32_16x16x32_bf16 a[48:51], v[128:131], v[100:103], a[48:51]\n\tv_exp_f32 v144, v40\n\tv_mfma_f32_16x16x32_bf16 a[80:83], v[128:131], v[104:107], a[80:83]\n\t" \
    "v_cvt_pk_bf16_f32 v91, v150, v151\n\tv_add_f32 v155, v155, v144\n\tv_mfma_f32_16x16x32_bf16 a[112:115], v[128:131], v[108:111], a[112:115]\n\ts_waitcnt lgkmcnt(5)\n\tv_exp_f32 v145, v41\n\t" \
    "v_mfma_f32_16x16x32_bf16 a[116:119], v[132:135], v[108:111], a[116:119]\n\tds_read_b128 v[124:127], v176 offset:35840\n\tv_add_f32 v155, v155, v145\n\t" \
    "v_mfma_f32_16x16x32_bf16 a[84:87], v[132:135], v[104:107], a[84:87]\n\tv_exp_f32 v146, v42\n\tv_mfma_f32_16x16x32_bf16 a[52:55], v[132:135], v[100:103], a[52:55]\n\t" \
    "v_cvt_pk_bf16_f32 v92, v144, v145\n\tv_add_f32 v155, v155, v146\n\tv_mfma_f32_16x16x32_bf16 a[20:23], v[132:135], v[96:99], a[20:23]\n\ts_waitcnt lgkmcnt(5)\n\tv_exp_f32 v147, v43\n\t" \
    "v_mfma_f32_16x16x32_bf16 a[24:27], v[136:139], v[96:99], a[24:27]\n\tds_read_b128 v[128:131], v176 offset:36864\n\tv_add_f32 v155, v155, v147\n\t" \
    "v_mfma_f32_16x16x32_bf16 a[56:59], v[136:139], v[100:103], a[56:59]\n\tv_exp_f32 v148, v44\n\tv_mfma_f32_16x16x32_bf16 a[88:91], v[136:139], v[104:107], a[88:91]\n\t" \
    "v_cvt_pk_bf16_f32 v93, v146, v147\n\tv_add_f32 v155, v155, v148\n\tv_mfma_f32_16x16x32_bf16 a[120:123], v[136:139], v[108:111], a[120:123]\n\ts_waitcnt lgkmcnt(5)\n\tv_exp_f32 v149, v45\n\t" \
    "v_mfma_f32_16x16x32_bf16 a[124:127], v[140:143], v[108:111], a[124:127]\n\tds_read_b128 v[132:135], v176 offset:37888\n\tv_add_f32 v155, v155, v149\n\t" \
    "v_mfma_f32_16x16x32_bf16 a[92:95], v[140:143], v[104:107], a[92:95]\n\tv_exp_f32 v150, v46\n\tv_mfma_f32_16x16x32_bf16 a[60:63], v[140:143], v[100:103], a[60:63]\n\t" \
    "v_cvt_pk_bf16_f32 v94, v148, v149\n\tv_add_f32 v155, v155, v150\n\tv_mfma_f32_16x16x32_bf16 a[28:31], v[140:143], v[96:99], a[28:31]\n\ts_waitcnt lgkmcnt(5)\n\tv_exp_f32 v151, v47\n\t" \
    "v_mfma_f32_16x16x32_bf16 v[16:19], v[112:115], a[128:131], 0\n\tds_read_b128 v[136:139], v176 offset:38912\n\tv_add_f32 v155, v155, v151\n\t" \
    "v_mfma_f32_16x16x32_bf16 v[24:27], v[112:115], a[144:147], 0\n\tv_exp_f32 v144, v48\n\tv_mfma_f32_16x16x32_bf16 v[32:35], v[112:115], a[160:163], 0\n\tv_cvt_pk_bf16_f32 v95, v150, v151\n\t" \
    "v_add_f32 v152, v152, v144\n\tv_mfma_f32_16x16x32_bf16 v[40:43], v[112:115], a[176:179], 0\n\ts_waitcnt lgkmcnt(5)\n\tv_exp_f32 v145, v49\n\t" \
    "v_mfma_f32_16x16x32_bf16 v[44:47], v[116:119], a[176:179], 0\n\tds_read_b128 v[140:143], v176 offset:39936\n\tv_add_f32 v152, v152, v145\n\t" \
    "v_mfma_f32_16x16x32_bf16 v[36:39], v[116:119], a[160:163], 0\n\tv_exp_f32 v146, v50\n\tv_mfma_f32_16x16x32_bf16 v[28:31], v[116:119], a[144:147], 0\n\tv_cvt_pk_bf16_f32 v96, v144, v145\n\t" \
    "v_add_f32 v152, v152, v146\n\tv_mfma_f32_16x16x32_bf16 v[20:23], v[116:119], a[128:131], 0\n\ts_waitcnt lgkmcnt(5)\n\tv_exp_f32 v147, v51\n\t" \
    "v_mfma_f32_16x16x32_bf16 v[16:19], v[120:123], a[132:135], v[16:19]\n\tds_read_b128 v[112:115], v177 offset:16384\n\tv_add_f32 v152, v152, v147\n\t" \
    "v_mfma_f32_16x16x32_bf16 v[24:27], v[120:123], a[148:151], v[24:27]\n\ts_add_u32 m0, %[lb], 8192\n\tv_exp_f32 v148, v52\n\tv_mfma_f32_16x16x32_bf16 v[32:35], v[120:123], a[164:167], v[32:35]\n\t" \
    "v_cvt_pk_bf16_f32 v97, v146, v147\n\tv_add_f32 v152, v152, v148\n\tv_mfma_f32_16x16x32_bf16 v[40:43], v[120:123], a[180:183], v[40:43]\n\ts_waitcnt lgkmcnt(5)\n\tv_exp_f32 v149, v53\n\t" \
    "v_mfma_f32_16x16x32_bf16 v[44:47], v[124:127], a[180:183], v[44:47]\n\tds_read_b128 v[116:119], v177 offset:17408\n\tbuffer_load_dwordx4 %[dv], %[rs], s62 offen sc1 lds\n\t" \
    "v_add_f32 v152, v152, v149\n\tv_mfma_f32_16x16x32_bf16 v[36:39], v[124:127], a[164:167], v[36:39]\n\tv_exp_f32 v150, v54\n\tv_mfma_f32_16x16x32_bf16 v[28:31], v[124:127], a[148:151], v[28:31]\n\t" \
    "v_cvt_pk_bf16_f32 v98, v148, v149\n\tv_add_f32 v152, v152, v150\n\tv_mfma_f32_16x16x32_bf16 v[20:23], v[124:127], a[132:135], v[20:23]\n\ts_waitcnt lgkmcnt(5)\n\tv_exp_f32 v151, v55\n\t" \
    "v_mfma_f32_16x16x32_bf16 v[16:19], v[128:131], a[136:139], v[16:19]\n\tds_read_b128 v[120:123], v177 offset:18432\n\tbuffer_load_dwordx4 %[dv], %[rs], s62 offen offset:1024 sc1 lds\n\t" \
    "v_add_f32 v152, v152, v151\n\tv_mfma_f32_16x16x32_bf16 v[24:27], v[128:131], a[152:155], v[24:27]\n\ts_add_u32 m0, %[lb], 114688\n\tv_exp_f32 v144, v56\n\t" \
    "v_mfma_f32_16x16x32_bf16 v[32:35], v[128:131], a[168:171], v[32:35]\n\ts_add_u32 s62, s62, 0x40000\n\tv_cvt_pk_bf16_f32 v99, v150, v151\n\tv_add_f32 v153, v153, v144\n\t" \
    "v_mfma_f32_16x16x32_bf16 v[40:43], v[128:131], a[184:187], v[40:43]\n\ts_waitcnt lgkmcnt(5)\n\tv_exp_f32 v145, v57\n\tv_mfma_f32_16x16x32_bf16 v[44:47], v[132:135], a[184:187], v[44:47]\n\t" \
    "ds_read_b128 v[124:127], v177 offset:19456\n\tbuffer_load_dwordx4 %[dv], %[rs], s63 offen sc1 lds\n\tv_add_f32 v153, v153, v145\n\t" \
    "v_mfma_f32_16x16x32_bf16 v[36:39], v[132:135], a[168:171], v[36:39]\n\tv_exp_f32 v146, v58\n\tv_mfma_f32_16x16x32_bf16 v[28:31], v[132:135], a[152:155], v[28:31]\n\t" \
    "v_cvt_pk_bf16_f32 v100, v144, v145\n\tv_add_f32 v153, v153, v146\n\tv_mfma_f32_16x16x32_bf16 v[20:23], v[132:135], a[136:139], v[20:23]\n\ts_waitcnt lgkmcnt(5)\n\tv_exp_f32 v147, v59\n\t" \
    "v_mfma_f32_16x16x32_bf16 v[16:19], v[136:139], a[140:143], v[16:19]\n\tds_read_b128 v[128:131], v177 offset:20480\n\tbuffer_load_dwordx4 %[dv], %[rs], s63 offen offset:1024 sc1 lds\n\t" \
    "v_add_f32 v153, v153, v147\n\tv_mfma_f32_16x16x32_bf16 v[24:27], v[136:139], a[156:159], v[24:27]\n\tv_exp_f32 v148, v60\n\tv_mfma_f32_16x16x32_bf16 v[32:35], v[136:139], a[172:175], v[32:35]\n\t" \
    "s_add_u32 s63, s63, 0x40000\n\tv_cvt_pk_bf16_f32 v101, v146, v147\n\tv_add_f32 v153, v153, v148\n\tv_mfma_f32_16x16x32_bf16 v[40:43], v[136:139], a[188:191], v[40:43]\n\ts_waitcnt lgkmcnt(5)\n\t" \
    "v_exp_f32 v149, v61\n\tv_mfma_f32_16x16x32_bf16 v[44:47], v[140:143], a[188:191], v[44:47]\n\tds_read_b128 v[132:135], v177 offset:21504\n\tv_add_f32 v153, v153, v149\n\t" \
    "v_mfma_f32_16x16x32_bf16 v[36:39], v[140:143], a[172:175], v[36:39]\n\tv_exp_f32 v150, v62\n\tv_mfma_f32_16x16x32_bf16 v[28:31], v[140:143], a[156:159], v[28:31]\n\t" \
    "v_cvt_pk_bf16_f32 v102, v148, v149\n\tv_add_f32 v153, v153, v150\n\tv_mfma_f32_16x16x32_bf16 v[20:23], v[140:143], a[140:143], v[20:23]\n\ts_waitcnt lgkmcnt(5)\n\tv_exp_f32 v151, v63\n\t" \
    "v_mfma_f32_16x16x32_bf16 a[0:3], v[112:115], v[80:83], a[0:3]\n\tds_read_b128 v[136:139], v177 offset:22528\n\tv_add_f32 v153, v153, v151\n\t" \
    "v_mfma_f32_16x16x32_bf16 a[32:35], v[112:115], v[84:87], a[32:35]\n\tv_exp_f32 v144, v64\n\tv_mfma_f32_16x16x32_bf16 a[64:67], v[112:115], v[88:91], a[64:67]\n\tv_cvt_pk_bf16_f32 v103, v150, v151\n\t" \
    "v_add_f32 v154, v154, v144\n\tv_mfma_f32_16x16x32_bf16 a[96:99], v[112:115], v[92:95], a[96:99]\n\ts_waitcnt lgkmcnt(5)\n\tv_exp_f32 v145, v65\n\t" \
    "v_mfma_f32_16x16x32_bf16 a[100:103], v[116:119], v[92:95], a[100:103]\n\tds_read_b128 v[140:143], v177 offset:23552\n\tv_add_f32 v154, v154, v145\n\t" \
    "v_mfma_f32_16x16x32_bf16 a[68:71], v[116:119], v[88:91], a[68:71]\n\tv_exp_f32 v146, v66\n\tv_mfma_f32_16x16x32_bf16 a[36:39], v[116:119], v[84:87], a[36:39]\n\tv_cvt_pk_bf16_f32 v104, v144, v145\n\t" \
    "v_add_f32 v154, v154, v146\n\tv_mfma_f32_16x16x32_bf16 a[4:7], v[116:119], v[80:83], a[4:7]\n\ts_waitcnt lgkmcnt(5)\n\tv_exp_f32 v147, v67\n\t" \
    "v_mfma_f32_16x16x32_bf16 a[8:11], v[120:123], v[80:83], a[8:11]\n\tds_read_b128 v[112:115], v176 offset:40960\n\tv_add_f32 v154, v154, v147\n\t" \
    "v_mfma_f32_16x16x32_bf16 a[40:43], v[120:123], v[84:87], a[40:43]\n\tv_exp_f32 v148, v68\n\tv_mfma_f32_16x16x32_bf16 a[72:75], v[120:123], v[88:91], a[72:75]\n\tv_cvt_pk_bf16_f32 v105, v146, v147\n\t" \
    "v_add_f32 v154, v154, v148\n\tv_mfma_f32_16x16x32_bf16 a[104:107], v[120:123], v[92:95], a[104:107]\n\ts_waitcnt lgkmcnt(5)\n\tv_exp_f32 v149, v69\n\t" \
    "v_mfma_f32_16x16x32_bf16 a[108:111], v[124:127], v[92:95], a[108:111]\n\tds_read_b128 v[116:119], v176 offset:41984\n\tv_add_f32 v154, v154, v149\n\t" \
    "v_mfma_f32_16x16x32_bf16 a[76:79], v[124:127], v[88:91], a[76:79]\n\tv_exp_f32 v150, v70\n\tv_mfma_f32_16x16x32_bf16 a[44:47], v[124:127], v[84:87], a[44:47]\n\tv_cvt_pk_bf16_f32 v106, v148, v149\n\t" \
    "v_add_f32 v154, v154, v150\n\tv_mfma_f32_16x16x32_bf16 a[12:15], v[124:127], v[80:83], a[12:15]\n\ts_waitcnt lgkmcnt(5)\n\tv_exp_f32 v151, v71\n\t" \
    "v_mfma_f32_16x16x32_bf16 a[16:19], v[128:131], v[80:83], a[16:19]\n\tds_read_b128 v[120:123], v176 offset:43008\n\tv_add_f32 v154, v154, v151\n\t" \
    "v_mfma_f32_16x16x32_bf16 a[48:51], v[128:131], v[84:87], a[48:51]\n\tv_exp_f32 v144, v72\n\tv_mfma_f32_16x16x32_bf16 a[80:83], v[128:131], v[88:91], a[80:83]\n\tv_cvt_pk_bf16_f32 v107, v150, v151\n\t" \
    "v_add_f32 v155, v155, v144\n\tv_mfma_f32_16x16x32_bf16 a[112:115], v[128:131], v[92:95], a[112:115]\n\ts_waitcnt lgkmcnt(5)\n\tv_exp_f32 v145, v73\n\t" \
    "v_mfma_f32_16x16x32_bf16 a[116:119], v[132:135], v[92:95], a[116:119]\n\tds_read_b128 v[124:127], v176 offset:44032\n\tv_add_f32 v155, v155, v145\n\t" \
    "v_mfma_f32_16x16x32_bf16 a[84:87], v[132:135], v[88:91], a[84:87]\n\tv_exp_f32 v146, v74\n\tv_mfma_f32_16x16x32_bf16 a[52:55], v[132:135], v[84:87], a[52:55]\n\tv_cvt_pk_bf16_f32 v108, v144, v145\n\t" \
    "v_add_f32 v155, v155, v146\n\tv_mfma_f32_16x16x32_bf16 a[20:23], v[132:135], v[80:83], a[20:23]\n\ts_waitcnt lgkmcnt(5)\n\tv_exp_f32 v147, v75\n\t" \
    "v_mfma_f32_16x16x32_bf16 a[24:27], v[136:139], v[80:83], a[24:27]\n\tds_read_b128 v[128:131], v176 offset:45056\n\tv_add_f32 v155, v155, v147\n\t" \
    "v_mfma_f32_16x16x32_bf16 a[56:59], v[136:139], v[84:87], a[56:59]\n\tv_exp_f32 v148, v76\n\tv_mfma_f32_16x16x32_bf16 a[88:91], v[136:139], v[88:91], a[88:91]\n\tv_cvt_pk_bf16_f32 v109, v146, v147\n\t" \
    "v_add_f32 v155, v155, v148\n\tv_mfma_f32_16x16x32_bf16 a[120:123], v[136:139], v[92:95], a[120:123]\n\ts_waitcnt lgkmcnt(5)\n\tv_exp_f32 v149, v77\n\t" \
    "v_mfma_f32_16x16x32_bf16 a[124:127], v[140:143], v[92:95], a[124:127]\n\tds_read_b128 v[132:135], v176 offset:46080\n\tv_add_f32 v155, v155, v149\n\t" \
    "v_mfma_f32_16x16x32_bf16 a[92:95], v[140:143], v[88:91], a[92:95]\n\tv_exp_f32 v150, v78\n\tv_mfma_f32_16x16x32_bf16 a[60:63], v[140:143], v[84:87], a[60:63]\n\tv_cvt_pk_bf16_f32 v110, v148, v149\n\t" \
    "v_add_f32 v155, v155, v150\n\tv_mfma_f32_16x16x32_bf16 a[28:31], v[140:143], v[80:83], a[28:31]\n\ts_waitcnt lgkmcnt(5)\n\tv_exp_f32 v151, v79\n\t" \
    "v_mfma_f32_16x16x32_bf16 v[48:51], v[112:115], a[128:131], 0\n\ts_waitcnt vmcnt(8)\n\ts_barrier\n\tds_read_b128 v[136:139], v176 offset:47104\n\tv_add_f32 v155, v155, v151\n\t" \
    "v_mfma_f32_16x16x32_bf16 v[56:59], v[112:115], a[144:147], 0\n\tv_exp_f32 v144, v16\n\tv_mfma_f32_16x16x32_bf16 v[64:67], v[112:115], a[160:163], 0\n\tv_cvt_pk_bf16_f32 v111, v150, v151\n\t" \
    "v_add_f32 v152, v152, v144\n\tv_mfma_f32_16x16x32_bf16 v[72:75], v[112:115], a[176:179], 0\n\ts_waitcnt lgkmcnt(5)\n\tv_exp_f32 v145, v17\n\t" \
    "v_mfma_f32_16x16x32_bf16 v[76:79], v[116:119], a[176:179], 0\n\tds_read_b128 v[140:143], v176 offset:48128\n\tv_add_f32 v152, v152, v145\n\t" \
    "v_mfma_f32_16x16x32_bf16 v[68:71], v[116:119], a[160:163], 0\n\tv_exp_f32 v146, v18\n\tv_mfma_f32_16x16x32_bf16 v[60:63], v[116:119], a[144:147], 0\n\tv_cvt_pk_bf16_f32 v80, v144, v145\n\t" \
    "v_add_f32 v152, v152, v146\n\tv_mfma_f32_16x16x32_bf16 v[52:55], v[116:119], a[128:131], 0\n\ts_waitcnt lgkmcnt(5)\n\tv_exp_f32 v147, v19\n\t" \
    "v_mfma_f32_16x16x32_bf16 v[48:51], v[120:123], a[132:135], v[48:51]\n\tds_read_b128 v[112:115], v177 offset:24576\n\tv_add_f32 v152, v152, v147\n\t" \
    "v_mfma_f32_16x16x32_bf16 v[56:59], v[120:123], a[148:151], v[56:59]\n\ts_add_u32 m0, %[lb], 16384\n\tv_exp_f32 v148, v20\n\tv_mfma_f32_16x16x32_bf16 v[64:67], v[120:123], a[164:167], v[64:67]\n\t" \
    "v_cvt_pk_bf16_f32 v81, v146, v147\n\tv_add_f32 v152, v152, v148\n\tv_mfma_f32_16x16x32_bf16 v[72:75], v[120:123], a[180:183], v[72:75]\n\ts_waitcnt lgkmcnt(5)\n\tv_exp_f32 v149, v21\n\t" \
    "v_mfma_f32_16x16x32_bf16 v[76:79], v[124:127], a[180:183], v[76:79]\n\tds_read_b128 v[116:119], v177 offset:25600\n\tbuffer_load_dwordx4 %[dv], %[rs], s62 offen sc1 lds\n\t" \
    "v_add_f32 v152, v152, v149\n\tv_mfma_f32_16x16x32_bf16 v[68:71], v[124:127], a[164:167], v[68:71]\n\tv_exp_f32 v150, v22\n\tv_mfma_f32_16x16x32_bf16 v[60:63], v[124:127], a[148:151], v[60:63]\n\t" \
    "v_cvt_pk_bf16_f32 v82, v148, v149\n\tv_add_f32 v152, v152, v150\n\tv_mfma_f32_16x16x32_bf16 v[52:55], v[124:127], a[132:135], v[52:55]\n\ts_waitcnt lgkmcnt(5)\n\tv_exp_f32 v151, v23\n\t" \
    "v_mfma_f32_16x16x32_bf16 v[48:51], v[128:131], a[136:139], v[48:51]\n\tds_read_b128 v[120:123], v177 offset:26624\n\tbuffer_load_dwordx4 %[dv], %[rs], s62 offen offset:1024 sc1 lds\n\t" \
    "v_add_f32 v152, v152, v151\n\tv_mfma_f32_16x16x32_bf16 v[56:59], v[128:131], a[152:155], v[56:59]\n\ts_add_u32 m0, %[lb], 122880\n\tv_exp_f32 v144, v24\n\t" \
    "v_mfma_f32_16x16x32_bf16 v[64:67], v[128:131], a[168:171], v[64:67]\n\ts_add_u32 s62, s62, 0x40000\n\tv_cvt_pk_bf16_f32 v83, v150, v151\n\tv_add_f32 v153, v153, v144\n\t" \
    "v_mfma_f32_16x16x32_bf16 v[72:75], v[128:131], a[184:187], v[72:75]\n\ts_waitcnt lgkmcnt(5)\n\tv_exp_f32 v145, v25\n\tv_mfma_f32_16x16x32_bf16 v[76:79], v[132:135], a[184:187], v[76:79]\n\t" \
    "ds_read_b128 v[124:127], v177 offset:27648\n\tbuffer_load_dwordx4 %[dv], %[rs], s63 offen sc1 lds\n\tv_add_f32 v153, v153, v145\n\t" \
    "v_mfma_f32_16x16x32_bf16 v[68:71], v[132:135], a[168:171], v[68:71]\n\tv_exp_f32 v146, v26\n\tv_mfma_f32_16x16x32_bf16 v[60:63], v[132:135], a[152:155], v[60:63]\n\t" \
    "v_cvt_pk_bf16_f32 v84, v144, v145\n\tv_add_f32 v153, v153, v146\n\tv_mfma_f32_16x16x32_bf16 v[52:55], v[132:135], a[136:139], v[52:55]\n\ts_waitcnt lgkmcnt(5)\n\tv_exp_f32 v147, v27\n\t" \
    "v_mfma_f32_16x16x32_bf16 v[48:51], v[136:139], a[140:143], v[48:51]\n\tds_read_b128 v[128:131], v177 offset:28672\n\tbuffer_load_dwordx4 %[dv], %[rs], s63 offen offset:1024 sc1 lds\n\t" \
    "v_add_f32 v153, v153, v147\n\tv_mfma_f32_16x16x32_bf16 v[56:59], v[136:139], a[156:159], v[56:59]\n\tv_exp_f32 v148, v28\n\tv_mfma_f32_16x16x32_bf16 v[64:67], v[136:139], a[172:175], v[64:67]\n\t" \
    "s_add_u32 s63, s63, 0x40000\n\tv_cvt_pk_bf16_f32 v85, v146, v147\n\tv_add_f32 v153, v153, v148\n\tv_mfma_f32_16x16x32_bf16 v[72:75], v[136:139], a[188:191], v[72:75]\n\ts_waitcnt lgkmcnt(5)\n\t" \
    "v_exp_f32 v149, v29\n\tv_mfma_f32_16x16x32_bf16 v[76:79], v[140:143], a[188:191], v[76:79]\n\tds_read_b128 v[132:135], v177 offset:29696\n\tv_add_f32 v153, v153, v149\n\t" \
    "v_mfma_f32_16x16x32_bf16 v[68:71], v[140:143], a[172:175], v[68:71]\n\tv_exp_f32 v150, v30\n\tv_mfma_f32_16x16x32_bf16 v[60:63], v[140:143], a[156:159], v[60:63]\n\t" \
    "v_cvt_pk_bf16_f32 v86, v148, v149\n\tv_add_f32 v153, v153, v150\n\tv_mfma_f32_16x16x32_bf16 v[52:55], v[140:143], a[140:143], v[52:55]\n\ts_waitcnt lgkmcnt(5)\n\tv_exp_f32 v151, v31\n\t" \
    "v_mfma_f32_16x16x32_bf16 a[0:3], v[112:115], v[96:99], a[0:3]\n\tds_read_b128 v[136:139], v177 offset:30720\n\tv_add_f32 v153, v153, v151\n\t" \
    "v_mfma_f32_16x16x32_bf16 a[32:35], v[112:115], v[100:103], a[32:35]\n\tv_exp_f32 v144, v32\n\tv_mfma_f32_16x16x32_bf16 a[64:67], v[112:115], v[104:107], a[64:67]\n\t" \
    "v_cvt_pk_bf16_f32 v87, v150, v151\n\tv_add_f32 v154, v154, v144\n\tv_mfma_f32_16x16x32_bf16 a[96:99], v[112:115], v[108:111], a[96:99]\n\ts_waitcnt lgkmcnt(5)\n\tv_exp_f32 v145, v33\n\t" \
    "v_mfma_f32_16x16x32_bf16 a[100:103], v[116:119], v[108:111], a[100:103]\n\tds_read_b128 v[140:143], v177 offset:31744\n\tv_add_f32 v154, v154, v145\n\t" \
    "v_mfma_f32_16x16x32_bf16 a[68:71], v[116:119], v[104:107], a[68:71]\n\tv_exp_f32 v146, v34\n\tv_mfma_f32_16x16x32_bf16 a[36:39], v[116:119], v[100:103], a[36:39]\n\t" \
    "v_cvt_pk_bf16_f32 v88, v144, v145\n\tv_add_f32 v154, v154, v146\n\tv_mfma_f32_16x16x32_bf16 a[4:7], v[116:119], v[96:99], a[4:7]\n\ts_waitcnt lgkmcnt(5)\n\tv_exp_f32 v147, v35\n\t" \
    "v_mfma_f32_16x16x32_bf16 a[8:11], v[120:123], v[96:99], a[8:11]\n\tds_read_b128 v[112:115], v176 offset:49152\n\tv_add_f32 v154, v154, v147\n\t" \
    "v_mfma_f32_16x16x32_bf16 a[40:43], v[120:123], v[100:103], a[40:43]\n\tv_exp_f32 v148, v36\n\tv_mfma_f32_16x16x32_bf16 a[72:75], v[120:123], v[104:107], a[72:75]\n\t" \
    "v_cvt_pk_bf16_f32 v89, v146, v147\n\tv_add_f32 v154, v154, v148\n\tv_mfma_f32_16x16x32_bf16 a[104:107], v[120:123], v[108:111], a[104:107]\n\ts_waitcnt lgkmcnt(5)\n\tv_exp_f32 v149, v37\n\t" \
    "v_mfma_f32_16x16x32_bf16 a[108:111], v[124:127], v[108:111], a[108:111]\n\tds_read_b128 v[116:119], v176 offset:50176\n\tv_add_f32 v154, v154, v149\n\t" \
    "v_mfma_f32_16x16x32_bf16 a[76:79], v[124:127], v[104:107], a[76:79]\n\tv_exp_f32 v150, v38\n\tv_mfma_f32_16x16x32_bf16 a[44:47], v[124:127], v[100:103], a[44:47]\n\t" \
    "v_cvt_pk_bf16_f32 v90, v148, v149\n\tv_add_f32 v154, v154, v150\n\tv_mfma_f32_16x16x32_bf16 a[12:15], v[124:127], v[96:99], a[12:15]\n\ts_waitcnt lgkmcnt(5)\n\tv_exp_f32 v151, v39\n\t" \
    "v_mfma_f32_16x16x32_bf16 a[16:19], v[128:131], v[96:99], a[16:19]\n\tds_read_b128 v[120:123], v176 offset:51200\n\tv_add_f32 v154, v154, v151\n\t" \
    "v_mfma_f32_16x16x32_bf16 a[48:51], v[128:131], v[100:103], a[48:51]\n\tv_exp_f32 v144, v40\n\tv_mfma_f32_16x16x32_bf16 a[80:83], v[128:131], v[104:107], a[80:83]\n\t" \
    "v_cvt_pk_bf16_f32 v91, v150, v151\n\tv_add_f32 v155, v155, v144\n\tv_mfma_f32_16x16x32_bf16 a[112:115], v[128:131], v[108:111], a[112:115]\n\ts_waitcnt lgkmcnt(5)\n\tv_exp_f32 v145, v41\n\t" \
    "v_mfma_f32_16x16x32_bf16 a[116:119], v[132:135], v[108:111], a[116:119]\n\tds_read_b128 v[124:127], v176 offset:52224\n\tv_add_f32 v155, v155, v145\n\t" \
    "v_mfma_f32_16x16x32_bf16 a[84:87], v[132:135], v[104:107], a[84:87]\n\tv_exp_f32 v146, v42\n\tv_mfma_f32_16x16x32_bf16 a[52:55], v[132:135], v[100:103], a[52:55]\n\t" \
    "v_cvt_pk_bf16_f32 v92, v144, v145\n\tv_add_f32 v155, v155, v146\n\tv_mfma_f32_16x16x32_bf16 a[20:23], v[132:135], v[96:99], a[20:23]\n\ts_waitcnt lgkmcnt(5)\n\tv_exp_f32 v147, v43\n\t" \
    "v_mfma_f32_16x16x32_bf16 a[24:27], v[136:139], v[96:99], a[24:27]\n\tds_read_b128 v[128:131], v176 offset:53248\n\tv_add_f32 v155, v155, v147\n\t" \
    "v_mfma_f32_16x16x32_bf16 a[56:59], v[136:139], v[100:103], a[56:59]\n\tv_exp_f32 v148, v44\n\tv_mfma_f32_16x16x32_bf16 a[88:91], v[136:139], v[104:107], a[88:91]\n\t" \
    "v_cvt_pk_bf16_f32 v93, v146, v147\n\tv_add_f32 v155, v155, v148\n\tv_mfma_f32_16x16x32_bf16 a[120:123], v[136:139], v[108:111], a[120:123]\n\ts_waitcnt lgkmcnt(5)\n\tv_exp_f32 v149, v45\n\t" \
    "v_mfma_f32_16x16x32_bf16 a[124:127], v[140:143], v[108:111], a[124:127]\n\tds_read_b128 v[132:135], v176 offset:54272\n\tv_add_f32 v155, v155, v149\n\t" \
    "v_mfma_f32_16x16x32_bf16 a[92:95], v[140:143], v[104:107], a[92:95]\n\tv_exp_f32 v150, v46\n\tv_mfma_f32_16x16x32_bf16 a[60:63], v[140:143], v[100:103], a[60:63]\n\t" \
    "v_cvt_pk_bf16_f32 v94, v148, v149\n\tv_add_f32 v155, v155, v150\n\tv_mfma_f32_16x16x32_bf16 a[28:31], v[140:143], v[96:99], a[28:31]\n\ts_waitcnt lgkmcnt(5)\n\tv_exp_f32 v151, v47\n\t" \
    "v_mfma_f32_16x16x32_bf16 v[16:19], v[112:115], a[128:131], 0\n\tds_read_b128 v[136:139], v176 offset:55296\n\tv_add_f32 v155, v155, v151\n\t" \
    "v_mfma_f32_16x16x32_bf16 v[24:27], v[112:115], a[144:147], 0\n\tv_exp_f32 v144, v48\n\tv_mfma_f32_16x16x32_bf16 v[32:35], v[112:115], a[160:163], 0\n\tv_cvt_pk_bf16_f32 v95, v150, v151\n\t" \
    "v_add_f32 v152, v152, v144\n\tv_mfma_f32_16x16x32_bf16 v[40:43], v[112:115], a[176:179], 0\n\ts_waitcnt lgkmcnt(5)\n\tv_exp_f32 v145, v49\n\t" \
    "v_mfma_f32_16x16x32_bf16 v[44:47], v[116:119], a[176:179], 0\n\tds_read_b128 v[140:143], v176 offset:56320\n\tv_add_f32 v152, v152, v145\n\t" \
    "v_mfma_f32_16x16x32_bf16 v[36:39], v[116:119], a[160:163], 0\n\tv_exp_f32 v146, v50\n\tv_mfma_f32_16x16x32_bf16 v[28:31], v[116:119], a[144:147], 0\n\tv_cvt_pk_bf16_f32 v96, v144, v145\n\t" \
    "v_add_f32 v152, v152, v146\n\tv_mfma_f32_16x16x32_bf16 v[20:23], v[116:119], a[128:131], 0\n\ts_waitcnt lgkmcnt(5)\n\tv_exp_f32 v147, v51\n\t" \
    "v_mfma_f32_16x16x32_bf16 v[16:19], v[120:123], a[132:135], v[16:19]\n\tds_read_b128 v[112:115], v177 offset:32768\n\tv_add_f32 v152, v152, v147\n\t" \
    "v_mfma_f32_16x16x32_bf16 v[24:27], v[120:123], a[148:151], v[24:27]\n\ts_add_u32 m0, %[lb], 24576\n\tv_exp_f32 v148, v52\n\tv_mfma_f32_16x16x32_bf16 v[32:35], v[120:123], a[164:167], v[32:35]\n\t" \
    "v_cvt_pk_bf16_f32 v97, v146, v147\n\tv_add_f32 v152, v152, v148\n\tv_mfma_f32_16x16x32_bf16 v[40:43], v[120:123], a[180:183], v[40:43]\n\ts_waitcnt lgkmcnt(5)\n\tv_exp_f32 v149, v53\n\t" \
    "v_mfma_f32_16x16x32_bf16 v[44:47], v[124:127], a[180:183], v[44:47]\n\tds_read_b128 v[116:119], v177 offset:33792\n\tbuffer_load_dwordx4 %[dv], %[rs], s62 offen sc1 lds\n\t" \
    "v_add_f32 v152, v152, v149\n\tv_mfma_f32_16x16x32_bf16 v[36:39], v[124:127], a[164:167], v[36:39]\n\tv_exp_f32 v150, v54\n\tv_mfma_f32_16x16x32_bf16 v[28:31], v[124:127], a[148:151], v[28:31]\n\t" \
    "v_cvt_pk_bf16_f32 v98, v148, v149\n\tv_add_f32 v152, v152, v150\n\tv_mfma_f32_16x16x32_bf16 v[20:23], v[124:127], a[132:135], v[20:23]\n\ts_waitcnt lgkmcnt(5)\n\tv_exp_f32 v151, v55\n\t" \
    "v_mfma_f32_16x16x32_bf16 v[16:19], v[128:131], a[136:139], v[16:19]\n\tds_read_b128 v[120:123], v177 offset:34816\n\tbuffer_load_dwordx4 %[dv], %[rs], s62 offen offset:1024 sc1 lds\n\t" \
    "v_add_f32 v152, v152, v151\n\tv_mfma_f32_16x16x32_bf16 v[24:27], v[128:131], a[152:155], v[24:27]\n\ts_add_u32 m0, %[lb], 65536\n\tv_exp_f32 v144, v56\n\t" \
    "v_mfma_f32_16x16x32_bf16 v[32:35], v[128:131], a[168:171], v[32:35]\n\ts_add_u32 s62, s62, 0x40000\n\tv_cvt_pk_bf16_f32 v99, v150, v151\n\tv_add_f32 v153, v153, v144\n\t" \
    "v_mfma_f32_16x16x32_bf16 v[40:43], v[128:131], a[184:187], v[40:43]\n\ts_waitcnt lgkmcnt(5)\n\tv_exp_f32 v145, v57\n\tv_mfma_f32_16x16x32_bf16 v[44:47], v[132:135], a[184:187], v[44:47]\n\t" \
    "ds_read_b128 v[124:127], v177 offset:35840\n\tbuffer_load_dwordx4 %[dv], %[rs], s63 offen sc1 lds\n\tv_add_f32 v153, v153, v145\n\t" \
    "v_mfma_f32_16x16x32_bf16 v[36:39], v[132:135], a[168:171], v[36:39]\n\tv_exp_f32 v146, v58\n\tv_mfma_f32_16x16x32_bf16 v[28:31], v[132:135], a[152:155], v[28:31]\n\t" \
    "v_cvt_pk_bf16_f32 v100, v144, v145\n\tv_add_f32 v153, v153, v146\n\tv_mfma_f32_16x16x32_bf16 v[20:23], v[132:135], a[136:139], v[20:23]\n\ts_waitcnt lgkmcnt(5)\n\tv_exp_f32 v147, v59\n\t" \
    "v_mfma_f32_16x16x32_bf16 v[16:19], v[136:139], a[140:143], v[16:19]\n\tds_read_b128 v[128:131], v177 offset:36864\n\tbuffer_load_dwordx4 %[dv], %[rs], s63 offen offset:1024 sc1 lds\n\t" \
    "v_add_f32 v153, v153, v147\n\tv_mfma_f32_16x16x32_bf16 v[24:27], v[136:139], a[156:159], v[24:27]\n\tv_exp_f32 v148, v60\n\tv_mfma_f32_16x16x32_bf16 v[32:35], v[136:139], a[172:175], v[32:35]\n\t" \
    "s_add_u32 s63, s63, 0x40000\n\tv_cvt_pk_bf16_f32 v101, v146, v147\n\tv_add_f32 v153, v153, v148\n\tv_mfma_f32_16x16x32_bf16 v[40:43], v[136:139], a[188:191], v[40:43]\n\ts_waitcnt lgkmcnt(5)\n\t" \
    "v_exp_f32 v149, v61\n\tv_mfma_f32_16x16x32_bf16 v[44:47], v[140:143], a[188:191], v[44:47]\n\tds_read_b128 v[132:135], v177 offset:37888\n\tv_add_f32 v153, v153, v149\n\t" \
    "v_mfma_f32_16x16x32_bf16 v[36:39], v[140:143], a[172:175], v[36:39]\n\tv_exp_f32 v150, v62\n\tv_mfma_f32_16x16x32_bf16 v[28:31], v[140:143], a[156:159], v[28:31]\n\t" \
    "v_cvt_pk_bf16_f32 v102, v148, v149\n\tv_add_f32 v153, v153, v150\n\tv_mfma_f32_16x16x32_bf16 v[20:23], v[140:143], a[140:143], v[20:23]\n\ts_waitcnt lgkmcnt(5)\n\tv_exp_f32 v151, v63\n\t" \
    "v_mfma_f32_16x16x32_bf16 a[0:3], v[112:115], v[80:83], a[0:3]\n\tds_read_b128 v[136:139], v177 offset:38912\n\tv_add_f32 v153, v153, v151\n\t" \
    "v_mfma_f32_16x16x32_bf16 a[32:35], v[112:115], v[84:87], a[32:35]\n\tv_exp_f32 v144, v64\n\tv_mfma_f32_16x16x32_bf16 a[64:67], v[112:115], v[88:91], a[64:67]\n\tv_cvt_pk_bf16_f32 v103, v150, v151\n\t" \
    "v_add_f32 v154, v154, v144\n\tv_mfma_f32_16x16x32_bf16 a[96:99], v[112:115], v[92:95], a[96:99]\n\ts_waitcnt lgkmcnt(5)\n\tv_exp_f32 v145, v65\n\t" \
    "v_mfma_f32_16x16x32_bf16 a[100:103], v[116:119], v[92:95], a[100:103]\n\tds_read_b128 v[140:143], v177 offset:39936\n\tv_add_f32 v154, v154, v145\n\t" \
    "v_mfma_f32_16x16x32_bf16 a[68:71], v[116:119], v[88:91], a[68:71]\n\tv_exp_f32 v146, v66\n\tv_mfma_f32_16x16x32_bf16 a[36:39], v[116:119], v[84:87], a[36:39]\n\tv_cvt_pk_bf16_f32 v104, v144, v145\n\t" \
    "v_add_f32 v154, v154, v146\n\tv_mfma_f32_16x16x32_bf16 a[4:7], v[116:119], v[80:83], a[4:7]\n\ts_waitcnt lgkmcnt(5)\n\tv_exp_f32 v147, v67\n\t" \
    "v_mfma_f32_16x16x32_bf16 a[8:11], v[120:123], v[80:83], a[8:11]\n\tds_read_b128 v[112:115], v176 offset:57344\n\tv_add_f32 v154, v154, v147\n\t" \
    "v_mfma_f32_16x16x32_bf16 a[40:43], v[120:123], v[84:87], a[40:43]\n\tv_exp_f32 v148, v68\n\tv_mfma_f32_16x16x32_bf16 a[72:75], v[120:123], v[88:91], a[72:75]\n\tv_cvt_pk_bf16_f32 v105, v146, v147\n\t" \
    "v_add_f32 v154, v154, v148\n\tv_mfma_f32_16x16x32_bf16 a[104:107], v[120:123], v[92:95], a[104:107]\n\ts_waitcnt lgkmcnt(5)\n\tv_exp_f32 v149, v69\n\t" \
    "v_mfma_f32_16x16x32_bf16 a[108:111], v[124:127], v[92:95], a[108:111]\n\tds_read_b128 v[116:119], v176 offset:58368\n\tv_add_f32 v154, v154, v149\n\t" \
    "v_mfma_f32_16x16x32_bf16 a[76:79], v[124:127], v[88:91], a[76:79]\n\tv_exp_f32 v150, v70\n\tv_mfma_f32_16x16x32_bf16 a[44:47], v[124:127], v[84:87], a[44:47]\n\tv_cvt_pk_bf16_f32 v106, v148, v149\n\t" \
    "v_add_f32 v154, v154, v150\n\tv_mfma_f32_16x16x32_bf16 a[12:15], v[124:127], v[80:83], a[12:15]\n\ts_waitcnt lgkmcnt(5)\n\tv_exp_f32 v151, v71\n\t" \
    "v_mfma_f32_16x16x32_bf16 a[16:19], v[128:131], v[80:83], a[16:19]\n\tds_read_b128 v[120:123], v176 offset:59392\n\tv_add_f32 v154, v154, v151\n\t" \
    "v_mfma_f32_16x16x32_bf16 a[48:51], v[128:131], v[84:87], a[48:51]\n\tv_exp_f32 v144, v72\n\tv_mfma_f32_16x16x32_bf16 a[80:83], v[128:131], v[88:91], a[80:83]\n\tv_cvt_pk_bf16_f32 v107, v150, v151\n\t" \
    "v_add_f32 v155, v155, v144\n\tv_mfma_f32_16x16x32_bf16 a[112:115], v[128:131], v[92:95], a[112:115]\n\ts_waitcnt lgkmcnt(5)\n\tv_exp_f32 v145, v73\n\t" \
    "v_mfma_f32_16x16x32_bf16 a[116:119], v[132:135], v[92:95], a[116:119]\n\tds_read_b128 v[124:127], v176 offset:60416\n\tv_add_f32 v155, v155, v145\n\t" \
    "v_mfma_f32_16x16x32_bf16 a[84:87], v[132:135], v[88:91], a[84:87]\n\tv_exp_f32 v146, v74\n\tv_mfma_f32_16x16x32_bf16 a[52:55], v[132:135], v[84:87], a[52:55]\n\tv_cvt_pk_bf16_f32 v108, v144, v145\n\t" \
    "v_add_f32 v155, v155, v146\n\tv_mfma_f32_16x16x32_bf16 a[20:23], v[132:135], v[80:83], a[20:23]\n\ts_waitcnt lgkmcnt(5)\n\tv_exp_f32 v147, v75\n\t" \
    "v_mfma_f32_16x16x32_bf16 a[24:27], v[136:139], v[80:83], a[24:27]\n\tds_read_b128 v[128:131], v176 offset:61440\n\tv_add_f32 v155, v155, v147\n\t" \
    "v_mfma_f32_16x16x32_bf16 a[56:59], v[136:139], v[84:87], a[56:59]\n\tv_exp_f32 v148, v76\n\tv_mfma_f32_16x16x32_bf16 a[88:91], v[136:139], v[88:91], a[88:91]\n\tv_cvt_pk_bf16_f32 v109, v146, v147\n\t" \
    "v_add_f32 v155, v155, v148\n\tv_mfma_f32_16x16x32_bf16 a[120:123], v[136:139], v[92:95], a[120:123]\n\ts_waitcnt lgkmcnt(5)\n\tv_exp_f32 v149, v77\n\t" \
    "v_mfma_f32_16x16x32_bf16 a[124:127], v[140:143], v[92:95], a[124:127]\n\tds_read_b128 v[132:135], v176 offset:62464\n\tv_add_f32 v155, v155, v149\n\t" \
    "v_mfma_f32_16x16x32_bf16 a[92:95], v[140:143], v[88:91], a[92:95]\n\tv_exp_f32 v150, v78\n\tv_mfma_f32_16x16x32_bf16 a[60:63], v[140:143], v[84:87], a[60:63]\n\tv_cvt_pk_bf16_f32 v110, v148, v149\n\t" \
    "v_add_f32 v155, v155, v150\n\tv_mfma_f32_16x16x32_bf16 a[28:31], v[140:143], v[80:83], a[28:31]\n\ts_waitcnt lgkmcnt(5)\n\tv_exp_f32 v151, v79\n\t" \
    "v_mfma_f32_16x16x32_bf16 v[48:51], v[112:115], a[128:131], 0\n\ts_waitcnt vmcnt(8)\n\ts_barrier\n\tds_read_b128 v[136:139], v176 offset:63488\n\tv_add_f32 v155, v155, v151\n\t" \
    "v_mfma_f32_16x16x32_bf16 v[56:59], v[112:115], a[144:147], 0\n\tv_exp_f32 v144, v16\n\tv_mfma_f32_16x16x32_bf16 v[64:67], v[112:115], a[160:163], 0\n\tv_cvt_pk_bf16_f32 v111, v150, v151\n\t" \
    "v_add_f32 v152, v152, v144\n\tv_mfma_f32_16x16x32_bf16 v[72:75], v[112:115], a[176:179], 0\n\ts_waitcnt lgkmcnt(5)\n\tv_exp_f32 v145, v17\n\t" \
    "v_mfma_f32_16x16x32_bf16 v[76:79], v[116:119], a[176:179], 0\n\tds_read_b128 v[140:143], v176 offset:64512\n\tv_add_f32 v152, v152, v145\n\t" \
    "v_mfma_f32_16x16x32_bf16 v[68:71], v[116:119], a[160:163], 0\n\tv_exp_f32 v146, v18\n\tv_mfma_f32_16x16x32_bf16 v[60:63], v[116:119], a[144:147], 0\n\tv_cvt_pk_bf16_f32 v80, v144, v145\n\t" \
    "v_add_f32 v152, v152, v146\n\tv_mfma_f32_16x16x32_bf16 v[52:55], v[116:119], a[128:131], 0\n\ts_waitcnt lgkmcnt(5)\n\tv_exp_f32 v147, v19\n\t" \
    "v_mfma_f32_16x16x32_bf16 v[48:51], v[120:123], a[132:135], v[48:51]\n\tds_read_b128 v[112:115], v177 offset:40960\n\tv_add_f32 v152, v152, v147\n\t" \
    "v_mfma_f32_16x16x32_bf16 v[56:59], v[120:123], a[148:151], v[56:59]\n\ts_add_u32 m0, %[lb], 32768\n\tv_exp_f32 v148, v20\n\tv_mfma_f32_16x16x32_bf16 v[64:67], v[120:123], a[164:167], v[64:67]\n\t" \
    "v_cvt_pk_bf16_f32 v81, v146, v147\n\tv_add_f32 v152, v152, v148\n\tv_mfma_f32_16x16x32_bf16 v[72:75], v[120:123], a[180:183], v[72:75]\n\ts_waitcnt lgkmcnt(5)\n\tv_exp_f32 v149, v21\n\t" \
    "v_mfma_f32_16x16x32_bf16 v[76:79], v[124:127], a[180:183], v[76:79]\n\tds_read_b128 v[116:119], v177 offset:41984\n\tbuffer_load_dwordx4 %[dv], %[rs], s62 offen sc1 lds\n\t" \
    "v_add_f32 v152, v152, v149\n\tv_mfma_f32_16x16x32_bf16 v[68:71], v[124:127], a[164:167], v[68:71]\n\tv_exp_f32 v150, v22\n\tv_mfma_f32_16x16x32_bf16 v[60:63], v[124:127], a[148:151], v[60:63]\n\t" \
    "v_cvt_pk_bf16_f32 v82, v148, v149\n\tv_add_f32 v152, v152, v150\n\tv_mfma_f32_16x16x32_bf16 v[52:55], v[124:127], a[132:135], v[52:55]\n\ts_waitcnt lgkmcnt(5)\n\tv_exp_f32 v151, v23\n\t" \
    "v_mfma_f32_16x16x32_bf16 v[48:51], v[128:131], a[136:139], v[48:51]\n\tds_read_b128 v[120:123], v177 offset:43008\n\tbuffer_load_dwordx4 %[dv], %[rs], s62 offen offset:1024 sc1 lds\n\t" \
    "v_add_f32 v152, v152, v151\n\tv_mfma_f32_16x16x32_bf16 v[56:59], v[128:131], a[152:155], v[56:59]\n\ts_add_u32 m0, %[lb], 73728\n\tv_exp_f32 v144, v24\n\t" \
    "v_mfma_f32_16x16x32_bf16 v[64:67], v[128:131], a[168:171], v[64:67]\n\ts_add_u32 s62, s62, 0x40000\n\tv_cvt_pk_bf16_f32 v83, v150, v151\n\tv_add_f32 v153, v153, v144\n\t" \
    "v_mfma_f32_16x16x32_bf16 v[72:75], v[128:131], a[184:187], v[72:75]\n\ts_waitcnt lgkmcnt(5)\n\tv_exp_f32 v145, v25\n\tv_mfma_f32_16x16x32_bf16 v[76:79], v[132:135], a[184:187], v[76:79]\n\t" \
    "ds_read_b128 v[124:127], v177 offset:44032\n\tbuffer_load_dwordx4 %[dv], %[rs], s63 offen sc1 lds\n\tv_add_f32 v153, v153, v145\n\t" \
    "v_mfma_f32_16x16x32_bf16 v[68:71], v[132:135], a[168:171], v[68:71]\n\tv_exp_f32 v146, v26\n\tv_mfma_f32_16x16x32_bf16 v[60:63], v[132:135], a[152:155], v[60:63]\n\t" \
    "v_cvt_pk_bf16_f32 v84, v144, v145\n\tv_add_f32 v153, v153, v146\n\tv_mfma_f32_16x16x32_bf16 v[52:55], v[132:135], a[136:139], v[52:55]\n\ts_waitcnt lgkmcnt(5)\n\tv_exp_f32 v147, v27\n\t" \
    "v_mfma_f32_16x16x32_bf16 v[48:51], v[136:139], a[140:143], v[48:51]\n\tds_read_b128 v[128:131], v177 offset:45056\n\tbuffer_load_dwordx4 %[dv], %[rs], s63 offen offset:1024 sc1 lds\n\t" \
    "v_add_f32 v153, v153, v147\n\tv_mfma_f32_16x16x32_bf16 v[56:59], v[136:139], a[156:159], v[56:59]\n\tv_exp_f32 v148, v28\n\tv_mfma_f32_16x16x32_bf16 v[64:67], v[136:139], a[172:175], v[64:67]\n\t" \
    "s_add_u32 s63, s63, 0x40000\n\tv_cvt_pk_bf16_f32 v85, v146, v147\n\tv_add_f32 v153, v153, v148\n\tv_mfma_f32_16x16x32_bf16 v[72:75], v[136:139], a[188:191], v[72:75]\n\ts_waitcnt lgkmcnt(5)\n\t" \
    "v_exp_f32 v149, v29\n\tv_mfma_f32_16x16x32_bf16 v[76:79], v[140:143], a[188:191], v[76:79]\n\tds_read_b128 v[132:135], v177 offset:46080\n\tv_add_f32 v153, v153, v149\n\t" \
    "v_mfma_f32_16x16x32_bf16 v[68:71], v[140:143], a[172:175], v[68:71]\n\tv_exp_f32 v150, v30\n\tv_mfma_f32_16x16x32_bf16 v[60:63], v[140:143], a[156:159], v[60:63]\n\t" \
    "v_cvt_pk_bf16_f32 v86, v148, v149\n\tv_add_f32 v153, v153, v150\n\tv_mfma_f32_16x16x32_bf16 v[52:55], v[140:143], a[140:143], v[52:55]\n\ts_waitcnt lgkmcnt(5)\n\tv_exp_f32 v151, v31\n\t" \
    "v_mfma_f32_16x16x32_bf16 a[0:3], v[112:115], v[96:99], a[0:3]\n\tds_read_b128 v[136:139], v177 offset:47104\n\tv_add_f32 v153, v153, v151\n\t" \
    "v_mfma_f32_16x16x32_bf16 a[32:35], v[112:115], v[100:103], a[32:35]\n\tv_exp_f32 v144, v32\n\tv_mfma_f32_16x16x32_bf16 a[64:67], v[112:115], v[104:107], a[64:67]\n\t" \
    "v_cvt_pk_bf16_f32 v87, v150, v151\n\tv_add_f32 v154, v154, v144\n\tv_mfma_f32_16x16x32_bf16 a[96:99], v[112:115], v[108:111], a[96:99]\n\ts_waitcnt lgkmcnt(5)\n\tv_exp_f32 v145, v33\n\t" \
    "v_mfma_f32_16x16x32_bf16 a[100:103], v[116:119], v[108:111], a[100:103]\n\tds_read_b128 v[140:143], v177 offset:48128\n\tv_add_f32 v154, v154, v145\n\t" \
    "v_mfma_f32_16x16x32_bf16 a[68:71], v[116:119], v[104:107], a[68:71]\n\tv_exp_f32 v146, v34\n\tv_mfma_f32_16x16x32_bf16 a[36:39], v[116:119], v[100:103], a[36:39]\n\t" \
    "v_cvt_pk_bf16_f32 v88, v144, v145\n\tv_add_f32 v154, v154, v146\n\tv_mfma_f32_16x16x32_bf16 a[4:7], v[116:119], v[96:99], a[4:7]\n\ts_waitcnt lgkmcnt(5)\n\tv_exp_f32 v147, v35\n\t" \
    "v_mfma_f32_16x16x32_bf16 a[8:11], v[120:123], v[96:99], a[8:11]\n\tds_read_b128 v[112:115], v176\n\tv_add_f32 v154, v154, v147\n\t" \
    "v_mfma_f32_16x16x32_bf16 a[40:43], v[120:123], v[100:103], a[40:43]\n\tv_exp_f32 v148, v36\n\tv_mfma_f32_16x16x32_bf16 a[72:75], v[120:123], v[104:107], a[72:75]\n\t" \
    "v_cvt_pk_bf16_f32 v89, v146, v147\n\tv_add_f32 v154, v154, v148\n\tv_mfma_f32_16x16x32_bf16 a[104:107], v[120:123], v[108:111], a[104:107]\n\ts_waitcnt lgkmcnt(5)\n\tv_exp_f32 v149, v37\n\t" \
    "v_mfma_f32_16x16x32_bf16 a[108:111], v[124:127], v[108:111], a[108:111]\n\tds_read_b128 v[116:119], v176 offset:1024\n\tv_add_f32 v154, v154, v149\n\t" \
    "v_mfma_f32_16x16x32_bf16 a[76:79], v[124:127], v[104:107], a[76:79]\n\tv_exp_f32 v150, v38\n\tv_mfma_f32_16x16x32_bf16 a[44:47], v[124:127], v[100:103], a[44:47]\n\t" \
    "v_cvt_pk_bf16_f32 v90, v148, v149\n\tv_add_f32 v154, v154, v150\n\tv_mfma_f32_16x16x32_bf16 a[12:15], v[124:127], v[96:99], a[12:15]\n\ts_waitcnt lgkmcnt(5)\n\tv_exp_f32 v151, v39\n\t" \
    "v_mfma_f32_16x16x32_bf16 a[16:19], v[128:131], v[96:99], a[16:19]\n\tds_read_b128 v[120:123], v176 offset:2048\n\tv_add_f32 v154, v154, v151\n\t" \
    "v_mfma_f32_16x16x32_bf16 a[48:51], v[128:131], v[100:103], a[48:51]\n\tv_exp_f32 v144, v40\n\tv_mfma_f32_16x16x32_bf16 a[80:83], v[128:131], v[104:107], a[80:83]\n\t" \
    "v_cvt_pk_bf16_f32 v91, v150, v151\n\tv_add_f32 v155, v155, v144\n\tv_mfma_f32_16x16x32_bf16 a[112:115], v[128:131], v[108:111], a[112:115]\n\ts_waitcnt lgkmcnt(5)\n\tv_exp_f32 v145, v41\n\t" \
    "v_mfma_f32_16x16x32_bf16 a[116:119], v[132:135], v[108:111], a[116:119]\n\tds_read_b128 v[124:127], v176 offset:3072\n\tv_add_f32 v155, v155, v145\n\t" \
    "v_mfma_f32_16x16x32_bf16 a[84:87], v[132:135], v[104:107], a[84:87]\n\tv_exp_f32 v146, v42\n\tv_mfma_f32_16x16x32_bf16 a[52:55], v[132:135], v[100:103], a[52:55]\n\t" \
    "v_cvt_pk_bf16_f32 v92, v144, v145\n\tv_add_f32 v155, v155, v146\n\tv_mfma_f32_16x16x32_bf16 a[20:23], v[132:135], v[96:99], a[20:23]\n\ts_waitcnt lgkmcnt(5)\n\tv_exp_f32 v147, v43\n\t" \
    "v_mfma_f32_16x16x32_bf16 a[24:27], v[136:139], v[96:99], a[24:27]\n\tds_read_b128 v[128:131], v176 offset:4096\n\tv_add_f32 v155, v155, v147\n\t" \
    "v_mfma_f32_16x16x32_bf16 a[56:59], v[136:139], v[100:103], a[56:59]\n\tv_exp_f32 v148, v44\n\tv_mfma_f32_16x16x32_bf16 a[88:91], v[136:139], v[104:107], a[88:91]\n\t" \
    "v_cvt_pk_bf16_f32 v93, v146, v147\n\tv_add_f32 v155, v155, v148\n\tv_mfma_f32_16x16x32_bf16 a[120:123], v[136:139], v[108:111], a[120:123]\n\ts_waitcnt lgkmcnt(5)\n\tv_exp_f32 v149, v45\n\t" \
    "v_mfma_f32_16x16x32_bf16 a[124:127], v[140:143], v[108:111], a[124:127]\n\ts_sub_u32 s64, s64, 1\n\tds_read_b128 v[132:135], v176 offset:5120\n\tv_add_f32 v155, v155, v149\n\t" \
    "v_mfma_f32_16x16x32_bf16 a[92:95], v[140:143], v[104:107], a[92:95]\n\ts_cmp_eq_u32 s64, 0\n\tv_exp_f32 v150, v46\n\tv_mfma_f32_16x16x32_bf16 a[60:63], v[140:143], v[100:103], a[60:63]\n\t" \
    "v_cvt_pk_bf16_f32 v94, v148, v149\n\tv_add_f32 v155, v155, v150\n\tv_mfma_f32_16x16x32_bf16 a[28:31], v[140:143], v[96:99], a[28:31]\n\ts_waitcnt lgkmcnt(5)\n\tv_exp_f32 v151, v47\n\t" \
    "s_cbranch_scc1 2f\n\tv_mfma_f32_16x16x32_bf16 v[16:19], v[112:115], a[128:131], 0\n\tds_read_b128 v[136:139], v176 offset:6144\n\tv_add_f32 v155, v155, v151\n\t" \
    "v_mfma_f32_16x16x32_bf16 v[24:27], v[112:115], a[144:147], 0\n\tv_exp_f32 v144, v48\n\tv_mfma_f32_16x16x32_bf16 v[32:35], v[112:115], a[160:163], 0\n\tv_cvt_pk_bf16_f32 v95, v150, v151\n\t" \
    "v_add_f32 v152, v152, v144\n\tv_mfma_f32_16x16x32_bf16 v[40:43], v[112:115], a[176:179], 0\n\ts_waitcnt lgkmcnt(5)\n\tv_exp_f32 v145, v49\n\t" \
    "v_mfma_f32_16x16x32_bf16 v[44:47], v[116:119], a[176:179], 0\n\tds_read_b128 v[140:143], v176 offset:7168\n\tv_add_f32 v152, v152, v145\n\t" \
    "v_mfma_f32_16x16x32_bf16 v[36:39], v[116:119], a[160:163], 0\n\tv_exp_f32 v146, v50\n\tv_mfma_f32_16x16x32_bf16 v[28:31], v[116:119], a[144:147], 0\n\tv_cvt_pk_bf16_f32 v96, v144, v145\n\t" \
    "v_add_f32 v152, v152, v146\n\tv_mfma_f32_16x16x32_bf16 v[20:23], v[116:119], a[128:131], 0\n\ts_waitcnt lgkmcnt(5)\n\tv_exp_f32 v147, v51\n\t" \
    "v_mfma_f32_16x16x32_bf16 v[16:19], v[120:123], a[132:135], v[16:19]\n\tds_read_b128 v[112:115], v177 offset:49152\n\tv_add_f32 v152, v152, v147\n\t" \
    "v_mfma_f32_16x16x32_bf16 v[24:27], v[120:123], a[148:151], v[24:27]\n\ts_add_u32 m0, %[lb], 40960\n\tv_exp_f32 v148, v52\n\tv_mfma_f32_16x16x32_bf16 v[32:35], v[120:123], a[164:167], v[32:35]\n\t" \
    "v_cvt_pk_bf16_f32 v97, v146, v147\n\tv_add_f32 v152, v152, v148\n\tv_mfma_f32_16x16x32_bf16 v[40:43], v[120:123], a[180:183], v[40:43]\n\ts_waitcnt lgkmcnt(5)\n\tv_exp_f32 v149, v53\n\t" \
    "v_mfma_f32_16x16x32_bf16 v[44:47], v[124:127], a[180:183], v[44:47]\n\tds_read_b128 v[116:119], v177 offset:50176\n\tbuffer_load_dwordx4 %[dv], %[rs], s62 offen sc1 lds\n\t" \
    "v_add_f32 v152, v152, v149\n\tv_mfma_f32_16x16x32_bf16 v[36:39], v[124:127], a[164:167], v[36:39]\n\tv_exp_f32 v150, v54\n\tv_mfma_f32_16x16x32_bf16 v[28:31], v[124:127], a[148:151], v[28:31]\n\t" \
    "v_cvt_pk_bf16_f32 v98, v148, v149\n\tv_add_f32 v152, v152, v150\n\tv_mfma_f32_16x16x32_bf16 v[20:23], v[124:127], a[132:135], v[20:23]\n\ts_waitcnt lgkmcnt(5)\n\tv_exp_f32 v151, v55\n\t" \
    "v_mfma_f32_16x16x32_bf16 v[16:19], v[128:131], a[136:139], v[16:19]\n\tds_read_b128 v[120:123], v177 offset:51200\n\tbuffer_load_dwordx4 %[dv], %[rs], s62 offen offset:1024 sc1 lds\n\t" \
    "v_add_f32 v152, v152, v151\n\tv_mfma_f32_16x16x32_bf16 v[24:27], v[128:131], a[152:155], v[24:27]\n\ts_add_u32 m0, %[lb], 81920\n\tv_exp_f32 v144, v56\n\t" \
    "v_mfma_f32_16x16x32_bf16 v[32:35], v[128:131], a[168:171], v[32:35]\n\ts_add_u32 s62, s62, 0x40000\n\tv_cvt_pk_bf16_f32 v99, v150, v151\n\tv_add_f32 v153, v153, v144\n\t" \
    "v_mfma_f32_16x16x32_bf16 v[40:43], v[128:131], a[184:187], v[40:43]\n\ts_waitcnt lgkmcnt(5)\n\tv_exp_f32 v145, v57\n\tv_mfma_f32_16x16x32_bf16 v[44:47], v[132:135], a[184:187], v[44:47]\n\t" \
    "ds_read_b128 v[124:127], v177 offset:52224\n\tbuffer_load_dwordx4 %[dv], %[rs], s63 offen sc1 lds\n\tv_add_f32 v153, v153, v145\n\t" \
    "v_mfma_f32_16x16x32_bf16 v[36:39], v[132:135], a[168:171], v[36:39]\n\tv_exp_f32 v146, v58\n\tv_mfma_f32_16x16x32_bf16 v[28:31], v[132:135], a[152:155], v[28:31]\n\t" \
    "v_cvt_pk_bf16_f32 v100, v144, v145\n\tv_add_f32 v153, v153, v146\n\tv_mfma_f32_16x16x32_bf16 v[20:23], v[132:135], a[136:139], v[20:23]\n\ts_waitcnt lgkmcnt(5)\n\tv_exp_f32 v147, v59\n\t" \
    "v_mfma_f32_16x16x32_bf16 v[16:19], v[136:139], a[140:143], v[16:19]\n\tds_read_b128 v[128:131], v177 offset:53248\n\tbuffer_load_dwordx4 %[dv], %[rs], s63 offen offset:1024 sc1 lds\n\t" \
    "v_add_f32 v153, v153, v147\n\tv_mfma_f32_16x16x32_bf16 v[24:27], v[136:139], a[156:159], v[24:27]\n\tv_exp_f32 v148, v60\n\tv_mfma_f32_16x16x32_bf16 v[32:35], v[136:139], a[172:175], v[32:35]\n\t" \
    "s_add_u32 s63, s63, 0x40000\n\tv_cvt_pk_bf16_f32 v101, v146, v147\n\tv_add_f32 v153, v153, v148\n\tv_mfma_f32_16x16x32_bf16 v[40:43], v[136:139], a[188:191], v[40:43]\n\ts_waitcnt lgkmcnt(5)\n\t" \
    "v_exp_f32 v149, v61\n\tv_mfma_f32_16x16x32_bf16 v[44:47], v[140:143], a[188:191], v[44:47]\n\tds_read_b128 v[132:135], v177 offset:54272\n\tv_add_f32 v153, v153, v149\n\t" \
    "v_mfma_f32_16x16x32_bf16 v[36:39], v[140:143], a[172:175], v[36:39]\n\tv_exp_f32 v150, v62\n\tv_mfma_f32_16x16x32_bf16 v[28:31], v[140:143], a[156:159], v[28:31]\n\t" \
    "v_cvt_pk_bf16_f32 v102, v148, v149\n\tv_add_f32 v153, v153, v150\n\tv_mfma_f32_16x16x32_bf16 v[20:23], v[140:143], a[140:143], v[20:23]\n\ts_waitcnt lgkmcnt(5)\n\tv_exp_f32 v151, v63\n\t" \
    "v_mfma_f32_16x16x32_bf16 a[0:3], v[112:115], v[80:83], a[0:3]\n\tds_read_b128 v[136:139], v177 offset:55296\n\tv_add_f32 v153, v153, v151\n\t" \
    "v_mfma_f32_16x16x32_bf16 a[32:35], v[112:115], v[84:87], a[32:35]\n\tv_exp_f32 v144, v64\n\tv_mfma_f32_16x16x32_bf16 a[64:67], v[112:115], v[88:91], a[64:67]\n\tv_cvt_pk_bf16_f32 v103, v150, v151\n\t" \
    "v_add_f32 v154, v154, v144\n\tv_mfma_f32_16x16x32_bf16 a[96:99], v[112:115], v[92:95], a[96:99]\n\ts_waitcnt lgkmcnt(5)\n\tv_exp_f32 v145, v65\n\t" \
    "v_mfma_f32_16x16x32_bf16 a[100:103], v[116:119], v[92:95], a[100:103]\n\tds_read_b128 v[140:143], v177 offset:56320\n\tv_add_f32 v154, v154, v145\n\t" \
    "v_mfma_f32_16x16x32_bf16 a[68:71], v[116:119], v[88:91], a[68:71]\n\tv_exp_f32 v146, v66\n\tv_mfma_f32_16x16x32_bf16 a[36:39], v[116:119], v[84:87], a[36:39]\n\tv_cvt_pk_bf16_f32 v104, v144, v145\n\t" \
    "v_add_f32 v154, v154, v146\n\tv_mfma_f32_16x16x32_bf16 a[4:7], v[116:119], v[80:83], a[4:7]\n\ts_waitcnt lgkmcnt(5)\n\tv_exp_f32 v147, v67\n\t" \
    "v_mfma_f32_16x16x32_bf16 a[8:11], v[120:123], v[80:83], a[8:11]\n\tds_read_b128 v[112:115], v176 offset:8192\n\tv_add_f32 v154, v154, v147\n\t" \
    "v_mfma_f32_16x16x32_bf16 a[40:43], v[120:123], v[84:87], a[40:43]\n\tv_exp_f32 v148, v68\n\tv_mfma_f32_16x16x32_bf16 a[72:75], v[120:123], v[88:91], a[72:75]\n\tv_cvt_pk_bf16_f32 v105, v146, v147\n\t" \
    "v_add_f32 v154, v154, v148\n\tv_mfma_f32_16x16x32_bf16 a[104:107], v[120:123], v[92:95], a[104:107]\n\ts_waitcnt lgkmcnt(5)\n\tv_exp_f32 v149, v69\n\t" \
    "v_mfma_f32_16x16x32_bf16 a[108:111], v[124:127], v[92:95], a[108:111]\n\tds_read_b128 v[116:119], v176 offset:9216\n\tv_add_f32 v154, v154, v149\n\t" \
    "v_mfma_f32_16x16x32_bf16 a[76:79], v[124:127], v[88:91], a[76:79]\n\tv_exp_f32 v150, v70\n\tv_mfma_f32_16x16x32_bf16 a[44:47], v[124:127], v[84:87], a[44:47]\n\tv_cvt_pk_bf16_f32 v106, v148, v149\n\t" \
    "v_add_f32 v154, v154, v150\n\tv_mfma_f32_16x16x32_bf16 a[12:15], v[124:127], v[80:83], a[12:15]\n\ts_waitcnt lgkmcnt(5)\n\tv_exp_f32 v151, v71\n\t" \
    "v_mfma_f32_16x16x32_bf16 a[16:19], v[128:131], v[80:83], a[16:19]\n\tds_read_b128 v[120:123], v176 offset:10240\n\tv_add_f32 v154, v154, v151\n\t" \
    "v_mfma_f32_16x16x32_bf16 a[48:51], v[128:131], v[84:87], a[48:51]\n\tv_exp_f32 v144, v72\n\tv_mfma_f32_16x16x32_bf16 a[80:83], v[128:131], v[88:91], a[80:83]\n\tv_cvt_pk_bf16_f32 v107, v150, v151\n\t" \
    "v_add_f32 v155, v155, v144\n\tv_mfma_f32_16x16x32_bf16 a[112:115], v[128:131], v[92:95], a[112:115]\n\ts_waitcnt lgkmcnt(5)\n\tv_exp_f32 v145, v73\n\t" \
    "v_mfma_f32_16x16x32_bf16 a[116:119], v[132:135], v[92:95], a[116:119]\n\tds_read_b128 v[124:127], v176 offset:11264\n\tv_add_f32 v155, v155, v145\n\t" \
    "v_mfma_f32_16x16x32_bf16 a[84:87], v[132:135], v[88:91], a[84:87]\n\tv_exp_f32 v146, v74\n\tv_mfma_f32_16x16x32_bf16 a[52:55], v[132:135], v[84:87], a[52:55]\n\tv_cvt_pk_bf16_f32 v108, v144, v145\n\t" \
    "v_add_f32 v155, v155, v146\n\tv_mfma_f32_16x16x32_bf16 a[20:23], v[132:135], v[80:83], a[20:23]\n\ts_waitcnt lgkmcnt(5)\n\tv_exp_f32 v147, v75\n\t" \
    "v_mfma_f32_16x16x32_bf16 a[24:27], v[136:139], v[80:83], a[24:27]\n\tds_read_b128 v[128:131], v176 offset:12288\n\tv_add_f32 v155, v155, v147\n\t" \
    "v_mfma_f32_16x16x32_bf16 a[56:59], v[136:139], v[84:87], a[56:59]\n\tv_exp_f32 v148, v76\n\tv_mfma_f32_16x16x32_bf16 a[88:91], v[136:139], v[88:91], a[88:91]\n\tv_cvt_pk_bf16_f32 v109, v146, v147\n\t" \
    "v_add_f32 v155, v155, v148\n\tv_mfma_f32_16x16x32_bf16 a[120:123], v[136:139], v[92:95], a[120:123]\n\ts_waitcnt lgkmcnt(5)\n\tv_exp_f32 v149, v77\n\t" \
    "v_mfma_f32_16x16x32_bf16 a[124:127], v[140:143], v[92:95], a[124:127]\n\tds_read_b128 v[132:135], v176 offset:13312\n\tv_add_f32 v155, v155, v149\n\t" \
    "v_mfma_f32_16x16x32_bf16 a[92:95], v[140:143], v[88:91], a[92:95]\n\tv_exp_f32 v150, v78\n\tv_mfma_f32_16x16x32_bf16 a[60:63], v[140:143], v[84:87], a[60:63]\n\tv_cvt_pk_bf16_f32 v110, v148, v149\n\t" \
    "v_add_f32 v155, v155, v150\n\tv_mfma_f32_16x16x32_bf16 a[28:31], v[140:143], v[80:83], a[28:31]\n\ts_waitcnt lgkmcnt(5)\n\tv_exp_f32 v151, v79\n\ts_branch 1b\n\t2:\n" \
    "v_mfma_f32_16x16x32_bf16 v[16:19], v[112:115], a[128:131], v[160:163]\n\tds_read_b128 v[136:139], v176 offset:6144\n\tv_add_f32 v155, v155, v151\n\t" \
    "v_mfma_f32_16x16x32_bf16 v[24:27], v[112:115], a[144:147], 0\n\tv_exp_f32 v144, v48\n\tv_mfma_f32_16x16x32_bf16 v[32:35], v[112:115], a[160:163], 0\n\tv_cvt_pk_bf16_f32 v95, v150, v151\n\t" \
    "v_add_f32 v152, v152, v144\n\tv_mfma_f32_16x16x32_bf16 v[40:43], v[112:115], a[176:179], 0\n\ts_waitcnt lgkmcnt(5)\n\tv_exp_f32 v145, v49\n\t" \
    "v_mfma_f32_16x16x32_bf16 v[44:47], v[116:119], a[176:179], 0\n\tds_read_b128 v[140:143], v176 offset:7168\n\tv_add_f32 v152, v152, v145\n\t" \
    "v_mfma_f32_16x16x32_bf16 v[36:39], v[116:119], a[160:163], 0\n\tv_exp_f32 v146, v50\n\tv_mfma_f32_16x16x32_bf16 v[28:31], v[116:119], a[144:147], 0\n\tv_cvt_pk_bf16_f32 v96, v144, v145\n\t" \
    "v_add_f32 v152, v152, v146\n\tv_mfma_f32_16x16x32_bf16 v[20:23], v[116:119], a[128:131], v[164:167]\n\ts_waitcnt lgkmcnt(5)\n\tv_exp_f32 v147, v51\n\t" \
    "v_mfma_f32_16x16x32_bf16 v[16:19], v[120:123], a[132:135], v[16:19]\n\tds_read_b128 v[112:115], v177 offset:49152\n\tv_add_f32 v152, v152, v147\n\t" \
    "v_mfma_f32_16x16x32_bf16 v[24:27], v[120:123], a[148:151], v[24:27]\n\ts_add_u32 m0, %[lb], 40960\n\tv_exp_f32 v148, v52\n\tv_mfma_f32_16x16x32_bf16 v[32:35], v[120:123], a[164:167], v[32:35]\n\t" \
    "v_cvt_pk_bf16_f32 v97, v146, v147\n\tv_add_f32 v152, v152, v148\n\tv_mfma_f32_16x16x32_bf16 v[40:43], v[120:123], a[180:183], v[40:43]\n\ts_waitcnt lgkmcnt(5)\n\tv_exp_f32 v149, v53\n\t" \
    "v_mfma_f32_16x16x32_bf16 v[44:47], v[124:127], a[180:183], v[44:47]\n\tds_read_b128 v[116:119], v177 offset:50176\n\tbuffer_load_dwordx4 %[dv], %[rs], s62 offen sc1 lds\n\t" \
    "v_add_f32 v152, v152, v149\n\tv_mfma_f32_16x16x32_bf16 v[36:39], v[124:127], a[164:167], v[36:39]\n\tv_exp_f32 v150, v54\n\tv_mfma_f32_16x16x32_bf16 v[28:31], v[124:127], a[148:151], v[28:31]\n\t" \
    "v_cvt_pk_bf16_f32 v98, v148, v149\n\tv_add_f32 v152, v152, v150\n\tv_mfma_f32_16x16x32_bf16 v[20:23], v[124:127], a[132:135], v[20:23]\n\ts_waitcnt lgkmcnt(5)\n\tv_exp_f32 v151, v55\n\t" \
    "v_mfma_f32_16x16x32_bf16 v[16:19], v[128:131], a[136:139], v[16:19]\n\tds_read_b128 v[120:123], v177 offset:51200\n\tbuffer_load_dwordx4 %[dv], %[rs], s62 offen offset:1024 sc1 lds\n\t" \
    "v_add_f32 v152, v152, v151\n\tv_mfma_f32_16x16x32_bf16 v[24:27], v[128:131], a[152:155], v[24:27]\n\ts_add_u32 m0, %[lb], 81920\n\tv_exp_f32 v144, v56\n\t" \
    "v_mfma_f32_16x16x32_bf16 v[32:35], v[128:131], a[168:171], v[32:35]\n\ts_add_u32 s62, s62, 0x40000\n\tv_cvt_pk_bf16_f32 v99, v150, v151\n\tv_add_f32 v153, v153, v144\n\t" \
    "v_mfma_f32_16x16x32_bf16 v[40:43], v[128:131], a[184:187], v[40:43]\n\ts_waitcnt lgkmcnt(5)\n\tv_exp_f32 v145, v57\n\tv_mfma_f32_16x16x32_bf16 v[44:47], v[132:135], a[184:187], v[44:47]\n\t" \
    "ds_read_b128 v[124:127], v177 offset:52224\n\tbuffer_load_dwordx4 %[dv], %[rs], s63 offen sc1 lds\n\tv_add_f32 v153, v153, v145\n\t" \
    "v_mfma_f32_16x16x32_bf16 v[36:39], v[132:135], a[168:171], v[36:39]\n\tv_exp_f32 v146, v58\n\tv_mfma_f32_16x16x32_bf16 v[28:31], v[132:135], a[152:155], v[28:31]\n\t" \
    "v_cvt_pk_bf16_f32 v100, v144, v145\n\tv_add_f32 v153, v153, v146\n\tv_mfma_f32_16x16x32_bf16 v[20:23], v[132:135], a[136:139], v[20:23]\n\ts_waitcnt lgkmcnt(5)\n\tv_exp_f32 v147, v59\n\t" \
    "v_mfma_f32_16x16x32_bf16 v[16:19], v[136:139], a[140:143], v[16:19]\n\tds_read_b128 v[128:131], v177 offset:53248\n\tbuffer_load_dwordx4 %[dv], %[rs], s63 offen offset:1024 sc1 lds\n\t" \
    "v_add_f32 v153, v153, v147\n\tv_mfma_f32_16x16x32_bf16 v[24:27], v[136:139], a[156:159], v[24:27]\n\tv_exp_f32 v148, v60\n\tv_mfma_f32_16x16x32_bf16 v[32:35], v[136:139], a[172:175], v[32:35]\n\t" \
    "s_add_u32 s63, s63, 0x40000\n\tv_cvt_pk_bf16_f32 v101, v146, v147\n\tv_add_f32 v153, v153, v148\n\tv_mfma_f32_16x16x32_bf16 v[40:43], v[136:139], a[188:191], v[40:43]\n\ts_waitcnt lgkmcnt(5)\n\t" \
    "v_exp_f32 v149, v61\n\tv_mfma_f32_16x16x32_bf16 v[44:47], v[140:143], a[188:191], v[44:47]\n\tds_read_b128 v[132:135], v177 offset:54272\n\tv_add_f32 v153, v153, v149\n\t" \
    "v_mfma_f32_16x16x32_bf16 v[36:39], v[140:143], a[172:175], v[36:39]\n\tv_exp_f32 v150, v62\n\tv_mfma_f32_16x16x32_bf16 v[28:31], v[140:143], a[156:159], v[28:31]\n\t" \
    "v_cvt_pk_bf16_f32 v102, v148, v149\n\tv_add_f32 v153, v153, v150\n\tv_mfma_f32_16x16x32_bf16 v[20:23], v[140:143], a[140:143], v[20:23]\n\ts_waitcnt lgkmcnt(5)\n\tv_exp_f32 v151, v63\n\t" \
    "v_mfma_f32_16x16x32_bf16 a[0:3], v[112:115], v[80:83], a[0:3]\n\tds_read_b128 v[136:139], v177 offset:55296\n\tv_add_f32 v153, v153, v151\n\t" \
    "v_mfma_f32_16x16x32_bf16 a[32:35], v[112:115], v[84:87], a[32:35]\n\tv_exp_f32 v144, v64\n\tv_mfma_f32_16x16x32_bf16 a[64:67], v[112:115], v[88:91], a[64:67]\n\tv_cvt_pk_bf16_f32 v103, v150, v151\n\t" \
    "v_add_f32 v154, v154, v144\n\tv_mfma_f32_16x16x32_bf16 a[96:99], v[112:115], v[92:95], a[96:99]\n\ts_waitcnt lgkmcnt(5)\n\tv_exp_f32 v145, v65\n\t" \
    "v_mfma_f32_16x16x32_bf16 a[100:103], v[116:119], v[92:95], a[100:103]\n\tds_read_b128 v[140:143], v177 offset:56320\n\tv_add_f32 v154, v154, v145\n\t" \
    "v_mfma_f32_16x16x32_bf16 a[68:71], v[116:119], v[88:91], a[68:71]\n\tv_exp_f32 v146, v66\n\tv_mfma_f32_16x16x32_bf16 a[36:39], v[116:119], v[84:87], a[36:39]\n\tv_cvt_pk_bf16_f32 v104, v144, v145\n\t" \
    "v_add_f32 v154, v154, v146\n\tv_mfma_f32_16x16x32_bf16 a[4:7], v[116:119], v[80:83], a[4:7]\n\ts_waitcnt lgkmcnt(5)\n\tv_exp_f32 v147, v67\n\t" \
    "v_mfma_f32_16x16x32_bf16 a[8:11], v[120:123], v[80:83], a[8:11]\n\tds_read_b128 v[112:115], v176 offset:8192\n\tv_add_f32 v154, v154, v147\n\t" \
    "v_mfma_f32_16x16x32_bf16 a[40:43], v[120:123], v[84:87], a[40:43]\n\tv_exp_f32 v148, v68\n\tv_mfma_f32_16x16x32_bf16 a[72:75], v[120:123], v[88:91], a[72:75]\n\tv_cvt_pk_bf16_f32 v105, v146, v147\n\t" \
    "v_add_f32 v154, v154, v148\n\tv_mfma_f32_16x16x32_bf16 a[104:107], v[120:123], v[92:95], a[104:107]\n\ts_waitcnt lgkmcnt(5)\n\tv_exp_f32 v149, v69\n\t" \
    "v_mfma_f32_16x16x32_bf16 a[108:111], v[124:127], v[92:95], a[108:111]\n\tds_read_b128 v[116:119], v176 offset:9216\n\tv_add_f32 v154, v154, v149\n\t" \
    "v_mfma_f32_16x16x32_bf16 a[76:79], v[124:127], v[88:91], a[76:79]\n\tv_exp_f32 v150, v70\n\tv_mfma_f32_16x16x32_bf16 a[44:47], v[124:127], v[84:87], a[44:47]\n\tv_cvt_pk_bf16_f32 v106, v148, v149\n\t" \
    "v_add_f32 v154, v154, v150\n\tv_mfma_f32_16x16x32_bf16 a[12:15], v[124:127], v[80:83], a[12:15]\n\ts_waitcnt lgkmcnt(5)\n\tv_exp_f32 v151, v71\n\t" \
    "v_mfma_f32_16x16x32_bf16 a[16:19], v[128:131], v[80:83], a[16:19]\n\tds_read_b128 v[120:123], v176 offset:10240\n\tv_add_f32 v154, v154, v151\n\t" \
    "v_mfma_f32_16x16x32_bf16 a[48:51], v[128:131], v[84:87], a[48:51]\n\tv_exp_f32 v144, v72\n\tv_mfma_f32_16x16x32_bf16 a[80:83], v[128:131], v[88:91], a[80:83]\n\tv_cvt_pk_bf16_f32 v107, v150, v151\n\t" \
    "v_add_f32 v155, v155, v144\n\tv_mfma_f32_16x16x32_bf16 a[112:115], v[128:131], v[92:95], a[112:115]\n\ts_waitcnt lgkmcnt(5)\n\tv_exp_f32 v145, v73\n\t" \
    "v_mfma_f32_16x16x32_bf16 a[116:119], v[132:135], v[92:95], a[116:119]\n\tds_read_b128 v[124:127], v176 offset:11264\n\tv_add_f32 v155, v155, v145\n\t" \
    "v_mfma_f32_16x16x32_bf16 a[84:87], v[132:135], v[88:91], a[84:87]\n\tv_exp_f32 v146, v74\n\tv_mfma_f32_16x16x32_bf16 a[52:55], v[132:135], v[84:87], a[52:55]\n\tv_cvt_pk_bf16_f32 v108, v144, v145\n\t" \
    "v_add_f32 v155, v155, v146\n\tv_mfma_f32_16x16x32_bf16 a[20:23], v[132:135], v[80:83], a[20:23]\n\ts_waitcnt lgkmcnt(5)\n\tv_exp_f32 v147, v75\n\t" \
    "v_mfma_f32_16x16x32_bf16 a[24:27], v[136:139], v[80:83], a[24:27]\n\tds_read_b128 v[128:131], v176 offset:12288\n\tv_add_f32 v155, v155, v147\n\t" \
    "v_mfma_f32_16x16x32_bf16 a[56:59], v[136:139], v[84:87], a[56:59]\n\tv_exp_f32 v148, v76\n\tv_mfma_f32_16x16x32_bf16 a[88:91], v[136:139], v[88:91], a[88:91]\n\tv_cvt_pk_bf16_f32 v109, v146, v147\n\t" \
    "v_add_f32 v155, v155, v148\n\tv_mfma_f32_16x16x32_bf16 a[120:123], v[136:139], v[92:95], a[120:123]\n\ts_waitcnt lgkmcnt(5)\n\tv_exp_f32 v149, v77\n\t" \
    "v_mfma_f32_16x16x32_bf16 a[124:127], v[140:143], v[92:95], a[124:127]\n\tds_read_b128 v[132:135], v176 offset:13312\n\tv_add_f32 v155, v155, v149\n\t" \
    "v_mfma_f32_16x16x32_bf16 a[92:95], v[140:143], v[88:91], a[92:95]\n\tv_exp_f32 v150, v78\n\tv_mfma_f32_16x16x32_bf16 a[60:63], v[140:143], v[84:87], a[60:63]\n\tv_cvt_pk_bf16_f32 v110, v148, v149\n\t" \
    "v_add_f32 v155, v155, v150\n\tv_mfma_f32_16x16x32_bf16 a[28:31], v[140:143], v[80:83], a[28:31]\n\ts_waitcnt lgkmcnt(5)\n\tv_exp_f32 v151, v79\n\t3:\n" \
    "v_mfma_f32_16x16x32_bf16 v[48:51], v[112:115], a[128:131], v[168:171]\n\ts_waitcnt vmcnt(8)\n\ts_barrier\n\tds_read_b128 v[136:139], v176 offset:14336\n\tv_add_f32 v155, v155, v151\n\t" \
    "v_mfma_f32_16x16x32_bf16 v[56:59], v[112:115], a[144:147], 0\n\tv_cvt_pk_bf16_f32 v111, v150, v151\n\tv_mfma_f32_16x16x32_bf16 v[64:67], v[112:115], a[160:163], 0\n\ts_mov_b64 exec, %[em]\n\t" \
    "global_atomic_add v212, v208, v209, %[qu] sc0\n\tglobal_load_dword v213, v208, %[ca] sc1\n\ts_mov_b64 exec, -1\n\tv_exp_f32 v144, v16\n\t" \
    "v_mfma_f32_16x16x32_bf16 v[72:75], v[112:115], a[176:179], 0\n\ts_waitcnt lgkmcnt(5)\n\tv_exp_f32 v145, v17\n\tv_mfma_f32_16x16x32_bf16 v[76:79], v[116:119], a[176:179], 0\n\t" \
    "ds_read_b128 v[140:143], v176 offset:15360\n\tv_add_f32 v152, v152, v144\n\tv_mfma_f32_16x16x32_bf16 v[68:71], v[116:119], a[160:163], 0\n\tv_exp_f32 v146, v18\n\t" \
    "v_mfma_f32_16x16x32_bf16 v[60:63], v[116:119], a[144:147], 0\n\tv_add_f32 v152, v152, v145\n\tv_cvt_pk_bf16_f32 v80, v144, v145\n\t" \
    "v_mfma_f32_16x16x32_bf16 v[52:55], v[116:119], a[128:131], v[172:175]\n\ts_waitcnt lgkmcnt(5)\n\tv_exp_f32 v147, v19\n\tv_mfma_f32_16x16x32_bf16 v[48:51], v[120:123], a[132:135], v[48:51]\n\t" \
    "ds_read_b128 v[112:115], v177 offset:57344\n\tv_add_f32 v152, v152, v146\n\tv_mfma_f32_16x16x32_bf16 v[56:59], v[120:123], a[148:151], v[56:59]\n\ts_add_u32 m0, %[lb], 49152\n\tv_exp_f32 v148, v20\n\t" \
    "v_mfma_f32_16x16x32_bf16 v[64:67], v[120:123], a[164:167], v[64:67]\n\tv_add_f32 v152, v152, v147\n\tv_cvt_pk_bf16_f32 v81, v146, v147\n\t" \
    "v_mfma_f32_16x16x32_bf16 v[72:75], v[120:123], a[180:183], v[72:75]\n\ts_waitcnt lgkmcnt(5)\n\tv_exp_f32 v149, v21\n\tv_mfma_f32_16x16x32_bf16 v[76:79], v[124:127], a[180:183], v[76:79]\n\t" \
    "ds_read_b128 v[116:119], v177 offset:58368\n\tbuffer_load_dwordx4 %[dv], %[rs], s62 offen sc1 lds\n\tv_add_f32 v152, v152, v148\n\t" \
    "v_mfma_f32_16x16x32_bf16 v[68:71], v[124:127], a[164:167], v[68:71]\n\tv_exp_f32 v150, v22\n\tv_mfma_f32_16x16x32_bf16 v[60:63], v[124:127], a[148:151], v[60:63]\n\tv_add_f32 v152, v152, v149\n\t" \
    "v_cvt_pk_bf16_f32 v82, v148, v149\n\tv_mfma_f32_16x16x32_bf16 v[52:55], v[124:127], a[132:135], v[52:55]\n\ts_waitcnt lgkmcnt(5)\n\tv_exp_f32 v151, v23\n\t" \
    "v_mfma_f32_16x16x32_bf16 v[48:51], v[128:131], a[136:139], v[48:51]\n\tds_read_b128 v[120:123], v177 offset:59392\n\tbuffer_load_dwordx4 %[dv], %[rs], s62 offen offset:1024 sc1 lds\n\t" \
    "v_add_f32 v152, v152, v150\n\tv_mfma_f32_16x16x32_bf16 v[56:59], v[128:131], a[152:155], v[56:59]\n\ts_add_u32 m0, %[lb], 90112\n\tv_exp_f32 v144, v24\n\t" \
    "v_mfma_f32_16x16x32_bf16 v[64:67], v[128:131], a[168:171], v[64:67]\n\ts_add_u32 s62, s62, 0x40000\n\tv_add_f32 v152, v152, v151\n\tv_cvt_pk_bf16_f32 v83, v150, v151\n\t" \
    "v_mfma_f32_16x16x32_bf16 v[72:75], v[128:131], a[184:187], v[72:75]\n\ts_waitcnt lgkmcnt(5)\n\tv_exp_f32 v145, v25\n\tv_mfma_f32_16x16x32_bf16 v[76:79], v[132:135], a[184:187], v[76:79]\n\t" \
    "ds_read_b128 v[124:127], v177 offset:60416\n\tbuffer_load_dwordx4 %[dv], %[rs], s63 offen sc1 lds\n\tv_add_f32 v153, v153, v144\n\t" \
    "v_mfma_f32_16x16x32_bf16 v[68:71], v[132:135], a[168:171], v[68:71]\n\tv_exp_f32 v146, v26\n\tv_mfma_f32_16x16x32_bf16 v[60:63], v[132:135], a[152:155], v[60:63]\n\tv_add_f32 v153, v153, v145\n\t" \
    "v_cvt_pk_bf16_f32 v84, v144, v145\n\tv_mfma_f32_16x16x32_bf16 v[52:55], v[132:135], a[136:139], v[52:55]\n\ts_waitcnt lgkmcnt(5)\n\tv_exp_f32 v147, v27\n\t" \
    "v_mfma_f32_16x16x32_bf16 v[48:51], v[136:139], a[140:143], v[48:51]\n\tds_read_b128 v[128:131], v177 offset:61440\n\tbuffer_load_dwordx4 %[dv], %[rs], s63 offen offset:1024 sc1 lds\n\t" \
    "v_add_f32 v153, v153, v146\n\tv_mfma_f32_16x16x32_bf16 v[56:59], v[136:139], a[156:159], v[56:59]\n\tv_exp_f32 v148, v28\n\tv_mfma_f32_16x16x32_bf16 v[64:67], v[136:139], a[172:175], v[64:67]\n\t" \
    "s_add_u32 s63, s63, 0x40000\n\tv_add_f32 v153, v153, v147\n\tv_cvt_pk_bf16_f32 v85, v146, v147\n\tv_mfma_f32_16x16x32_bf16 v[72:75], v[136:139], a[188:191], v[72:75]\n\ts_waitcnt lgkmcnt(5)\n\t" \
    "v_exp_f32 v149, v29\n\tv_mfma_f32_16x16x32_bf16 v[76:79], v[140:143], a[188:191], v[76:79]\n\tds_read_b128 v[132:135], v177 offset:62464\n\tv_add_f32 v153, v153, v148\n\t" \
    "v_mfma_f32_16x16x32_bf16 v[68:71], v[140:143], a[172:175], v[68:71]\n\tv_exp_f32 v150, v30\n\tv_mfma_f32_16x16x32_bf16 v[60:63], v[140:143], a[156:159], v[60:63]\n\tv_add_f32 v153, v153, v149\n\t" \
    "v_cvt_pk_bf16_f32 v86, v148, v149\n\tv_mfma_f32_16x16x32_bf16 v[52:55], v[140:143], a[140:143], v[52:55]\n\ts_waitcnt lgkmcnt(5)\n\tv_exp_f32 v151, v31\n\t" \
    "v_mfma_f32_16x16x32_bf16 a[0:3], v[112:115], v[96:99], a[0:3]\n\tds_read_b128 v[136:139], v177 offset:63488\n\tv_add_f32 v153, v153, v150\n\t" \
    "v_mfma_f32_16x16x32_bf16 a[32:35], v[112:115], v[100:103], a[32:35]\n\tv_exp_f32 v144, v32\n\tv_mfma_f32_16x16x32_bf16 a[64:67], v[112:115], v[104:107], a[64:67]\n\tv_add_f32 v153, v153, v151\n\t" \
    "v_cvt_pk_bf16_f32 v87, v150, v151\n\tv_mfma_f32_16x16x32_bf16 a[96:99], v[112:115], v[108:111], a[96:99]\n\ts_waitcnt lgkmcnt(5)\n\tv_exp_f32 v145, v33\n\t" \
    "v_mfma_f32_16x16x32_bf16 a[100:103], v[116:119], v[108:111], a[100:103]\n\tds_read_b128 v[140:143], v177 offset:64512\n\tv_add_f32 v154, v154, v144\n\t" \
    "v_mfma_f32_16x16x32_bf16 a[68:71], v[116:119], v[104:107], a[68:71]\n\tv_exp_f32 v146, v34\n\tv_mfma_f32_16x16x32_bf16 a[36:39], v[116:119], v[100:103], a[36:39]\n\tv_add_f32 v154, v154, v145\n\t" \
    "v_cvt_pk_bf16_f32 v88, v144, v145\n\tv_mfma_f32_16x16x32_bf16 a[4:7], v[116:119], v[96:99], a[4:7]\n\ts_waitcnt lgkmcnt(5)\n\tv_exp_f32 v147, v35\n\t" \
    "v_mfma_f32_16x16x32_bf16 a[8:11], v[120:123], v[96:99], a[8:11]\n\tds_read_b128 v[112:115], v176 offset:16384\n\tv_add_f32 v154, v154, v146\n\t" \
    "v_mfma_f32_16x16x32_bf16 a[40:43], v[120:123], v[100:103], a[40:43]\n\tv_exp_f32 v148, v36\n\tv_mfma_f32_16x16x32_bf16 a[72:75], v[120:123], v[104:107], a[72:75]\n\tv_add_f32 v154, v154, v147\n\t" \
    "v_cvt_pk_bf16_f32 v89, v146, v147\n\tv_mfma_f32_16x16x32_bf16 a[104:107], v[120:123], v[108:111], a[104:107]\n\ts_waitcnt lgkmcnt(5)\n\tv_exp_f32 v149, v37\n\t" \
    "v_mfma_f32_16x16x32_bf16 a[108:111], v[124:127], v[108:111], a[108:111]\n\tds_read_b128 v[116:119], v176 offset:17408\n\tv_add_f32 v154, v154, v148\n\t" \
    "v_mfma_f32_16x16x32_bf16 a[76:79], v[124:127], v[104:107], a[76:79]\n\tv_exp_f32 v150, v38\n\tv_mfma_f32_16x16x32_bf16 a[44:47], v[124:127], v[100:103], a[44:47]\n\tv_add_f32 v154, v154, v149\n\t" \
    "v_cvt_pk_bf16_f32 v90, v148, v149\n\tv_mfma_f32_16x16x32_bf16 a[12:15], v[124:127], v[96:99], a[12:15]\n\ts_waitcnt lgkmcnt(5)\n\tv_exp_f32 v151, v39\n\t" \
    "v_mfma_f32_16x16x32_bf16 a[16:19], v[128:131], v[96:99], a[16:19]\n\tds_read_b128 v[120:123], v176 offset:18432\n\tv_add_f32 v154, v154, v150\n\t" \
    "v_mfma_f32_16x16x32_bf16 a[48:51], v[128:131], v[100:103], a[48:51]\n\tv_exp_f32 v144, v40\n\tv_mfma_f32_16x16x32_bf16 a[80:83], v[128:131], v[104:107], a[80:83]\n\tv_add_f32 v154, v154, v151\n\t" \
    "v_cvt_pk_bf16_f32 v91, v150, v151\n\tv_mfma_f32_16x16x32_bf16 a[112:115], v[128:131], v[108:111], a[112:115]\n\ts_waitcnt lgkmcnt(5)\n\tv_exp_f32 v145, v41\n\t" \
    "v_mfma_f32_16x16x32_bf16 a[116:119], v[132:135], v[108:111], a[116:119]\n\tds_read_b128 v[124:127], v176 offset:19456\n\tv_add_f32 v155, v155, v144\n\t" \
    "v_mfma_f32_16x16x32_bf16 a[84:87], v[132:135], v[104:107], a[84:87]\n\tv_exp_f32 v146, v42\n\tv_mfma_f32_16x16x32_bf16 a[52:55], v[132:135], v[100:103], a[52:55]\n\tv_add_f32 v155, v155, v145\n\t" \
    "v_cvt_pk_bf16_f32 v92, v144, v145\n\tv_mfma_f32_16x16x32_bf16 a[20:23], v[132:135], v[96:99], a[20:23]\n\ts_waitcnt lgkmcnt(5)\n\tv_exp_f32 v147, v43\n\t" \
    "v_mfma_f32_16x16x32_bf16 a[24:27], v[136:139], v[96:99], a[24:27]\n\tds_read_b128 v[128:131], v176 offset:20480\n\tv_add_f32 v155, v155, v146\n\t" \
    "v_mfma_f32_16x16x32_bf16 a[56:59], v[136:139], v[100:103], a[56:59]\n\tv_exp_f32 v148, v44\n\tv_mfma_f32_16x16x32_bf16 a[88:91], v[136:139], v[104:107], a[88:91]\n\tv_add_f32 v155, v155, v147\n\t" \
    "v_cvt_pk_bf16_f32 v93, v146, v147\n\tv_mfma_f32_16x16x32_bf16 a[120:123], v[136:139], v[108:111], a[120:123]\n\ts_waitcnt lgkmcnt(5)\n\tv_exp_f32 v149, v45\n\t" \
    "v_mfma_f32_16x16x32_bf16 a[124:127], v[140:143], v[108:111], a[124:127]\n\tds_read_b128 v[132:135], v176 offset:21504\n\tv_add_f32 v155, v155, v148\n\t" \
    "v_mfma_f32_16x16x32_bf16 a[92:95], v[140:143], v[104:107], a[92:95]\n\tv_exp_f32 v150, v46\n\tv_mfma_f32_16x16x32_bf16 a[60:63], v[140:143], v[100:103], a[60:63]\n\tv_add_f32 v155, v155, v149\n\t" \
    "v_cvt_pk_bf16_f32 v94, v148, v149\n\tv_mfma_f32_16x16x32_bf16 a[28:31], v[140:143], v[96:99], a[28:31]\n\ts_waitcnt lgkmcnt(5)\n\tv_exp_f32 v151, v47\n\tv_add_f32 v155, v155, v150\n\ts_nop 0\n\t" \
    "v_add_f32 v155, v155, v151\n\tv_cvt_pk_bf16_f32 v95, v150, v151\n\tv_add_u32 v177, 0x10000, %[ka]\n\tv_mfma_f32_16x16x32_bf16 v[24:27], v[112:115], a[144:147], v[160:163]\n\t" \
    "ds_read_b128 v[136:139], v176 offset:22528\n\tv_mfma_f32_16x16x32_bf16 v[32:35], v[112:115], a[160:163], 0\n\tv_exp_f32 v144, v48\n\tv_mfma_f32_16x16x32_bf16 v[40:43], v[112:115], a[176:179], 0\n\t" \
    "s_waitcnt lgkmcnt(5)\n\tv_exp_f32 v145, v49\n\tv_mfma_f32_16x16x32_bf16 v[44:47], v[116:119], a[176:179], 0\n\tds_read_b128 v[140:143], v176 offset:23552\n\tv_add_f32 v152, v152, v144\n\t" \
    "v_mfma_f32_16x16x32_bf16 v[36:39], v[116:119], a[160:163], 0\n\tv_exp_f32 v146, v50\n\tv_mfma_f32_16x16x32_bf16 v[28:31], v[116:119], a[144:147], v[164:167]\n\ts_waitcnt lgkmcnt(5)\n\t" \
    "v_add_f32 v152, v152, v145\n\tv_cvt_pk_bf16_f32 v96, v144, v145\n\tv_mfma_f32_16x16x32_bf16 v[24:27], v[120:123], a[148:151], v[24:27]\n\tds_read_b128 v[112:115], v177\n\t" \
    "v_add_f32 v152, v152, v146\n\tv_mfma_f32_16x16x32_bf16 v[32:35], v[120:123], a[164:167], v[32:35]\n\tv_exp_f32 v147, v51\n\tv_mfma_f32_16x16x32_bf16 v[40:43], v[120:123], a[180:183], v[40:43]\n\t" \
    "s_waitcnt lgkmcnt(5)\n\tv_exp_f32 v148, v52\n\tv_mfma_f32_16x16x32_bf16 v[44:47], v[124:127], a[180:183], v[44:47]\n\ts_add_u32 m0, %[lb], 57344\n\tds_read_b128 v[116:119], v177 offset:1024\n\t" \
    "v_add_f32 v152, v152, v147\n\tv_mfma_f32_16x16x32_bf16 v[36:39], v[124:127], a[164:167], v[36:39]\n\tv_cvt_pk_bf16_f32 v97, v146, v147\n\tv_add_f32 v152, v152, v148\n\t" \
    "v_mfma_f32_16x16x32_bf16 v[28:31], v[124:127], a[148:151], v[28:31]\n\ts_waitcnt lgkmcnt(5)\n\tv_exp_f32 v149, v53\n\tv_mfma_f32_16x16x32_bf16 v[24:27], v[128:131], a[152:155], v[24:27]\n\t" \
    "ds_read_b128 v[120:123], v177 offset:2048\n\tbuffer_load_dwordx4 %[dv], %[rs], s62 offen sc1 lds\n\tv_add_f32 v152, v152, v149\n\t" \
    "v_mfma_f32_16x16x32_bf16 v[32:35], v[128:131], a[168:171], v[32:35]\n\tv_exp_f32 v150, v54\n\tv_mfma_f32_16x16x32_bf16 v[40:43], v[128:131], a[184:187], v[40:43]\n\ts_waitcnt lgkmcnt(5)\n\t" \
    "v_cvt_pk_bf16_f32 v98, v148, v149\n\tv_add_f32 v152, v152, v150\n\tv_mfma_f32_16x16x32_bf16 v[44:47], v[132:135], a[184:187], v[44:47]\n\tds_read_b128 v[124:127], v177 offset:3072\n\t" \
    "v_mfma_f32_16x16x32_bf16 v[36:39], v[132:135], a[168:171], v[36:39]\n\tbuffer_load_dwordx4 %[dv], %[rs], s62 offen offset:1024 sc1 lds\n\tv_exp_f32 v151, v55\n\t" \
    "v_mfma_f32_16x16x32_bf16 v[28:31], v[132:135], a[152:155], v[28:31]\n\ts_waitcnt lgkmcnt(5)\n\ts_add_u32 m0, %[lb], 98304\n\tv_exp_f32 v144, v56\n\t" \
    "v_mfma_f32_16x16x32_bf16 v[24:27], v[136:139], a[156:159], v[24:27]\n\ts_add_u32 s62, s62, 0x40000\n\tds_read_b128 v[128:131], v177 offset:4096\n\tv_add_f32 v152, v152, v151\n\t" \
    "v_mfma_f32_16x16x32_bf16 v[32:35], v[136:139], a[172:175], v[32:35]\n\tv_cvt_pk_bf16_f32 v99, v150, v151\n\tv_add_f32 v153, v153, v144\n\t" \
    "v_mfma_f32_16x16x32_bf16 v[40:43], v[136:139], a[188:191], v[40:43]\n\ts_waitcnt lgkmcnt(5)\n\tbuffer_load_dwordx4 %[dv], %[rs], s63 offen sc1 lds\n\tv_exp_f32 v145, v57\n\t" \
    "v_mfma_f32_16x16x32_bf16 v[44:47], v[140:143], a[188:191], v[44:47]\n\tds_read_b128 v[132:135], v177 offset:5120\n\tv_add_f32 v153, v153, v145\n\t" \
    "v_mfma_f32_16x16x32_bf16 v[36:39], v[140:143], a[172:175], v[36:39]\n\tv_exp_f32 v146, v58\n\tv_mfma_f32_16x16x32_bf16 v[28:31], v[140:143], a[156:159], v[28:31]\n\ts_waitcnt lgkmcnt(5)\n\t" \
    "v_cvt_pk_bf16_f32 v100, v144, v145\n\tv_add_f32 v153, v153, v146\n\tv_mfma_f32_16x16x32_bf16 a[0:3], v[112:115], v[80:83], a[0:3]\n\tds_read_b128 v[136:139], v177 offset:6144\n\t" \
    "buffer_load_dwordx4 %[dv], %[rs], s63 offen offset:1024 sc1 lds\n\tv_mfma_f32_16x16x32_bf16 a[32:35], v[112:115], v[84:87], a[32:35]\n\tv_exp_f32 v147, v59\n\t" \
    "v_mfma_f32_16x16x32_bf16 a[64:67], v[112:115], v[88:91], a[64:67]\n\ts_add_u32 s63, s63, 0x40000\n\tv_exp_f32 v148, v60\n\tv_mfma_f32_16x16x32_bf16 a[96:99], v[112:115], v[92:95], a[96:99]\n\t" \
    "s_waitcnt lgkmcnt(5)\n\tv_add_f32 v153, v153, v147\n\tv_cvt_pk_bf16_f32 v101, v146, v147\n\tv_mfma_f32_16x16x32_bf16 a[100:103], v[116:119], v[92:95], a[100:103]\n\t" \
    "ds_read_b128 v[140:143], v177 offset:7168\n\tv_add_f32 v153, v153, v148\n\tv_mfma_f32_16x16x32_bf16 a[68:71], v[116:119], v[88:91], a[68:71]\n\tv_exp_f32 v149, v61\n\t" \
    "v_mfma_f32_16x16x32_bf16 a[36:39], v[116:119], v[84:87], a[36:39]\n\tv_exp_f32 v150, v62\n\tv_mfma_f32_16x16x32_bf16 a[4:7], v[116:119], v[80:83], a[4:7]\n\ts_waitcnt lgkmcnt(5)\n\t" \
    "v_add_f32 v153, v153, v149\n\tv_cvt_pk_bf16_f32 v102, v148, v149\n\tv_mfma_f32_16x16x32_bf16 a[8:11], v[120:123], v[80:83], a[8:11]\n\tds_read_b128 v[112:115], v176 offset:24576\n\t" \
    "v_add_f32 v153, v153, v150\n\tv_mfma_f32_16x16x32_bf16 a[40:43], v[120:123], v[84:87], a[40:43]\n\tv_exp_f32 v151, v63\n\tv_mfma_f32_16x16x32_bf16 a[72:75], v[120:123], v[88:91], a[72:75]\n\t" \
    "v_exp_f32 v144, v64\n\tv_mfma_f32_16x16x32_bf16 a[104:107], v[120:123], v[92:95], a[104:107]\n\ts_waitcnt lgkmcnt(5)\n\tv_add_f32 v153, v153, v151\n\tv_cvt_pk_bf16_f32 v103, v150, v151\n\t" \
    "v_mfma_f32_16x16x32_bf16 a[108:111], v[124:127], v[92:95], a[108:111]\n\tds_read_b128 v[116:119], v176 offset:25600\n\tv_add_f32 v154, v154, v144\n\t" \
    "v_mfma_f32_16x16x32_bf16 a[76:79], v[124:127], v[88:91], a[76:79]\n\tv_exp_f32 v145, v65\n\tv_mfma_f32_16x16x32_bf16 a[44:47], v[124:127], v[84:87], a[44:47]\n\tv_exp_f32 v146, v66\n\t" \
    "v_mfma_f32_16x16x32_bf16 a[12:15], v[124:127], v[80:83], a[12:15]\n\ts_waitcnt lgkmcnt(5)\n\tv_add_f32 v154, v154, v145\n\tv_cvt_pk_bf16_f32 v104, v144, v145\n\t" \
    "v_mfma_f32_16x16x32_bf16 a[16:19], v[128:131], v[80:83], a[16:19]\n\tds_read_b128 v[120:123], v176 offset:26624\n\tv_add_f32 v154, v154, v146\n\t" \
    "v_mfma_f32_16x16x32_bf16 a[48:51], v[128:131], v[84:87], a[48:51]\n\tv_exp_f32 v147, v67\n\tv_mfma_f32_16x16x32_bf16 a[80:83], v[128:131], v[88:91], a[80:83]\n\tv_exp_f32 v148, v68\n\t" \
    "v_mfma_f32_16x16x32_bf16 a[112:115], v[128:131], v[92:95], a[112:115]\n\ts_waitcnt lgkmcnt(5)\n\tv_add_f32 v154, v154, v147\n\tv_cvt_pk_bf16_f32 v105, v146, v147\n\t" \
    "v_mfma_f32_16x16x32_bf16 a[116:119], v[132:135], v[92:95], a[116:119]\n\tds_read_b128 v[124:127], v176 offset:27648\n\tv_add_f32 v154, v154, v148\n\t" \
    "v_mfma_f32_16x16x32_bf16 a[84:87], v[132:135], v[88:91], a[84:87]\n\tv_exp_f32 v149, v69\n\tv_mfma_f32_16x16x32_bf16 a[52:55], v[132:135], v[84:87], a[52:55]\n\tv_exp_f32 v150, v70\n\t" \
    "v_mfma_f32_16x16x32_bf16 a[20:23], v[132:135], v[80:83], a[20:23]\n\ts_waitcnt lgkmcnt(5)\n\tv_add_f32 v154, v154, v149\n\tv_cvt_pk_bf16_f32 v106, v148, v149\n\t" \
    "v_mfma_f32_16x16x32_bf16 a[24:27], v[136:139], v[80:83], a[24:27]\n\tds_read_b128 v[128:131], v176 offset:28672\n\tv_add_f32 v154, v154, v150\n\t" \
    "v_mfma_f32_16x16x32_bf16 a[56:59], v[136:139], v[84:87], a[56:59]\n\tv_exp_f32 v151, v71\n\tv_mfma_f32_16x16x32_bf16 a[88:91], v[136:139], v[88:91], a[88:91]\n\tv_exp_f32 v144, v72\n\t" \
    "v_mfma_f32_16x16x32_bf16 a[120:123], v[136:139], v[92:95], a[120:123]\n\ts_waitcnt lgkmcnt(5)\n\tv_add_f32 v154, v154, v151\n\tv_cvt_pk_bf16_f32 v107, v150, v151\n\t" \
    "v_mfma_f32_16x16x32_bf16 a[124:127], v[140:143], v[92:95], a[124:127]\n\tds_read_b128 v[132:135], v176 offset:29696\n\tv_add_f32 v155, v155, v144\n\t" \
    "v_mfma_f32_16x16x32_bf16 a[92:95], v[140:143], v[88:91], a[92:95]\n\tv_exp_f32 v145, v73\n\tv_mfma_f32_16x16x32_bf16 a[60:63], v[140:143], v[84:87], a[60:63]\n\tv_exp_f32 v146, v74\n\t" \
    "v_mfma_f32_16x16x32_bf16 a[28:31], v[140:143], v[80:83], a[28:31]\n\ts_waitcnt lgkmcnt(5)\n\tv_add_f32 v155, v155, v145\n\tv_cvt_pk_bf16_f32 v108, v144, v145\n\tv_exp_f32 v147, v75\n\t" \
    "v_add_f32 v155, v155, v146\n\tv_exp_f32 v148, v76\n\tv_add_f32 v155, v155, v147\n\tv_cvt_pk_bf16_f32 v109, v146, v147\n\tv_exp_f32 v149, v77\n\tv_add_f32 v155, v155, v148\n\tv_exp_f32 v150, v78\n\t" \
    "v_add_f32 v155, v155, v149\n\tv_cvt_pk_bf16_f32 v110, v148, v149\n\tv_exp_f32 v151, v79\n\tv_mfma_f32_16x16x32_bf16 v[56:59], v[112:115], a[144:147], v[168:171]\n\ts_waitcnt vmcnt(8)\n\ts_barrier\n\t" \
    "ds_read_b128 v[136:139], v176 offset:30720\n\tv_add_f32 v155, v155, v150\n\tv_mfma_f32_16x16x32_bf16 v[64:67], v[112:115], a[160:163], 0\n\ts_add_u32 m0, %[lb], 106496\n\ts_nop 0\n\t" \
    "v_add_f32 v155, v155, v151\n\tv_mfma_f32_16x16x32_bf16 v[72:75], v[112:115], a[176:179], 0\n\ts_waitcnt lgkmcnt(5)\n\tv_cvt_pk_bf16_f32 v111, v150, v151\n\t" \
    "v_mfma_f32_16x16x32_bf16 v[76:79], v[116:119], a[176:179], 0\n\tds_read_b128 v[140:143], v176 offset:31744\n\tv_mfma_f32_16x16x32_bf16 v[68:71], v[116:119], a[160:163], 0\n\t" \
    "buffer_load_dwordx4 %[dv], %[rs], s63 offen sc1 lds\n\tv_exp_f32 v144, v24\n\tv_mfma_f32_16x16x32_bf16 v[60:63], v[116:119], a[144:147], v[172:175]\n\ts_waitcnt lgkmcnt(5)\n\tv_exp_f32 v145, v25\n\t" \
    "v_mfma_f32_16x16x32_bf16 v[56:59], v[120:123], a[148:151], v[56:59]\n\tds_read_b128 v[112:115], v177 offset:8192\n\tv_add_f32 v153, v153, v144\n\t" \
    "v_mfma_f32_16x16x32_bf16 v[64:67], v[120:123], a[164:167], v[64:67]\n\tv_exp_f32 v146, v26\n\tv_mfma_f32_16x16x32_bf16 v[72:75], v[120:123], a[180:183], v[72:75]\n\ts_waitcnt lgkmcnt(5)\n\t" \
    "buffer_load_dwordx4 %[dv], %[rs], s63 offen offset:1024 sc1 lds\n\tv_add_f32 v153, v153, v145\n\tv_cvt_pk_bf16_f32 v84, v144, v145\n\t" \
    "v_mfma_f32_16x16x32_bf16 v[76:79], v[124:127], a[180:183], v[76:79]\n\tds_read_b128 v[116:119], v177 offset:9216\n\tv_add_f32 v153, v153, v146\n\t" \
    "v_mfma_f32_16x16x32_bf16 v[68:71], v[124:127], a[164:167], v[68:71]\n\ts_add_u32 s63, s63, 0x40000\n\tv_exp_f32 v147, v27\n\tv_mfma_f32_16x16x32_bf16 v[60:63], v[124:127], a[148:151], v[60:63]\n\t" \
    "s_waitcnt lgkmcnt(5)\n\tv_exp_f32 v148, v28\n\tv_mfma_f32_16x16x32_bf16 v[56:59], v[128:131], a[152:155], v[56:59]\n\tds_read_b128 v[120:123], v177 offset:10240\n\tv_add_f32 v153, v153, v147\n\t" \
    "v_mfma_f32_16x16x32_bf16 v[64:67], v[128:131], a[168:171], v[64:67]\n\tv_cvt_pk_bf16_f32 v85, v146, v147\n\tv_add_f32 v153, v153, v148\n\t" \
    "v_mfma_f32_16x16x32_bf16 v[72:75], v[128:131], a[184:187], v[72:75]\n\ts_waitcnt lgkmcnt(5)\n\tv_exp_f32 v149, v29\n\tv_mfma_f32_16x16x32_bf16 v[76:79], v[132:135], a[184:187], v[76:79]\n\t" \
    "ds_read_b128 v[124:127], v177 offset:11264\n\tv_add_f32 v153, v153, v149\n\tv_mfma_f32_16x16x32_bf16 v[68:71], v[132:135], a[168:171], v[68:71]\n\tv_exp_f32 v150, v30\n\t" \
    "v_mfma_f32_16x16x32_bf16 v[60:63], v[132:135], a[152:155], v[60:63]\n\ts_waitcnt lgkmcnt(5)\n\tv_cvt_pk_bf16_f32 v86, v148, v149\n\tv_add_f32 v153, v153, v150\n\t" \
    "v_mfma_f32_16x16x32_bf16 v[56:59], v[136:139], a[156:159], v[56:59]\n\tds_read_b128 v[128:131], v177 offset:12288\n\tv_mfma_f32_16x16x32_bf16 v[64:67], v[136:139], a[172:175], v[64:67]\n\t" \
    "v_exp_f32 v151, v31\n\tv_mfma_f32_16x16x32_bf16 v[72:75], v[136:139], a[188:191], v[72:75]\n\ts_waitcnt lgkmcnt(5)\n\tv_exp_f32 v144, v32\n\t" \
    "v_mfma_f32_16x16x32_bf16 v[76:79], v[140:143], a[188:191], v[76:79]\n\tds_read_b128 v[132:135], v177 offset:13312\n\tv_add_f32 v153, v153, v151\n\t" \
    "v_mfma_f32_16x16x32_bf16 v[68:71], v[140:143], a[172:175], v[68:71]\n\tv_cvt_pk_bf16_f32 v87, v150, v151\n\tv_add_f32 v154, v154, v144\n\t" \
    "v_mfma_f32_16x16x32_bf16 v[60:63], v[140:143], a[156:159], v[60:63]\n\ts_waitcnt lgkmcnt(5)\n\tv_exp_f32 v145, v33\n\tv_mfma_f32_16x16x32_bf16 a[0:3], v[112:115], v[96:99], a[0:3]\n\t" \
    "ds_read_b128 v[136:139], v177 offset:14336\n\tv_add_f32 v154, v154, v145\n\tv_mfma_f32_16x16x32_bf16 a[32:35], v[112:115], v[100:103], a[32:35]\n\tv_exp_f32 v146, v34\n\t" \
    "v_mfma_f32_16x16x32_bf16 a[64:67], v[112:115], v[104:107], a[64:67]\n\tv_cvt_pk_bf16_f32 v88, v144, v145\n\tv_add_f32 v154, v154, v146\n\t" \
    "v_mfma_f32_16x16x32_bf16 a[96:99], v[112:115], v[108:111], a[96:99]\n\ts_waitcnt lgkmcnt(5)\n\tv_exp_f32 v147, v35\n\tv_mfma_f32_16x16x32_bf16 a[100:103], v[116:119], v[108:111], a[100:103]\n\t" \
    "ds_read_b128 v[140:143], v177 offset:15360\n\tv_add_f32 v154, v154, v147\n\tv_mfma_f32_16x16x32_bf16 a[68:71], v[116:119], v[104:107], a[68:71]\n\tv_exp_f32 v148, v36\n\t" \
    "v_mfma_f32_16x16x32_bf16 a[36:39], v[116:119], v[100:103], a[36:39]\n\tv_cvt_pk_bf16_f32 v89, v146, v147\n\tv_add_f32 v154, v154, v148\n\t" \
    "v_mfma_f32_16x16x32_bf16 a[4:7], v[116:119], v[96:99], a[4:7]\n\ts_waitcnt lgkmcnt(5)\n\tv_exp_f32 v149, v37\n\tv_mfma_f32_16x16x32_bf16 a[8:11], v[120:123], v[96:99], a[8:11]\n\t" \
    "ds_read_b128 v[112:115], v176 offset:32768\n\tv_add_f32 v154, v154, v149\n\tv_mfma_f32_16x16x32_bf16 a[40:43], v[120:123], v[100:103], a[40:43]\n\tv_exp_f32 v150, v38\n\t" \
    "v_mfma_f32_16x16x32_bf16 a[72:75], v[120:123], v[104:107], a[72:75]\n\tv_cvt_pk_bf16_f32 v90, v148, v149\n\tv_add_f32 v154, v154, v150\n\t" \
    "v_mfma_f32_16x16x32_bf16 a[104:107], v[120:123], v[108:111], a[104:107]\n\ts_waitcnt lgkmcnt(5)\n\tv_exp_f32 v151, v39\n\tv_mfma_f32_16x16x32_bf16 a[108:111], v[124:127], v[108:111], a[108:111]\n\t" \
    "ds_read_b128 v[116:119], v176 offset:33792\n\tv_add_f32 v154, v154, v151\n\tv_mfma_f32_16x16x32_bf16 a[76:79], v[124:127], v[104:107], a[76:79]\n\tv_exp_f32 v144, v40\n\t" \
    "v_mfma_f32_16x16x32_bf16 a[44:47], v[124:127], v[100:103], a[44:47]\n\tv_cvt_pk_bf16_f32 v91, v150, v151\n\tv_add_f32 v155, v155, v144\n\t" \
    "v_mfma_f32_16x16x32_bf16 a[12:15], v[124:127], v[96:99], a[12:15]\n\ts_waitcnt lgkmcnt(5)\n\tv_exp_f32 v145, v41\n\tv_mfma_f32_16x16x32_bf16 a[16:19], v[128:131], v[96:99], a[16:19]\n\t" \
    "ds_read_b128 v[120:123], v176 offset:34816\n\tv_add_f32 v155, v155, v145\n\tv_mfma_f32_16x16x32_bf16 a[48:51], v[128:131], v[100:103], a[48:51]\n\tv_exp_f32 v146, v42\n\t" \
    "v_mfma_f32_16x16x32_bf16 a[80:83], v[128:131], v[104:107], a[80:83]\n\tv_cvt_pk_bf16_f32 v92, v144, v145\n\tv_add_f32 v155, v155, v146\n\t" \
    "v_mfma_f32_16x16x32_bf16 a[112:115], v[128:131], v[108:111], a[112:115]\n\ts_waitcnt lgkmcnt(5)\n\tv_exp_f32 v147, v43\n\tv_mfma_f32_16x16x32_bf16 a[116:119], v[132:135], v[108:111], a[116:119]\n\t" \
    "ds_read_b128 v[124:127], v176 offset:35840\n\tv_add_f32 v155, v155, v147\n\tv_mfma_f32_16x16x32_bf16 a[84:87], v[132:135], v[104:107], a[84:87]\n\tv_exp_f32 v148, v44\n\t" \
    "v_mfma_f32_16x16x32_bf16 a[52:55], v[132:135], v[100:103], a[52:55]\n\tv_cvt_pk_bf16_f32 v93, v146, v147\n\tv_add_f32 v155, v155, v148\n\t" \
    "v_mfma_f32_16x16x32_bf16 a[20:23], v[132:135], v[96:99], a[20:23]\n\ts_waitcnt lgkmcnt(5)\n\tv_exp_f32 v149, v45\n\tv_mfma_f32_16x16x32_bf16 a[24:27], v[136:139], v[96:99], a[24:27]\n\t" \
    "ds_read_b128 v[128:131], v176 offset:36864\n\tv_add_f32 v155, v155, v149\n\tv_mfma_f32_16x16x32_bf16 a[56:59], v[136:139], v[100:103], a[56:59]\n\tv_exp_f32 v150, v46\n\t" \
    "v_mfma_f32_16x16x32_bf16 a[88:91], v[136:139], v[104:107], a[88:91]\n\tv_cvt_pk_bf16_f32 v94, v148, v149\n\tv_add_f32 v155, v155, v150\n\t" \
    "v_mfma_f32_16x16x32_bf16 a[120:123], v[136:139], v[108:111], a[120:123]\n\ts_waitcnt lgkmcnt(5)\n\tv_exp_f32 v151, v47\n\tv_mfma_f32_16x16x32_bf16 a[124:127], v[140:143], v[108:111], a[124:127]\n\t" \
    "ds_read_b128 v[132:135], v176 offset:37888\n\ts_nop 0\n\tv_mfma_f32_16x16x32_bf16 a[92:95], v[140:143], v[104:107], a[92:95]\n\tv_add_f32 v155, v155, v151\n\tv_cvt_pk_bf16_f32 v95, v150, v151\n\t" \
    "v_mfma_f32_16x16x32_bf16 a[60:63], v[140:143], v[100:103], a[60:63]\n\tv_mfma_f32_16x16x32_bf16 a[28:31], v[140:143], v[96:99], a[28:31]\n\ts_waitcnt lgkmcnt(5)\n\tv_exp_f32 v144, v56\n\t" \
    "v_mfma_f32_16x16x32_bf16 v[32:35], v[112:115], a[160:163], v[160:163]\n\tds_read_b128 v[136:139], v176 offset:38912\n\tv_add_f32 v153, v153, v144\n\t" \
    "v_mfma_f32_16x16x32_bf16 v[40:43], v[112:115], a[176:179], 0\n\ts_waitcnt lgkmcnt(5)\n\ts_add_u32 m0, %[lb], 114688\n\tv_exp_f32 v145, v57\n\t" \
    "v_mfma_f32_16x16x32_bf16 v[44:47], v[116:119], a[176:179], 0\n\tds_read_b128 v[140:143], v176 offset:39936\n\tv_add_f32 v153, v153, v145\n\t" \
    "v_mfma_f32_16x16x32_bf16 v[36:39], v[116:119], a[160:163], v[164:167]\n\ts_waitcnt lgkmcnt(5)\n\tv_exp_f32 v146, v58\n\tv_mfma_f32_16x16x32_bf16 v[32:35], v[120:123], a[164:167], v[32:35]\n\t" \
    "ds_read_b128 v[112:115], v177 offset:16384\n\tbuffer_load_dwordx4 %[dv], %[rs], s63 offen sc1 lds\n\tv_cvt_pk_bf16_f32 v100, v144, v145\n\t" \
    "v_mfma_f32_16x16x32_bf16 v[40:43], v[120:123], a[180:183], v[40:43]\n\ts_waitcnt lgkmcnt(5)\n\tv_exp_f32 v147, v59\n\tv_mfma_f32_16x16x32_bf16 v[44:47], v[124:127], a[180:183], v[44:47]\n\t" \
    "ds_read_b128 v[116:119], v177 offset:17408\n\tv_add_f32 v153, v153, v146\n\tv_mfma_f32_16x16x32_bf16 v[36:39], v[124:127], a[164:167], v[36:39]\n\ts_waitcnt lgkmcnt(5)\n\tv_exp_f32 v148, v60\n\t" \
    "v_mfma_f32_16x16x32_bf16 v[32:35], v[128:131], a[168:171], v[32:35]\n\tds_read_b128 v[120:123], v177 offset:18432\n\tbuffer_load_dwordx4 %[dv], %[rs], s63 offen offset:1024 sc1 lds\n\t" \
    "v_add_f32 v153, v153, v147\n\tv_mfma_f32_16x16x32_bf16 v[40:43], v[128:131], a[184:187], v[40:43]\n\ts_waitcnt lgkmcnt(5)\n\tv_cvt_pk_bf16_f32 v101, v146, v147\n\tv_add_f32 v153, v153, v148\n\t" \
    "v_mfma_f32_16x16x32_bf16 v[44:47], v[132:135], a[184:187], v[44:47]\n\ts_add_u32 s63, s63, 0x40000\n\tds_read_b128 v[124:127], v177 offset:19456\n\t" \
    "v_mfma_f32_16x16x32_bf16 v[36:39], v[132:135], a[168:171], v[36:39]\n\ts_waitcnt lgkmcnt(5)\n\tv_exp_f32 v149, v61\n\tv_mfma_f32_16x16x32_bf16 v[32:35], v[136:139], a[172:175], v[32:35]\n\t" \
    "ds_read_b128 v[128:131], v177 offset:20480\n\tv_add_f32 v153, v153, v149\n\tv_mfma_f32_16x16x32_bf16 v[40:43], v[136:139], a[188:191], v[40:43]\n\ts_waitcnt lgkmcnt(5)\n\tv_exp_f32 v150, v62\n\t" \
    "v_mfma_f32_16x16x32_bf16 v[44:47], v[140:143], a[188:191], v[44:47]\n\tds_read_b128 v[132:135], v177 offset:21504\n\tv_cvt_pk_bf16_f32 v102, v148, v149\n\t" \
    "v_mfma_f32_16x16x32_bf16 v[36:39], v[140:143], a[172:175], v[36:39]\n\ts_waitcnt lgkmcnt(5)\n\tv_exp_f32 v151, v63\n\tv_mfma_f32_16x16x32_bf16 a[32:35], v[112:115], v[84:87], a[32:35]\n\t" \
    "ds_read_b128 v[136:139], v177 offset:22528\n\tv_add_f32 v153, v153, v150\n\tv_mfma_f32_16x16x32_bf16 a[64:67], v[112:115], v[88:91], a[64:67]\n\tv_exp_f32 v144, v64\n\t" \
    "v_mfma_f32_16x16x32_bf16 a[96:99], v[112:115], v[92:95], a[96:99]\n\ts_waitcnt lgkmcnt(5)\n\tv_add_f32 v153, v153, v151\n\tv_cvt_pk_bf16_f32 v103, v150, v151\n\t" \
    "v_mfma_f32_16x16x32_bf16 a[100:103], v[116:119], v[92:95], a[100:103]\n\tds_read_b128 v[140:143], v177 offset:23552\n\tv_add_f32 v154, v154, v144\n\t" \
    "v_mfma_f32_16x16x32_bf16 a[68:71], v[116:119], v[88:91], a[68:71]\n\tv_exp_f32 v145, v65\n\tv_mfma_f32_16x16x32_bf16 a[36:39], v[116:119], v[84:87], a[36:39]\n\ts_waitcnt lgkmcnt(5)\n\t" \
    "v_exp_f32 v146, v66\n\tv_mfma_f32_16x16x32_bf16 a[40:43], v[120:123], v[84:87], a[40:43]\n\tds_read_b128 v[112:115], v176 offset:40960\n\tv_add_f32 v154, v154, v145\n\t" \
    "v_mfma_f32_16x16x32_bf16 a[72:75], v[120:123], v[88:91], a[72:75]\n\tv_cvt_pk_bf16_f32 v104, v144, v145\n\tv_add_f32 v154, v154, v146\n\t" \
    "v_mfma_f32_16x16x32_bf16 a[104:107], v[120:123], v[92:95], a[104:107]\n\ts_waitcnt lgkmcnt(5)\n\tv_exp_f32 v147, v67\n\tv_mfma_f32_16x16x32_bf16 a[108:111], v[124:127], v[92:95], a[108:111]\n\t" \
    "ds_read_b128 v[116:119], v176 offset:41984\n\tv_add_f32 v154, v154, v147\n\tv_mfma_f32_16x16x32_bf16 a[76:79], v[124:127], v[88:91], a[76:79]\n\tv_exp_f32 v148, v68\n\t" \
    "v_mfma_f32_16x16x32_bf16 a[44:47], v[124:127], v[84:87], a[44:47]\n\ts_waitcnt lgkmcnt(5)\n\tv_cvt_pk_bf16_f32 v105, v146, v147\n\tv_add_f32 v154, v154, v148\n\t" \
    "v_mfma_f32_16x16x32_bf16 a[48:51], v[128:131], v[84:87], a[48:51]\n\tds_read_b128 v[120:123], v176 offset:43008\n\tv_mfma_f32_16x16x32_bf16 a[80:83], v[128:131], v[88:91], a[80:83]\n\t" \
    "v_exp_f32 v149, v69\n\tv_mfma_f32_16x16x32_bf16 a[112:115], v[128:131], v[92:95], a[112:115]\n\ts_waitcnt lgkmcnt(5)\n\tv_exp_f32 v150, v70\n\t" \
    "v_mfma_f32_16x16x32_bf16 a[116:119], v[132:135], v[92:95], a[116:119]\n\tds_read_b128 v[124:127], v176 offset:44032\n\tv_add_f32 v154, v154, v149\n\t" \
    "v_mfma_f32_16x16x32_bf16 a[84:87], v[132:135], v[88:91], a[84:87]\n\tv_cvt_pk_bf16_f32 v106, v148, v149\n\tv_add_f32 v154, v154, v150\n\t" \
    "v_mfma_f32_16x16x32_bf16 a[52:55], v[132:135], v[84:87], a[52:55]\n\ts_waitcnt lgkmcnt(5)\n\tv_exp_f32 v151, v71\n\tv_mfma_f32_16x16x32_bf16 a[56:59], v[136:139], v[84:87], a[56:59]\n\t" \
    "ds_read_b128 v[128:131], v176 offset:45056\n\tv_add_f32 v154, v154, v151\n\tv_mfma_f32_16x16x32_bf16 a[88:91], v[136:139], v[88:91], a[88:91]\n\tv_exp_f32 v144, v72\n\t" \
    "v_mfma_f32_16x16x32_bf16 a[120:123], v[136:139], v[92:95], a[120:123]\n\ts_waitcnt lgkmcnt(5)\n\tv_cvt_pk_bf16_f32 v107, v150, v151\n\tv_add_f32 v155, v155, v144\n\t" \
    "v_mfma_f32_16x16x32_bf16 a[124:127], v[140:143], v[92:95], a[124:127]\n\tds_read_b128 v[132:135], v176 offset:46080\n\tv_mfma_f32_16x16x32_bf16 a[92:95], v[140:143], v[88:91], a[92:95]\n\t" \
    "v_exp_f32 v145, v73\n\tv_mfma_f32_16x16x32_bf16 a[60:63], v[140:143], v[84:87], a[60:63]\n\ts_waitcnt lgkmcnt(5)\n\tv_exp_f32 v146, v74\n\tv_add_f32 v155, v155, v145\n\t" \
    "v_cvt_pk_bf16_f32 v108, v144, v145\n\tv_exp_f32 v147, v75\n\tv_add_f32 v155, v155, v146\n\tv_exp_f32 v148, v76\n\tv_add_f32 v155, v155, v147\n\tv_cvt_pk_bf16_f32 v109, v146, v147\n\t" \
    "v_exp_f32 v149, v77\n\tv_add_f32 v155, v155, v148\n\tv_exp_f32 v150, v78\n\tv_add_f32 v155, v155, v149\n\tv_cvt_pk_bf16_f32 v110, v148, v149\n\tv_exp_f32 v151, v79\n\t" \
    "v_mfma_f32_16x16x32_bf16 v[64:67], v[112:115], a[160:163], v[168:171]\n\ts_waitcnt vmcnt(4)\n\ts_barrier\n\ts_mov_b64 exec, %[em]\n\tds_write_b64 %[lq], v[212:213]\n\ts_mov_b64 exec, -1\n\t" \
    "ds_read_b128 v[136:139], v176 offset:47104\n\tv_add_f32 v155, v155, v150\n\tv_mfma_f32_16x16x32_bf16 v[72:75], v[112:115], a[176:179], 0\n\ts_waitcnt lgkmcnt(5)\n\ts_add_u32 m0, %[lb], 122880\n\t" \
    "s_nop 0\n\tv_add_f32 v155, v155, v151\n\tv_mfma_f32_16x16x32_bf16 v[76:79], v[116:119], a[176:179], 0\n\tds_read_b128 v[140:143], v176 offset:48128\n\tv_cvt_pk_bf16_f32 v111, v150, v151\n\t" \
    "v_mfma_f32_16x16x32_bf16 v[68:71], v[116:119], a[160:163], v[172:175]\n\ts_waitcnt lgkmcnt(5)\n\tv_mov_b32 v180, v152\n\ts_nop 1\n\t" \
    "v_mfma_f32_16x16x32_bf16 v[64:67], v[120:123], a[164:167], v[64:67]\n\tds_read_b128 v[112:115], v177 offset:24576\n\tbuffer_load_dwordx4 %[dv], %[rs], s63 offen sc1 lds\n\t" \
    "v_mfma_f32_16x16x32_bf16 v[72:75], v[120:123], a[180:183], v[72:75]\n\ts_waitcnt lgkmcnt(5)\n\tv_permlane16_swap_b32 v180, v152\n\t" \
    "v_mfma_f32_16x16x32_bf16 v[76:79], v[124:127], a[180:183], v[76:79]\n\tds_read_b128 v[116:119], v177 offset:25600\n\tv_add_f32 v152, v152, v180\n\t" \
    "v_mfma_f32_16x16x32_bf16 v[68:71], v[124:127], a[164:167], v[68:71]\n\ts_waitcnt lgkmcnt(5)\n\tv_mov_b32 v180, v152\n\ts_nop 1\n\t" \
    "v_mfma_f32_16x16x32_bf16 v[64:67], v[128:131], a[168:171], v[64:67]\n\tds_read_b128 v[120:123], v177 offset:26624\n\tbuffer_load_dwordx4 %[dv], %[rs], s63 offen offset:1024 sc1 lds\n\t" \
    "v_mfma_f32_16x16x32_bf16 v[72:75], v[128:131], a[184:187], v[72:75]\n\ts_waitcnt lgkmcnt(5)\n\tv_permlane32_swap_b32 v180, v152\n\t" \
    "v_mfma_f32_16x16x32_bf16 v[76:79], v[132:135], a[184:187], v[76:79]\n\ts_add_u32 s63, s63, 0x40000\n\tds_read_b128 v[124:127], v177 offset:27648\n\tv_add_f32 v152, v152, v180\n\t" \
    "v_mfma_f32_16x16x32_bf16 v[68:71], v[132:135], a[168:171], v[68:71]\n\ts_waitcnt lgkmcnt(5)\n\tv_subrev_u32 v180, 0x1f800000, v152\n\t" \
    "v_mfma_f32_16x16x32_bf16 v[64:67], v[136:139], a[172:175], v[64:67]\n\tds_read_b128 v[128:131], v177 offset:28672\n\tv_mfma_f32_16x16x32_bf16 v[72:75], v[136:139], a[188:191], v[72:75]\n\t" \
    "s_waitcnt lgkmcnt(5)\n\tv_rcp_f32 v152, v152\n\tv_mfma_f32_16x16x32_bf16 v[76:79], v[140:143], a[188:191], v[76:79]\n\tds_read_b128 v[132:135], v177 offset:29696\n\tv_cmp_le_u32 vcc, 2.0, v180\n\t" \
    "v_mfma_f32_16x16x32_bf16 v[68:71], v[140:143], a[172:175], v[68:71]\n\ts_waitcnt lgkmcnt(5)\n\ts_or_b64 s[66:67], s[66:67], vcc\n\ts_add_u32 s61, %[sq], 50331648\n\t" \
    "v_mfma_f32_16x16x32_bf16 a[32:35], v[112:115], v[100:103], a[32:35]\n\tds_read_b128 v[136:139], v177 offset:30720\n\tv_accvgpr_read_b32 v184, a0\n\t" \
    "v_mfma_f32_16x16x32_bf16 a[64:67], v[112:115], v[104:107], a[64:67]\n\tv_accvgpr_read_b32 v185, a1\n\tv_accvgpr_read_b32 v186, a2\n\t" \
    "v_mfma_f32_16x16x32_bf16 a[96:99], v[112:115], v[108:111], a[96:99]\n\ts_waitcnt lgkmcnt(5)\n\tv_accvgpr_read_b32 v187, a3\n\tv_accvgpr_read_b32 v188, a4\n\t" \
    "v_mfma_f32_16x16x32_bf16 a[100:103], v[116:119], v[108:111], a[100:103]\n\tds_read_b128 v[140:143], v177 offset:31744\n\tv_accvgpr_read_b32 v189, a5\n\t" \
    "v_mfma_f32_16x16x32_bf16 a[68:71], v[116:119], v[104:107], a[68:71]\n\tv_accvgpr_read_b32 v190, a6\n\tv_accvgpr_read_b32 v191, a7\n\t" \
    "v_mfma_f32_16x16x32_bf16 a[36:39], v[116:119], v[100:103], a[36:39]\n\ts_waitcnt lgkmcnt(5)\n\tv_mul_f32 v184, v152, v184\n\tv_mul_f32 v185, v152, v185\n\t" \
    "v_mfma_f32_16x16x32_bf16 a[40:43], v[120:123], v[100:103], a[40:43]\n\tds_read_b128 v[112:115], v176 offset:49152\n\tv_mul_f32 v186, v152, v186\n\t" \
    "v_mfma_f32_16x16x32_bf16 a[72:75], v[120:123], v[104:107], a[72:75]\n\tv_mul_f32 v187, v152, v187\n\tv_mul_f32 v188, v152, v188\n\t" \
    "v_mfma_f32_16x16x32_bf16 a[104:107], v[120:123], v[108:111], a[104:107]\n\ts_waitcnt lgkmcnt(5)\n\tv_mul_f32 v189, v152, v189\n\tv_mul_f32 v190, v152, v190\n\t" \
    "v_mfma_f32_16x16x32_bf16 a[108:111], v[124:127], v[108:111], a[108:111]\n\tds_read_b128 v[116:119], v176 offset:50176\n\tv_mul_f32 v191, v152, v191\n\t" \
    "v_mfma_f32_16x16x32_bf16 a[76:79], v[124:127], v[104:107], a[76:79]\n\tv_cvt_pk_bf16_f32 v200, v184, v185\n\tv_cvt_pk_bf16_f32 v201, v186, v187\n\t" \
    "v_mfma_f32_16x16x32_bf16 a[44:47], v[124:127], v[100:103], a[44:47]\n\ts_waitcnt lgkmcnt(5)\n\tv_cvt_pk_bf16_f32 v202, v188, v189\n\tv_cvt_pk_bf16_f32 v203, v190, v191\n\t" \
    "v_mfma_f32_16x16x32_bf16 a[48:51], v[128:131], v[100:103], a[48:51]\n\tds_read_b128 v[120:123], v176 offset:51200\n\tbuffer_store_dwordx4 v[200:203], %[dv], %[rs], s61 offen sc1\n\t" \
    "v_mfma_f32_16x16x32_bf16 a[80:83], v[128:131], v[104:107], a[80:83]\n\tv_accvgpr_read_b32 v192, a8\n\tv_accvgpr_read_b32 v193, a9\n\t" \
    "v_mfma_f32_16x16x32_bf16 a[112:115], v[128:131], v[108:111], a[112:115]\n\ts_waitcnt lgkmcnt(5)\n\tv_accvgpr_read_b32 v194, a10\n\tv_accvgpr_read_b32 v195, a11\n\t" \
    "v_mfma_f32_16x16x32_bf16 a[116:119], v[132:135], v[108:111], a[116:119]\n\tds_read_b128 v[124:127], v176 offset:52224\n\tv_accvgpr_read_b32 v196, a12\n\t" \
    "v_mfma_f32_16x16x32_bf16 a[84:87], v[132:135], v[104:107], a[84:87]\n\tv_accvgpr_read_b32 v197, a13\n\tv_accvgpr_read_b32 v198, a14\n\t" \
    "v_mfma_f32_16x16x32_bf16 a[52:55], v[132:135], v[100:103], a[52:55]\n\ts_waitcnt lgkmcnt(5)\n\tv_accvgpr_read_b32 v199, a15\n\tv_mul_f32 v192, v152, v192\n\t" \
    "v_mfma_f32_16x16x32_bf16 a[56:59], v[136:139], v[100:103], a[56:59]\n\tds_read_b128 v[128:131], v176 offset:53248\n\tv_mul_f32 v193, v152, v193\n\t" \
    "v_mfma_f32_16x16x32_bf16 a[88:91], v[136:139], v[104:107], a[88:91]\n\tv_mul_f32 v194, v152, v194\n\tv_mul_f32 v195, v152, v195\n\t" \
    "v_mfma_f32_16x16x32_bf16 a[120:123], v[136:139], v[108:111], a[120:123]\n\ts_waitcnt lgkmcnt(5)\n\tv_mul_f32 v196, v152, v196\n\tv_mul_f32 v197, v152, v197\n\t" \
    "v_mfma_f32_16x16x32_bf16 a[124:127], v[140:143], v[108:111], a[124:127]\n\tds_read_b128 v[132:135], v176 offset:54272\n\tv_mul_f32 v198, v152, v198\n\t" \
    "v_mfma_f32_16x16x32_bf16 a[92:95], v[140:143], v[104:107], a[92:95]\n\tv_mul_f32 v199, v152, v199\n\tv_cvt_pk_bf16_f32 v204, v192, v193\n\t" \
    "v_mfma_f32_16x16x32_bf16 a[60:63], v[140:143], v[100:103], a[60:63]\n\ts_waitcnt lgkmcnt(5)\n\tv_cvt_pk_bf16_f32 v205, v194, v195\n\tv_cvt_pk_bf16_f32 v206, v196, v197\n\t" \
    "v_cvt_pk_bf16_f32 v207, v198, v199\n\tbuffer_store_dwordx4 v[204:207], %[dv], %[rs], s61 offen offset:2048 sc1\n\tv_accvgpr_read_b32 v184, a16\n\tv_accvgpr_read_b32 v185, a17\n\t" \
    "v_accvgpr_read_b32 v186, a18\n\tv_accvgpr_read_b32 v187, a19\n\tv_accvgpr_read_b32 v188, a20\n\tv_accvgpr_read_b32 v189, a21\n\tv_accvgpr_read_b32 v190, a22\n\tv_accvgpr_read_b32 v191, a23\n\t" \
    "v_mul_f32 v184, v152, v184\n\tv_mul_f32 v185, v152, v185\n\tv_mul_f32 v186, v152, v186\n\tv_mul_f32 v187, v152, v187\n\tv_mul_f32 v188, v152, v188\n\tv_mul_f32 v189, v152, v189\n\t" \
    "v_mul_f32 v190, v152, v190\n\tv_mul_f32 v191, v152, v191\n\tv_cvt_pk_bf16_f32 v200, v184, v185\n\tv_cvt_pk_bf16_f32 v201, v186, v187\n\tv_cvt_pk_bf16_f32 v202, v188, v189\n\t" \
    "v_cvt_pk_bf16_f32 v203, v190, v191\n\tbuffer_store_dwordx4 v[200:203], %[dw], %[rs], s61 offen sc1\n\tv_accvgpr_read_b32 v192, a24\n\tv_accvgpr_read_b32 v193, a25\n\tv_accvgpr_read_b32 v194, a26\n\t" \
    "v_accvgpr_read_b32 v195, a27\n\tv_accvgpr_read_b32 v196, a28\n\tv_accvgpr_read_b32 v197, a29\n\tv_accvgpr_read_b32 v198, a30\n\tv_accvgpr_read_b32 v199, a31\n\tv_mul_f32 v192, v152, v192\n\t" \
    "v_mul_f32 v193, v152, v193\n\tv_mul_f32 v194, v152, v194\n\tv_mul_f32 v195, v152, v195\n\tv_mul_f32 v196, v152, v196\n\tv_mul_f32 v197, v152, v197\n\tv_mul_f32 v198, v152, v198\n\t" \
    "v_mul_f32 v199, v152, v199\n\tv_cvt_pk_bf16_f32 v204, v192, v193\n\tv_cvt_pk_bf16_f32 v205, v194, v195\n\tv_cvt_pk_bf16_f32 v206, v196, v197\n\tv_cvt_pk_bf16_f32 v207, v198, v199\n\t" \
    "buffer_store_dwordx4 v[204:207], %[dw], %[rs], s61 offen offset:2048 sc1\n\tv_exp_f32 v144, v32\n\tv_exp_f32 v145, v33\n\tv_add_f32 v154, v154, v144\n\tv_exp_f32 v146, v34\n\t" \
    "v_add_f32 v154, v154, v145\n\tv_cvt_pk_bf16_f32 v88, v144, v145\n\tv_exp_f32 v147, v35\n\tv_add_f32 v154, v154, v146\n\tv_exp_f32 v148, v36\n\tv_add_f32 v154, v154, v147\n\t" \
    "v_cvt_pk_bf16_f32 v89, v146, v147\n\tv_exp_f32 v149, v37\n\tv_add_f32 v154, v154, v148\n\tv_exp_f32 v150, v38\n\tv_add_f32 v154, v154, v149\n\tv_cvt_pk_bf16_f32 v90, v148, v149\n\t" \
    "v_exp_f32 v151, v39\n\tv_add_f32 v154, v154, v150\n\tv_exp_f32 v144, v40\n\tv_add_f32 v154, v154, v151\n\tv_cvt_pk_bf16_f32 v91, v150, v151\n\tv_exp_f32 v145, v41\n\tv_add_f32 v155, v155, v144\n\t" \
    "v_exp_f32 v146, v42\n\tv_add_f32 v155, v155, v145\n\tv_cvt_pk_bf16_f32 v92, v144, v145\n\tv_exp_f32 v147, v43\n\tv_add_f32 v155, v155, v146\n\tv_exp_f32 v148, v44\n\tv_add_f32 v155, v155, v147\n\t" \
    "v_cvt_pk_bf16_f32 v93, v146, v147\n\tv_exp_f32 v149, v45\n\tv_add_f32 v155, v155, v148\n\tv_exp_f32 v150, v46\n\tv_add_f32 v155, v155, v149\n\tv_cvt_pk_bf16_f32 v94, v148, v149\n\t" \
    "v_exp_f32 v151, v47\n\tv_mfma_f32_16x16x32_bf16 v[40:43], v[112:115], a[176:179], v[160:163]\n\ts_waitcnt lgkmcnt(4)\n\tds_read_b128 v[136:139], v176 offset:55296\n\tv_add_f32 v155, v155, v150\n\t" \
    "v_mfma_f32_16x16x32_bf16 v[44:47], v[116:119], a[176:179], v[164:167]\n\ts_waitcnt lgkmcnt(4)\n\tds_read_b128 v[140:143], v176 offset:56320\n\ts_nop 0\n\t" \
    "v_mfma_f32_16x16x32_bf16 v[40:43], v[120:123], a[180:183], v[40:43]\n\ts_waitcnt lgkmcnt(4)\n\tds_read_b128 v[112:115], v177 offset:32768\n\tv_add_f32 v155, v155, v151\n\t" \
    "v_mfma_f32_16x16x32_bf16 v[44:47], v[124:127], a[180:183], v[44:47]\n\ts_waitcnt lgkmcnt(4)\n\tds_read_b128 v[116:119], v177 offset:33792\n\tv_cvt_pk_bf16_f32 v95, v150, v151\n\t" \
    "v_mfma_f32_16x16x32_bf16 v[40:43], v[128:131], a[184:187], v[40:43]\n\ts_waitcnt lgkmcnt(4)\n\tds_read_b128 v[120:123], v177 offset:34816\n\t" \
    "v_mfma_f32_16x16x32_bf16 v[44:47], v[132:135], a[184:187], v[44:47]\n\ts_waitcnt lgkmcnt(4)\n\tds_read_b128 v[124:127], v177 offset:35840\n\t" \
    "v_mfma_f32_16x16x32_bf16 v[40:43], v[136:139], a[188:191], v[40:43]\n\ts_waitcnt lgkmcnt(4)\n\tds_read_b128 v[128:131], v177 offset:36864\n\t" \
    "v_mfma_f32_16x16x32_bf16 v[44:47], v[140:143], a[188:191], v[44:47]\n\ts_waitcnt lgkmcnt(4)\n\tds_read_b128 v[132:135], v177 offset:37888\n\t" \
    "v_mfma_f32_16x16x32_bf16 a[64:67], v[112:115], v[88:91], a[64:67]\n\tds_read_b128 v[136:139], v177 offset:38912\n\tv_mfma_f32_16x16x32_bf16 a[96:99], v[112:115], v[92:95], a[96:99]\n\t" \
    "s_waitcnt lgkmcnt(5)\n\tv_exp_f32 v144, v64\n\tv_mfma_f32_16x16x32_bf16 a[100:103], v[116:119], v[92:95], a[100:103]\n\tds_read_b128 v[140:143], v177 offset:39936\n\tv_add_f32 v154, v154, v144\n\t" \
    "v_mfma_f32_16x16x32_bf16 a[68:71], v[116:119], v[88:91], a[68:71]\n\ts_waitcnt lgkmcnt(5)\n\tv_exp_f32 v145, v65\n\tv_mfma_f32_16x16x32_bf16 a[72:75], v[120:123], v[88:91], a[72:75]\n\t" \
    "ds_read_b128 v[112:115], v176 offset:57344\n\tv_add_f32 v154, v154, v145\n\tv_mfma_f32_16x16x32_bf16 a[104:107], v[120:123], v[92:95], a[104:107]\n\ts_waitcnt lgkmcnt(5)\n\tv_exp_f32 v146, v66\n\t" \
    "v_mfma_f32_16x16x32_bf16 a[108:111], v[124:127], v[92:95], a[108:111]\n\tds_read_b128 v[116:119], v176 offset:58368\n\tv_cvt_pk_bf16_f32 v104, v144, v145\n\t" \
    "v_mfma_f32_16x16x32_bf16 a[76:79], v[124:127], v[88:91], a[76:79]\n\ts_waitcnt lgkmcnt(5)\n\tv_exp_f32 v147, v67\n\tv_mfma_f32_16x16x32_bf16 a[80:83], v[128:131], v[88:91], a[80:83]\n\t" \
    "ds_read_b128 v[120:123], v176 offset:59392\n\tv_add_f32 v154, v154, v146\n\tv_mfma_f32_16x16x32_bf16 a[112:115], v[128:131], v[92:95], a[112:115]\n\ts_waitcnt lgkmcnt(5)\n\tv_exp_f32 v148, v68\n\t" \
    "v_mfma_f32_16x16x32_bf16 a[116:119], v[132:135], v[92:95], a[116:119]\n\tds_read_b128 v[124:127], v176 offset:60416\n\tv_add_f32 v154, v154, v147\n\t" \
    "v_mfma_f32_16x16x32_bf16 a[84:87], v[132:135], v[88:91], a[84:87]\n\ts_waitcnt lgkmcnt(5)\n\tv_cvt_pk_bf16_f32 v105, v146, v147\n\tv_add_f32 v154, v154, v148\n\t" \
    "v_mfma_f32_16x16x32_bf16 a[88:91], v[136:139], v[88:91], a[88:91]\n\tds_read_b128 v[128:131], v176 offset:61440\n\tv_mfma_f32_16x16x32_bf16 a[120:123], v[136:139], v[92:95], a[120:123]\n\t" \
    "s_waitcnt lgkmcnt(5)\n\tv_exp_f32 v149, v69\n\tv_mfma_f32_16x16x32_bf16 a[124:127], v[140:143], v[92:95], a[124:127]\n\tds_read_b128 v[132:135], v176 offset:62464\n\tv_add_f32 v154, v154, v149\n\t" \
    "v_mfma_f32_16x16x32_bf16 a[92:95], v[140:143], v[88:91], a[92:95]\n\ts_waitcnt lgkmcnt(5)\n\tv_exp_f32 v150, v70\n\tv_cvt_pk_bf16_f32 v106, v148, v149\n\tv_exp_f32 v151, v71\n\t" \
    "v_add_f32 v154, v154, v150\n\tv_exp_f32 v144, v72\n\tv_add_f32 v154, v154, v151\n\tv_cvt_pk_bf16_f32 v107, v150, v151\n\tv_exp_f32 v145, v73\n\tv_add_f32 v155, v155, v144\n\tv_exp_f32 v146, v74\n\t" \
    "v_add_f32 v155, v155, v145\n\tv_cvt_pk_bf16_f32 v108, v144, v145\n\tv_exp_f32 v147, v75\n\tv_add_f32 v155, v155, v146\n\tv_exp_f32 v148, v76\n\tv_add_f32 v155, v155, v147\n\t" \
    "v_cvt_pk_bf16_f32 v109, v146, v147\n\tv_exp_f32 v149, v77\n\tv_add_f32 v155, v155, v148\n\tv_exp_f32 v150, v78\n\tv_add_f32 v155, v155, v149\n\tv_cvt_pk_bf16_f32 v110, v148, v149\n\t" \
    "v_exp_f32 v151, v79\n\tv_mfma_f32_16x16x32_bf16 v[72:75], v[112:115], a[176:179], v[168:171]\n\ts_waitcnt lgkmcnt(4)\n\ts_waitcnt vmcnt(4)\n\ts_barrier\n\tds_read_b64 v[212:213], %[lq]\n\t" \
    "ds_read_b128 v[136:139], v176 offset:63488\n\tv_add_f32 v155, v155, v150\n\tv_mfma_f32_16x16x32_bf16 v[76:79], v[116:119], a[176:179], v[172:175]\n\ts_waitcnt lgkmcnt(5)\n\t" \
    "ds_read_b128 v[140:143], v176 offset:64512\n\ts_nop 0\n\tv_mfma_f32_16x16x32_bf16 v[72:75], v[120:123], a[180:183], v[72:75]\n\ts_waitcnt lgkmcnt(5)\n\tds_read_b128 v[112:115], v177 offset:40960\n\t" \
    "v_add_f32 v155, v155, v151\n\tv_mfma_f32_16x16x32_bf16 v[76:79], v[124:127], a[180:183], v[76:79]\n\ts_waitcnt lgkmcnt(5)\n\tds_read_b128 v[116:119], v177 offset:41984\n\t" \
    "v_cvt_pk_bf16_f32 v111, v150, v151\n\tv_mfma_f32_16x16x32_bf16 v[72:75], v[128:131], a[184:187], v[72:75]\n\ts_waitcnt lgkmcnt(5)\n\tds_read_b128 v[120:123], v177 offset:43008\n\t" \
    "v_mov_b32 v180, v153\n\tv_mfma_f32_16x16x32_bf16 v[76:79], v[132:135], a[184:187], v[76:79]\n\ts_waitcnt lgkmcnt(4)\n\tds_read_b128 v[124:127], v177 offset:44032\n\ts_nop 1\n\t" \
    "v_mfma_f32_16x16x32_bf16 v[72:75], v[136:139], a[188:191], v[72:75]\n\ts_waitcnt lgkmcnt(4)\n\tds_read_b128 v[128:131], v177 offset:45056\n\t" \
    "v_mfma_f32_16x16x32_bf16 v[76:79], v[140:143], a[188:191], v[76:79]\n\ts_waitcnt lgkmcnt(4)\n\tds_read_b128 v[132:135], v177 offset:46080\n\t" \
    "v_mfma_f32_16x16x32_bf16 a[64:67], v[112:115], v[104:107], a[64:67]\n\tds_read_b128 v[136:139], v177 offset:47104\n\tv_mfma_f32_16x16x32_bf16 a[96:99], v[112:115], v[108:111], a[96:99]\n\t" \
    "s_waitcnt lgkmcnt(5)\n\tv_permlane16_swap_b32 v180, v153\n\tv_mfma_f32_16x16x32_bf16 a[100:103], v[116:119], v[108:111], a[100:103]\n\tds_read_b128 v[140:143], v177 offset:48128\n\t" \
    "v_add_f32 v153, v153, v180\n\tv_mfma_f32_16x16x32_bf16 a[68:71], v[116:119], v[104:107], a[68:71]\n\ts_waitcnt lgkmcnt(5)\n\tv_mov_b32 v180, v153\n\ts_nop 1\n\t" \
    "v_mfma_f32_16x16x32_bf16 a[72:75], v[120:123], v[104:107], a[72:75]\n\tds_read_b128 v[112:115], v177 offset:49152\n\tv_mfma_f32_16x16x32_bf16 a[104:107], v[120:123], v[108:111], a[104:107]\n\t" \
    "s_waitcnt lgkmcnt(5)\n\tv_permlane32_swap_b32 v180, v153\n\tv_mfma_f32_16x16x32_bf16 a[108:111], v[124:127], v[108:111], a[108:111]\n\tds_read_b128 v[116:119], v177 offset:50176\n\t" \
    "v_add_f32 v153, v153, v180\n\tv_mfma_f32_16x16x32_bf16 a[76:79], v[124:127], v[104:107], a[76:79]\n\ts_waitcnt lgkmcnt(5)\n\tv_subrev_u32 v180, 0x1f800000, v153\n\t" \
    "v_mfma_f32_16x16x32_bf16 a[80:83], v[128:131], v[104:107], a[80:83]\n\tds_read_b128 v[120:123], v177 offset:51200\n\tv_mfma_f32_16x16x32_bf16 a[112:115], v[128:131], v[108:111], a[112:115]\n\t" \
    "s_waitcnt lgkmcnt(5)\n\tv_rcp_f32 v153, v153\n\tv_mfma_f32_16x16x32_bf16 a[116:119], v[132:135], v[108:111], a[116:119]\n\tds_read_b128 v[124:127], v177 offset:52224\n\tv_cmp_le_u32 vcc, 2.0, v180\n\t" \
    "v_mfma_f32_16x16x32_bf16 a[84:87], v[132:135], v[104:107], a[84:87]\n\ts_waitcnt lgkmcnt(5)\n\ts_or_b64 s[66:67], s[66:67], vcc\n\ts_add_u32 s61, %[sq], 50855936\n\t" \
    "v_mfma_f32_16x16x32_bf16 a[88:91], v[136:139], v[104:107], a[88:91]\n\tds_read_b128 v[128:131], v177 offset:53248\n\tv_accvgpr_read_b32 v184, a32\n\t" \
    "v_mfma_f32_16x16x32_bf16 a[120:123], v[136:139], v[108:111], a[120:123]\n\ts_waitcnt lgkmcnt(5)\n\tv_accvgpr_read_b32 v185, a33\n\tv_accvgpr_read_b32 v186, a34\n\t" \
    "v_mfma_f32_16x16x32_bf16 a[124:127], v[140:143], v[108:111], a[124:127]\n\tds_read_b128 v[132:135], v177 offset:54272\n\tv_accvgpr_read_b32 v187, a35\n\tv_accvgpr_read_b32 v188, a36\n\t" \
    "v_accvgpr_read_b32 v189, a37\n\tv_accvgpr_read_b32 v190, a38\n\tv_accvgpr_read_b32 v191, a39\n\tv_mul_f32 v184, v153, v184\n\tv_mul_f32 v185, v153, v185\n\tv_mul_f32 v186, v153, v186\n\t" \
    "v_mul_f32 v187, v153, v187\n\tv_mul_f32 v188, v153, v188\n\tv_mul_f32 v189, v153, v189\n\tv_mul_f32 v190, v153, v190\n\tv_mul_f32 v191, v153, v191\n\tv_cvt_pk_bf16_f32 v200, v184, v185\n\t" \
    "v_cvt_pk_bf16_f32 v201, v186, v187\n\tv_cvt_pk_bf16_f32 v202, v188, v189\n\tv_cvt_pk_bf16_f32 v203, v190, v191\n\tbuffer_store_dwordx4 v[200:203], %[dv], %[rs], s61 offen sc1\n\t" \
    "v_accvgpr_read_b32 v192, a40\n\tv_accvgpr_read_b32 v193, a41\n\tv_accvgpr_read_b32 v194, a42\n\tv_accvgpr_read_b32 v195, a43\n\tv_accvgpr_read_b32 v196, a44\n\tv_accvgpr_read_b32 v197, a45\n\t" \
    "v_accvgpr_read_b32 v198, a46\n\tv_accvgpr_read_b32 v199, a47\n\tv_mul_f32 v192, v153, v192\n\tv_mul_f32 v193, v153, v193\n\tv_mul_f32 v194, v153, v194\n\tv_mul_f32 v195, v153, v195\n\t" \
    "v_mul_f32 v196, v153, v196\n\tv_mul_f32 v197, v153, v197\n\tv_mul_f32 v198, v153, v198\n\tv_mul_f32 v199, v153, v199\n\tv_cvt_pk_bf16_f32 v204, v192, v193\n\tv_cvt_pk_bf16_f32 v205, v194, v195\n\t" \
    "v_cvt_pk_bf16_f32 v206, v196, v197\n\tv_cvt_pk_bf16_f32 v207, v198, v199\n\tbuffer_store_dwordx4 v[204:207], %[dv], %[rs], s61 offen offset:2048 sc1\n\tv_accvgpr_read_b32 v184, a48\n\t" \
    "v_accvgpr_read_b32 v185, a49\n\tv_accvgpr_read_b32 v186, a50\n\tv_accvgpr_read_b32 v187, a51\n\tv_accvgpr_read_b32 v188, a52\n\tv_accvgpr_read_b32 v189, a53\n\tv_accvgpr_read_b32 v190, a54\n\t" \
    "v_accvgpr_read_b32 v191, a55\n\tv_mul_f32 v184, v153, v184\n\tv_mul_f32 v185, v153, v185\n\tv_mul_f32 v186, v153, v186\n\tv_mul_f32 v187, v153, v187\n\tv_mul_f32 v188, v153, v188\n\t" \
    "v_mul_f32 v189, v153, v189\n\tv_mul_f32 v190, v153, v190\n\tv_mul_f32 v191, v153, v191\n\tv_cvt_pk_bf16_f32 v200, v184, v185\n\tv_cvt_pk_bf16_f32 v201, v186, v187\n\t" \
    "v_cvt_pk_bf16_f32 v202, v188, v189\n\tv_cvt_pk_bf16_f32 v203, v190, v191\n\tbuffer_store_dwordx4 v[200:203], %[dw], %[rs], s61 offen sc1\n\tv_accvgpr_read_b32 v192, a56\n\t" \
    "v_accvgpr_read_b32 v193, a57\n\tv_accvgpr_read_b32 v194, a58\n\tv_accvgpr_read_b32 v195, a59\n\tv_accvgpr_read_b32 v196, a60\n\tv_accvgpr_read_b32 v197, a61\n\tv_accvgpr_read_b32 v198, a62\n\t" \
    "v_accvgpr_read_b32 v199, a63\n\tv_mul_f32 v192, v153, v192\n\tv_mul_f32 v193, v153, v193\n\tv_mul_f32 v194, v153, v194\n\tv_mul_f32 v195, v153, v195\n\tv_mul_f32 v196, v153, v196\n\t" \
    "v_mul_f32 v197, v153, v197\n\tv_mul_f32 v198, v153, v198\n\tv_mul_f32 v199, v153, v199\n\tv_cvt_pk_bf16_f32 v204, v192, v193\n\tv_cvt_pk_bf16_f32 v205, v194, v195\n\t" \
    "v_cvt_pk_bf16_f32 v206, v196, v197\n\tv_cvt_pk_bf16_f32 v207, v198, v199\n\tbuffer_store_dwordx4 v[204:207], %[dw], %[rs], s61 offen offset:2048 sc1\n\tv_exp_f32 v144, v40\n\tv_exp_f32 v145, v41\n\t" \
    "v_add_f32 v155, v155, v144\n\tv_exp_f32 v146, v42\n\tv_add_f32 v155, v155, v145\n\tv_cvt_pk_bf16_f32 v92, v144, v145\n\tv_exp_f32 v147, v43\n\tv_add_f32 v155, v155, v146\n\tv_exp_f32 v148, v44\n\t" \
    "v_add_f32 v155, v155, v147\n\tv_cvt_pk_bf16_f32 v93, v146, v147\n\tv_exp_f32 v149, v45\n\tv_add_f32 v155, v155, v148\n\tv_exp_f32 v150, v46\n\tv_add_f32 v155, v155, v149\n\t" \
    "v_cvt_pk_bf16_f32 v94, v148, v149\n\tv_exp_f32 v151, v47\n\tv_add_f32 v155, v155, v150\n\ts_nop 0\n\tv_add_f32 v155, v155, v151\n\tv_cvt_pk_bf16_f32 v95, v150, v151\n\t" \
    "v_mfma_f32_16x16x32_bf16 a[92:95], v[140:143], v[104:107], a[92:95]\n\ts_waitcnt lgkmcnt(5)\n\tv_exp_f32 v144, v72\n\tv_mfma_f32_16x16x32_bf16 a[96:99], v[112:115], v[92:95], a[96:99]\n\t" \
    "s_waitcnt lgkmcnt(4)\n\tds_read_b128 v[136:139], v177 offset:55296\n\tv_add_f32 v155, v155, v144\n\tv_mfma_f32_16x16x32_bf16 a[100:103], v[116:119], v[92:95], a[100:103]\n\ts_waitcnt lgkmcnt(4)\n\t" \
    "ds_read_b128 v[140:143], v177 offset:56320\n\tv_mfma_f32_16x16x32_bf16 a[104:107], v[120:123], v[92:95], a[104:107]\n\ts_waitcnt lgkmcnt(4)\n\tds_read_b128 v[112:115], v177 offset:57344\n\t" \
    "v_mfma_f32_16x16x32_bf16 a[108:111], v[124:127], v[92:95], a[108:111]\n\ts_waitcnt lgkmcnt(4)\n\tds_read_b128 v[116:119], v177 offset:58368\n\t" \
    "v_mfma_f32_16x16x32_bf16 a[112:115], v[128:131], v[92:95], a[112:115]\n\ts_waitcnt lgkmcnt(4)\n\tds_read_b128 v[120:123], v177 offset:59392\n\t" \
    "v_mfma_f32_16x16x32_bf16 a[116:119], v[132:135], v[92:95], a[116:119]\n\ts_waitcnt lgkmcnt(4)\n\tds_read_b128 v[124:127], v177 offset:60416\n\t" \
    "v_mfma_f32_16x16x32_bf16 a[120:123], v[136:139], v[92:95], a[120:123]\n\ts_waitcnt lgkmcnt(4)\n\tds_read_b128 v[128:131], v177 offset:61440\n\tv_exp_f32 v145, v73\n\tv_exp_f32 v146, v74\n\t" \
    "v_add_f32 v155, v155, v145\n\tv_cvt_pk_bf16_f32 v108, v144, v145\n\tv_exp_f32 v147, v75\n\tv_add_f32 v155, v155, v146\n\tv_exp_f32 v148, v76\n\tv_add_f32 v155, v155, v147\n\t" \
    "v_cvt_pk_bf16_f32 v109, v146, v147\n\tv_exp_f32 v149, v77\n\tv_add_f32 v155, v155, v148\n\tv_exp_f32 v150, v78\n\tv_add_f32 v155, v155, v149\n\tv_cvt_pk_bf16_f32 v110, v148, v149\n\t" \
    "v_exp_f32 v151, v79\n\tv_add_f32 v155, v155, v150\n\ts_nop 0\n\tv_add_f32 v155, v155, v151\n\tv_cvt_pk_bf16_f32 v111, v150, v151\n\t" \
    "v_mfma_f32_16x16x32_bf16 a[124:127], v[140:143], v[92:95], a[124:127]\n\ts_waitcnt lgkmcnt(4)\n\tds_read_b128 v[132:135], v177 offset:62464\n\tv_mov_b32 v180, v154\n\t" \
    "v_mfma_f32_16x16x32_bf16 a[96:99], v[112:115], v[108:111], a[96:99]\n\ts_waitcnt lgkmcnt(4)\n\tds_read_b128 v[136:139], v177 offset:63488\n\ts_nop 1\n\t" \
    "v_mfma_f32_16x16x32_bf16 a[100:103], v[116:119], v[108:111], a[100:103]\n\ts_waitcnt lgkmcnt(4)\n\tds_read_b128 v[140:143], v177 offset:64512\n\t" \
    "v_mfma_f32_16x16x32_bf16 a[104:107], v[120:123], v[108:111], a[104:107]\n\ts_waitcnt lgkmcnt(4)\n\tv_permlane16_swap_b32 v180, v154\n\t" \
    "v_mfma_f32_16x16x32_bf16 a[108:111], v[124:127], v[108:111], a[108:111]\n\ts_waitcnt lgkmcnt(3)\n\tv_add_f32 v154, v154, v180\n\tv_mov_b32 v180, v154\n\t" \
    "v_mfma_f32_16x16x32_bf16 a[112:115], v[128:131], v[108:111], a[112:115]\n\ts_waitcnt lgkmcnt(2)\n\ts_nop 1\n\tv_mfma_f32_16x16x32_bf16 a[116:119], v[132:135], v[108:111], a[116:119]\n\t" \
    "s_waitcnt lgkmcnt(1)\n\tv_permlane32_swap_b32 v180, v154\n\tv_mfma_f32_16x16x32_bf16 a[120:123], v[136:139], v[108:111], a[120:123]\n\ts_waitcnt lgkmcnt(0)\n\tv_add_f32 v154, v154, v180\n\t" \
    "v_subrev_u32 v180, 0x1f800000, v154\n\tv_mfma_f32_16x16x32_bf16 a[124:127], v[140:143], v[108:111], a[124:127]\n\tv_rcp_f32 v154, v154\n\tv_cmp_le_u32 vcc, 2.0, v180\n\t" \
    "s_or_b64 s[66:67], s[66:67], vcc\n\ts_add_u32 s61, %[sq], 51380224\n\tv_accvgpr_read_b32 v184, a64\n\tv_accvgpr_read_b32 v185, a65\n\tv_accvgpr_read_b32 v186, a66\n\tv_accvgpr_read_b32 v187, a67\n\t" \
    "v_accvgpr_read_b32 v188, a68\n\tv_accvgpr_read_b32 v189, a69\n\tv_accvgpr_read_b32 v190, a70\n\tv_accvgpr_read_b32 v191, a71\n\tv_mul_f32 v184, v154, v184\n\tv_mul_f32 v185, v154, v185\n\t" \
    "v_mul_f32 v186, v154, v186\n\tv_mul_f32 v187, v154, v187\n\tv_mul_f32 v188, v154, v188\n\tv_mul_f32 v189, v154, v189\n\tv_mul_f32 v190, v154, v190\n\tv_mul_f32 v191, v154, v191\n\t" \
    "v_cvt_pk_bf16_f32 v200, v184, v185\n\tv_cvt_pk_bf16_f32 v201, v186, v187\n\tv_cvt_pk_bf16_f32 v202, v188, v189\n\tv_cvt_pk_bf16_f32 v203, v190, v191\n\t" \
    "buffer_store_dwordx4 v[200:203], %[dv], %[rs], s61 offen sc1\n\tv_accvgpr_read_b32 v192, a72\n\tv_accvgpr_read_b32 v193, a73\n\tv_accvgpr_read_b32 v194, a74\n\tv_accvgpr_read_b32 v195, a75\n\t" \
    "v_accvgpr_read_b32 v196, a76\n\tv_accvgpr_read_b32 v197, a77\n\tv_accvgpr_read_b32 v198, a78\n\tv_accvgpr_read_b32 v199, a79\n\tv_mul_f32 v192, v154, v192\n\tv_mul_f32 v193, v154, v193\n\t" \
    "v_mul_f32 v194, v154, v194\n\tv_mul_f32 v195, v154, v195\n\tv_mul_f32 v196, v154, v196\n\tv_mul_f32 v197, v154, v197\n\tv_mul_f32 v198, v154, v198\n\tv_mul_f32 v199, v154, v199\n\t" \
    "v_cvt_pk_bf16_f32 v204, v192, v193\n\tv_cvt_pk_bf16_f32 v205, v194, v195\n\tv_cvt_pk_bf16_f32 v206, v196, v197\n\tv_cvt_pk_bf16_f32 v207, v198, v199\n\t" \
    "buffer_store_dwordx4 v[204:207], %[dv], %[rs], s61 offen offset:2048 sc1\n\tv_accvgpr_read_b32 v184, a80\n\tv_accvgpr_read_b32 v185, a81\n\tv_accvgpr_read_b32 v186, a82\n\t" \
    "v_accvgpr_read_b32 v187, a83\n\tv_accvgpr_read_b32 v188, a84\n\tv_accvgpr_read_b32 v189, a85\n\tv_accvgpr_read_b32 v190, a86\n\tv_accvgpr_read_b32 v191, a87\n\tv_mul_f32 v184, v154, v184\n\t" \
    "v_mul_f32 v185, v154, v185\n\tv_mul_f32 v186, v154, v186\n\tv_mul_f32 v187, v154, v187\n\tv_mul_f32 v188, v154, v188\n\tv_mul_f32 v189, v154, v189\n\tv_mul_f32 v190, v154, v190\n\t" \
    "v_mul_f32 v191, v154, v191\n\tv_cvt_pk_bf16_f32 v200, v184, v185\n\tv_cvt_pk_bf16_f32 v201, v186, v187\n\tv_cvt_pk_bf16_f32 v202, v188, v189\n\tv_cvt_pk_bf16_f32 v203, v190, v191\n\t" \
    "buffer_store_dwordx4 v[200:203], %[dw], %[rs], s61 offen sc1\n\tv_accvgpr_read_b32 v192, a88\n\tv_accvgpr_read_b32 v193, a89\n\tv_accvgpr_read_b32 v194, a90\n\tv_accvgpr_read_b32 v195, a91\n\t" \
    "v_accvgpr_read_b32 v196, a92\n\tv_accvgpr_read_b32 v197, a93\n\tv_accvgpr_read_b32 v198, a94\n\tv_accvgpr_read_b32 v199, a95\n\tv_mul_f32 v192, v154, v192\n\tv_mul_f32 v193, v154, v193\n\t" \
    "v_mul_f32 v194, v154, v194\n\tv_mul_f32 v195, v154, v195\n\tv_mul_f32 v196, v154, v196\n\tv_mul_f32 v197, v154, v197\n\tv_mul_f32 v198, v154, v198\n\tv_mul_f32 v199, v154, v199\n\t" \
    "v_cvt_pk_bf16_f32 v204, v192, v193\n\tv_cvt_pk_bf16_f32 v205, v194, v195\n\tv_cvt_pk_bf16_f32 v206, v196, v197\n\tv_cvt_pk_bf16_f32 v207, v198, v199\n\t" \
    "buffer_store_dwordx4 v[204:207], %[dw], %[rs], s61 offen offset:2048 sc1\n\ts_nop 7\n\tv_mov_b32 v180, v155\n\ts_nop 1\n\tv_permlane16_swap_b32 v180, v155\n\tv_add_f32 v155, v155, v180\n\t" \
    "v_mov_b32 v180, v155\n\ts_nop 1\n\tv_permlane32_swap_b32 v180, v155\n\tv_add_f32 v155, v155, v180\n\tv_subrev_u32 v180, 0x1f800000, v155\n\tv_rcp_f32 v155, v155\n\tv_cmp_le_u32 vcc, 2.0, v180\n\t" \
    "s_or_b64 s[66:67], s[66:67], vcc\n\ts_add_u32 s61, %[sq], 51904512\n\tv_accvgpr_read_b32 v184, a96\n\tv_accvgpr_read_b32 v185, a97\n\tv_accvgpr_read_b32 v186, a98\n\tv_accvgpr_read_b32 v187, a99\n\t" \
    "v_accvgpr_read_b32 v188, a100\n\tv_accvgpr_read_b32 v189, a101\n\tv_accvgpr_read_b32 v190, a102\n\tv_accvgpr_read_b32 v191, a103\n\tv_mul_f32 v184, v155, v184\n\tv_mul_f32 v185, v155, v185\n\t" \
    "v_mul_f32 v186, v155, v186\n\tv_mul_f32 v187, v155, v187\n\tv_mul_f32 v188, v155, v188\n\tv_mul_f32 v189, v155, v189\n\tv_mul_f32 v190, v155, v190\n\tv_mul_f32 v191, v155, v191\n\t" \
    "v_cvt_pk_bf16_f32 v200, v184, v185\n\tv_cvt_pk_bf16_f32 v201, v186, v187\n\tv_cvt_pk_bf16_f32 v202, v188, v189\n\tv_cvt_pk_bf16_f32 v203, v190, v191\n\t" \
    "buffer_store_dwordx4 v[200:203], %[dv], %[rs], s61 offen sc1\n\tv_accvgpr_read_b32 v192, a104\n\tv_accvgpr_read_b32 v193, a105\n\tv_accvgpr_read_b32 v194, a106\n\tv_accvgpr_read_b32 v195, a107\n\t" \
    "v_accvgpr_read_b32 v196, a108\n\tv_accvgpr_read_b32 v197, a109\n\tv_accvgpr_read_b32 v198, a110\n\tv_accvgpr_read_b32 v199, a111\n\tv_mul_f32 v192, v155, v192\n\tv_mul_f32 v193, v155, v193\n\t" \
    "v_mul_f32 v194, v155, v194\n\tv_mul_f32 v195, v155, v195\n\tv_mul_f32 v196, v155, v196\n\tv_mul_f32 v197, v155, v197\n\tv_mul_f32 v198, v155, v198\n\tv_mul_f32 v199, v155, v199\n\t" \
    "v_cvt_pk_bf16_f32 v204, v192, v193\n\tv_cvt_pk_bf16_f32 v205, v194, v195\n\tv_cvt_pk_bf16_f32 v206, v196, v197\n\tv_cvt_pk_bf16_f32 v207, v198, v199\n\t" \
    "buffer_store_dwordx4 v[204:207], %[dv], %[rs], s61 offen offset:2048 sc1\n\tv_accvgpr_read_b32 v184, a112\n\tv_accvgpr_read_b32 v185, a113\n\tv_accvgpr_read_b32 v186, a114\n\t" \
    "v_accvgpr_read_b32 v187, a115\n\tv_accvgpr_read_b32 v188, a116\n\tv_accvgpr_read_b32 v189, a117\n\tv_accvgpr_read_b32 v190, a118\n\tv_accvgpr_read_b32 v191, a119\n\tv_mul_f32 v184, v155, v184\n\t" \
    "v_mul_f32 v185, v155, v185\n\tv_mul_f32 v186, v155, v186\n\tv_mul_f32 v187, v155, v187\n\tv_mul_f32 v188, v155, v188\n\tv_mul_f32 v189, v155, v189\n\tv_mul_f32 v190, v155, v190\n\t" \
    "v_mul_f32 v191, v155, v191\n\tv_cvt_pk_bf16_f32 v200, v184, v185\n\tv_cvt_pk_bf16_f32 v201, v186, v187\n\tv_cvt_pk_bf16_f32 v202, v188, v189\n\tv_cvt_pk_bf16_f32 v203, v190, v191\n\t" \
    "buffer_store_dwordx4 v[200:203], %[dw], %[rs], s61 offen sc1\n\tv_accvgpr_read_b32 v192, a120\n\tv_accvgpr_read_b32 v193, a121\n\tv_accvgpr_read_b32 v194, a122\n\tv_accvgpr_read_b32 v195, a123\n\t" \
    "v_accvgpr_read_b32 v196, a124\n\tv_accvgpr_read_b32 v197, a125\n\tv_accvgpr_read_b32 v198, a126\n\tv_accvgpr_read_b32 v199, a127\n\tv_mul_f32 v192, v155, v192\n\tv_mul_f32 v193, v155, v193\n\t" \
    "v_mul_f32 v194, v155, v194\n\tv_mul_f32 v195, v155, v195\n\tv_mul_f32 v196, v155, v196\n\tv_mul_f32 v197, v155, v197\n\tv_mul_f32 v198, v155, v198\n\tv_mul_f32 v199, v155, v199\n\t" \
    "v_cvt_pk_bf16_f32 v204, v192, v193\n\tv_cvt_pk_bf16_f32 v205, v194, v195\n\tv_cvt_pk_bf16_f32 v206, v196, v197\n\tv_cvt_pk_bf16_f32 v207, v198, v199\n\t" \
    "buffer_store_dwordx4 v[204:207], %[dw], %[rs], s61 offen offset:2048 sc1\n\tv_readfirstlane_b32 %[nx], v212\n\tv_readfirstlane_b32 %[pa], v213\n\ts_mov_b32 m0, s60\n\ts_mov_b64 %[bad], s[66:67]\n\t" \
    "s_branch 9f\n\t6:\ns_mov_b64 exec, vcc\n\tglobal_atomic_add v210, v208, v209, %[li] sc0\n\ts_waitcnt vmcnt(0)\n\tv_lshlrev_b32 v210, 2, v210\n\tv_lshlrev_b32 v210, v210, %[le]\n\t" \
    "global_atomic_add v208, v210, %[li] offset:128\n\ts_mov_b64 exec, -1\n\ts_branch 7b\n\t5:\nv_add_u32 v177, 8192, %[ka]\n\ts_waitcnt lgkmcnt(5)\n\t" \
    "v_mfma_f32_16x16x32_bf16 v[16:19], v[112:115], a[128:131], v[160:163]\n\tds_read_b128 v[136:139], v176 offset:6144\n\tv_mfma_f32_16x16x32_bf16 v[24:27], v[112:115], a[144:147], 0\n\t" \
    "v_mfma_f32_16x16x32_bf16 v[32:35], v[112:115], a[160:163], 0\n\tv_mfma_f32_16x16x32_bf16 v[40:43], v[112:115], a[176:179], 0\n\ts_waitcnt lgkmcnt(5)\n\t" \
    "v_mfma_f32_16x16x32_bf16 v[44:47], v[116:119], a[176:179], 0\n\tds_read_b128 v[140:143], v176 offset:7168\n\tv_mfma_f32_16x16x32_bf16 v[36:39], v[116:119], a[160:163], 0\n\t" \
    "v_mfma_f32_16x16x32_bf16 v[28:31], v[116:119], a[144:147], 0\n\tv_mfma_f32_16x16x32_bf16 v[20:23], v[116:119], a[128:131], v[164:167]\n\ts_waitcnt lgkmcnt(5)\n\t" \
    "v_mfma_f32_16x16x32_bf16 v[16:19], v[120:123], a[132:135], v[16:19]\n\tds_read_b128 v[112:115], v176 offset:8192\n\tv_mfma_f32_16x16x32_bf16 v[24:27], v[120:123], a[148:151], v[24:27]\n\t" \
    "v_mfma_f32_16x16x32_bf16 v[32:35], v[120:123], a[164:167], v[32:35]\n\tv_mfma_f32_16x16x32_bf16 v[40:43], v[120:123], a[180:183], v[40:43]\n\ts_waitcnt lgkmcnt(5)\n\t" \
    "v_mfma_f32_16x16x32_bf16 v[44:47], v[124:127], a[180:183], v[44:47]\n\tds_read_b128 v[116:119], v176 offset:9216\n\tv_mfma_f32_16x16x32_bf16 v[36:39], v[124:127], a[164:167], v[36:39]\n\t" \
    "v_mfma_f32_16x16x32_bf16 v[28:31], v[124:127], a[148:151], v[28:31]\n\tv_mfma_f32_16x16x32_bf16 v[20:23], v[124:127], a[132:135], v[20:23]\n\ts_waitcnt lgkmcnt(5)\n\t" \
    "v_mfma_f32_16x16x32_bf16 v[16:19], v[128:131], a[136:139], v[16:19]\n\tds_read_b128 v[120:123], v176 offset:10240\n\tv_mfma_f32_16x16x32_bf16 v[24:27], v[128:131], a[152:155], v[24:27]\n\t" \
    "v_mfma_f32_16x16x32_bf16 v[32:35], v[128:131], a[168:171], v[32:35]\n\tv_mfma_f32_16x16x32_bf16 v[40:43], v[128:131], a[184:187], v[40:43]\n\ts_waitcnt lgkmcnt(5)\n\t" \
    "v_mfma_f32_16x16x32_bf16 v[44:47], v[132:135], a[184:187], v[44:47]\n\tds_read_b128 v[124:127], v176 offset:11264\n\tv_mfma_f32_16x16x32_bf16 v[36:39], v[132:135], a[168:171], v[36:39]\n\t" \
    "v_mfma_f32_16x16x32_bf16 v[28:31], v[132:135], a[152:155], v[28:31]\n\tv_mfma_f32_16x16x32_bf16 v[20:23], v[132:135], a[136:139], v[20:23]\n\ts_waitcnt lgkmcnt(5)\n\t" \
    "v_mfma_f32_16x16x32_bf16 v[16:19], v[136:139], a[140:143], v[16:19]\n\tds_read_b128 v[128:131], v176 offset:12288\n\tv_mfma_f32_16x16x32_bf16 v[24:27], v[136:139], a[156:159], v[24:27]\n\t" \
    "v_mfma_f32_16x16x32_bf16 v[32:35], v[136:139], a[172:175], v[32:35]\n\tv_mfma_f32_16x16x32_bf16 v[40:43], v[136:139], a[188:191], v[40:43]\n\ts_waitcnt lgkmcnt(5)\n\t" \
    "v_mfma_f32_16x16x32_bf16 v[44:47], v[140:143], a[188:191], v[44:47]\n\tds_read_b128 v[132:135], v176 offset:13312\n\tv_mfma_f32_16x16x32_bf16 v[36:39], v[140:143], a[172:175], v[36:39]\n\t" \
    "v_mfma_f32_16x16x32_bf16 v[28:31], v[140:143], a[156:159], v[28:31]\n\tv_mfma_f32_16x16x32_bf16 v[20:23], v[140:143], a[140:143], v[20:23]\n\ts_waitcnt lgkmcnt(5)\n\ts_branch 3b\n\t9:\n"
#define ASM_STAGE_VOX \
    "s_mov_b32 s60, m0\n\tv_add_u32 v16, 0x10000, %[a0]\n\tv_add_u32 v17, 0x10000, %[b0]\n\tv_add_u32 v18, 0x10000, %[b1]\n\tv_add_u32 v19, 0x10000, %[b2]\n\tv_add_u32 v20, 0x10000, %[b3]\n\t" \
    "s_add_u32 m0, %[lb], 0\n\ts_mov_b32 s61, 0\n\tbuffer_load_dwordx4 %[vv], %[rv], s61 offen lds\n\ts_add_u32 m0, %[lb], 1024\n\ts_mov_b32 s61, 131072\n\t" \
    "buffer_load_dwordx4 %[vv], %[rv], s61 offen lds\n\ts_add_u32 m0, %[lb], 2048\n\ts_mov_b32 s61, 262144\n\tbuffer_load_dwordx4 %[vv], %[rv], s61 offen lds\n\ts_add_u32 m0, %[lb], 3072\n\t" \
    "s_mov_b32 s61, 393216\n\tbuffer_load_dwordx4 %[vv], %[rv], s61 offen lds\n\ts_add_u32 m0, %[lb], 16384\n\ts_mov_b32 s61, 524288\n\tbuffer_load_dwordx4 %[vv], %[rv], s61 offen lds\n\t" \
    "s_add_u32 m0, %[lb], 17408\n\ts_mov_b32 s61, 655360\n\tbuffer_load_dwordx4 %[vv], %[rv], s61 offen lds\n\ts_add_u32 m0, %[lb], 18432\n\ts_mov_b32 s61, 786432\n\t" \
    "buffer_load_dwordx4 %[vv], %[rv], s61 offen lds\n\ts_add_u32 m0, %[lb], 19456\n\ts_mov_b32 s61, 917504\n\tbuffer_load_dwordx4 %[vv], %[rv], s61 offen lds\n\ts_add_u32 m0, %[lb], 32768\n\t" \
    "s_mov_b32 s61, 1048576\n\tbuffer_load_dwordx4 %[vv], %[rv], s61 offen lds\n\ts_add_u32 m0, %[lb], 33792\n\ts_mov_b32 s61, 1179648\n\tbuffer_load_dwordx4 %[vv], %[rv], s61 offen lds\n\t" \
    "s_add_u32 m0, %[lb], 34816\n\ts_mov_b32 s61, 1310720\n\tbuffer_load_dwordx4 %[vv], %[rv], s61 offen lds\n\ts_add_u32 m0, %[lb], 35840\n\ts_mov_b32 s61, 1441792\n\t" \
    "buffer_load_dwordx4 %[vv], %[rv], s61 offen lds\n\ts_add_u32 m0, %[lb], 49152\n\ts_mov_b32 s61, 1572864\n\tbuffer_load_dwordx4 %[vv], %[rv], s61 offen lds\n\ts_add_u32 m0, %[lb], 50176\n\t" \
    "s_mov_b32 s61, 1703936\n\tbuffer_load_dwordx4 %[vv], %[rv], s61 offen lds\n\ts_add_u32 m0, %[lb], 51200\n\ts_mov_b32 s61, 1835008\n\tbuffer_load_dwordx4 %[vv], %[rv], s61 offen lds\n\t" \
    "s_add_u32 m0, %[lb], 52224\n\ts_mov_b32 s61, 1966080\n\tbuffer_load_dwordx4 %[vv], %[rv], s61 offen lds\n\ts_add_u32 m0, %[lb], 65536\n\ts_mov_b32 s61, 2097152\n\t" \
    "buffer_load_dwordx4 %[vv], %[rv], s61 offen lds\n\ts_add_u32 m0, %[lb], 66560\n\ts_mov_b32 s61, 2228224\n\tbuffer_load_dwordx4 %[vv], %[rv], s61 offen lds\n\ts_add_u32 m0, %[lb], 67584\n\t" \
    "s_mov_b32 s61, 2359296\n\tbuffer_load_dwordx4 %[vv], %[rv], s61 offen lds\n\ts_add_u32 m0, %[lb], 68608\n\ts_mov_b32 s61, 2490368\n\tbuffer_load_dwordx4 %[vv], %[rv], s61 offen lds\n\t" \
    "s_add_u32 m0, %[lb], 81920\n\ts_mov_b32 s61, 2621440\n\tbuffer_load_dwordx4 %[vv], %[rv], s61 offen lds\n\ts_add_u32 m0, %[lb], 82944\n\ts_mov_b32 s61, 2752512\n\t" \
    "buffer_load_dwordx4 %[vv], %[rv], s61 offen lds\n\ts_add_u32 m0, %[lb], 83968\n\ts_mov_b32 s61, 2883584\n\tbuffer_load_dwordx4 %[vv], %[rv], s61 offen lds\n\ts_add_u32 m0, %[lb], 84992\n\t" \
    "s_mov_b32 s61, 3014656\n\tbuffer_load_dwordx4 %[vv], %[rv], s61 offen lds\n\ts_add_u32 m0, %[lb], 98304\n\ts_mov_b32 s61, 3145728\n\tbuffer_load_dwordx4 %[vv], %[rv], s61 offen lds\n\t" \
    "s_add_u32 m0, %[lb], 99328\n\ts_mov_b32 s61, 3276800\n\tbuffer_load_dwordx4 %[vv], %[rv], s61 offen lds\n\ts_add_u32 m0, %[lb], 100352\n\ts_mov_b32 s61, 3407872\n\t" \
    "buffer_load_dwordx4 %[vv], %[rv], s61 offen lds\n\ts_add_u32 m0, %[lb], 101376\n\ts_mov_b32 s61, 3538944\n\tbuffer_load_dwordx4 %[vv], %[rv], s61 offen lds\n\ts_add_u32 m0, %[lb], 114688\n\t" \
    "s_mov_b32 s61, 3670016\n\tbuffer_load_dwordx4 %[vv], %[rv], s61 offen lds\n\ts_add_u32 m0, %[lb], 115712\n\ts_mov_b32 s61, 3801088\n\tbuffer_load_dwordx4 %[vv], %[rv], s61 offen lds\n\t" \
    "s_add_u32 m0, %[lb], 116736\n\ts_mov_b32 s61, 3932160\n\tbuffer_load_dwordx4 %[vv], %[rv], s61 offen lds\n\ts_add_u32 m0, %[lb], 117760\n\ts_mov_b32 s61, 4063232\n\t" \
    "buffer_load_dwordx4 %[vv], %[rv], s61 offen lds\n\ts_waitcnt vmcnt(28)\n\tds_read_b64_tr_b16 a[0:1], %[a0]\n\tds_read_b64_tr_b16 a[64:65], %[a0] offset:8\n\t" \
    "ds_read_b64_tr_b16 a[2:3], %[a0] offset:256\n\tds_read_b64_tr_b16 a[66:67], %[a0] offset:264\n\tds_read_b64_tr_b16 a[4:5], %[a0] offset:2048\n\tds_read_b64_tr_b16 a[68:69], %[a0] offset:2056\n\t" \
    "ds_read_b64_tr_b16 a[6:7], %[a0] offset:2304\n\tds_read_b64_tr_b16 a[70:71], %[a0] offset:2312\n\ts_waitcnt lgkmcnt(0)\n\ts_add_u32 m0, %[lb], 0\n\ts_mov_b32 s61, 0\n\t" \
    "buffer_load_dwordx4 %[vo], %[ro], s61 offen lds\n\ts_add_u32 m0, %[lb], 1024\n\ts_mov_b32 s61, 8192\n\tbuffer_load_dwordx4 %[vo], %[ro], s61 offen lds\n\ts_add_u32 m0, %[lb], 2048\n\t" \
    "s_mov_b32 s61, 16384\n\tbuffer_load_dwordx4 %[vo], %[ro], s61 offen lds\n\ts_add_u32 m0, %[lb], 3072\n\ts_mov_b32 s61, 24576\n\tbuffer_load_dwordx4 %[vo], %[ro], s61 offen lds\n\t" \
    "s_waitcnt vmcnt(28)\n\tds_read_b64_tr_b16 a[8:9], %[a0] offset:16384\n\tds_read_b64_tr_b16 a[72:73], %[a0] offset:16392\n\tds_read_b64_tr_b16 a[10:11], %[a0] offset:16640\n\t" \
    "ds_read_b64_tr_b16 a[74:75], %[a0] offset:16648\n\tds_read_b64_tr_b16 a[12:13], %[a0] offset:18432\n\tds_read_b64_tr_b16 a[76:77], %[a0] offset:18440\n\t" \
    "ds_read_b64_tr_b16 a[14:15], %[a0] offset:18688\n\tds_read_b64_tr_b16 a[78:79], %[a0] offset:18696\n\ts_waitcnt lgkmcnt(0)\n\ts_add_u32 m0, %[lb], 16384\n\ts_mov_b32 s61, 4096\n\t" \
    "buffer_load_dwordx4 %[vo], %[ro], s61 offen lds\n\ts_add_u32 m0, %[lb], 17408\n\ts_mov_b32 s61, 12288\n\tbuffer_load_dwordx4 %[vo], %[ro], s61 offen lds\n\ts_add_u32 m0, %[lb], 18432\n\t" \
    "s_mov_b32 s61, 20480\n\tbuffer_load_dwordx4 %[vo], %[ro], s61 offen lds\n\ts_add_u32 m0, %[lb], 19456\n\ts_mov_b32 s61, 28672\n\tbuffer_load_dwordx4 %[vo], %[ro], s61 offen lds\n\t" \
    "s_waitcnt vmcnt(28)\n\tds_read_b64_tr_b16 a[16:17], %[a0] offset:32768\n\tds_read_b64_tr_b16 a[80:81], %[a0] offset:32776\n\tds_read_b64_tr_b16 a[18:19], %[a0] offset:33024\n\t" \
    "ds_read_b64_tr_b16 a[82:83], %[a0] offset:33032\n\tds_read_b64_tr_b16 a[20:21], %[a0] offset:34816\n\tds_read_b64_tr_b16 a[84:85], %[a0] offset:34824\n\t" \
    "ds_read_b64_tr_b16 a[22:23], %[a0] offset:35072\n\tds_read_b64_tr_b16 a[86:87], %[a0] offset:35080\n\ts_waitcnt lgkmcnt(0)\n\ts_add_u32 m0, %[lb], 32768\n\ts_mov_b32 s61, 32768\n\t" \
    "buffer_load_dwordx4 %[vo], %[ro], s61 offen lds\n\ts_add_u32 m0, %[lb], 33792\n\ts_mov_b32 s61, 40960\n\tbuffer_load_dwordx4 %[vo], %[ro], s61 offen lds\n\ts_add_u32 m0, %[lb], 34816\n\t" \
    "s_mov_b32 s61, 49152\n\tbuffer_load_dwordx4 %[vo], %[ro], s61 offen lds\n\ts_add_u32 m0, %[lb], 35840\n\ts_mov_b32 s61, 57344\n\tbuffer_load_dwordx4 %[vo], %[ro], s61 offen lds\n\t" \
    "s_waitcnt vmcnt(28)\n\tds_read_b64_tr_b16 a[24:25], %[a0] offset:49152\n\tds_read_b64_tr_b16 a[88:89], %[a0] offset:49160\n\tds_read_b64_tr_b16 a[26:27], %[a0] offset:49408\n\t" \
    "ds_read_b64_tr_b16 a[90:91], %[a0] offset:49416\n\tds_read_b64_tr_b16 a[28:29], %[a0] offset:51200\n\tds_read_b64_tr_b16 a[92:93], %[a0] offset:51208\n\t" \
    "ds_read_b64_tr_b16 a[30:31], %[a0] offset:51456\n\tds_read_b64_tr_b16 a[94:95], %[a0] offset:51464\n\ts_waitcnt lgkmcnt(0)\n\ts_add_u32 m0, %[lb], 49152\n\ts_mov_b32 s61, 36864\n\t" \
    "buffer_load_dwordx4 %[vo], %[ro], s61 offen lds\n\ts_add_u32 m0, %[lb], 50176\n\ts_mov_b32 s61, 45056\n\tbuffer_load_dwordx4 %[vo], %[ro], s61 offen lds\n\ts_add_u32 m0, %[lb], 51200\n\t" \
    "s_mov_b32 s61, 53248\n\tbuffer_load_dwordx4 %[vo], %[ro], s61 offen lds\n\ts_add_u32 m0, %[lb], 52224\n\ts_mov_b32 s61, 61440\n\tbuffer_load_dwordx4 %[vo], %[ro], s61 offen lds\n\t" \
    "s_waitcnt vmcnt(28)\n\tds_read_b64_tr_b16 a[32:33], v16\n\tds_read_b64_tr_b16 a[96:97], v16 offset:8\n\tds_read_b64_tr_b16 a[34:35], v16 offset:256\n\tds_read_b64_tr_b16 a[98:99], v16 offset:264\n\t" \
    "ds_read_b64_tr_b16 a[36:37], v16 offset:2048\n\tds_read_b64_tr_b16 a[100:101], v16 offset:2056\n\tds_read_b64_tr_b16 a[38:39], v16 offset:2304\n\tds_read_b64_tr_b16 a[102:103], v16 offset:2312\n\t" \
    "s_waitcnt lgkmcnt(0)\n\ts_add_u32 m0, %[lb], 65536\n\ts_mov_b32 s61, 65536\n\tbuffer_load_dwordx4 %[vo], %[ro], s61 offen lds\n\ts_add_u32 m0, %[lb], 66560\n\ts_mov_b32 s61, 73728\n\t" \
    "buffer_load_dwordx4 %[vo], %[ro], s61 offen lds\n\ts_add_u32 m0, %[lb], 67584\n\ts_mov_b32 s61, 81920\n\tbuffer_load_dwordx4 %[vo], %[ro], s61 offen lds\n\ts_add_u32 m0, %[lb], 68608\n\t" \
    "s_mov_b32 s61, 90112\n\tbuffer_load_dwordx4 %[vo], %[ro], s61 offen lds\n\ts_waitcnt vmcnt(28)\n\tds_read_b64_tr_b16 a[40:41], v16 offset:16384\n\tds_read_b64_tr_b16 a[104:105], v16 offset:16392\n\t" \
    "ds_read_b64_tr_b16 a[42:43], v16 offset:16640\n\tds_read_b64_tr_b16 a[106:107], v16 offset:16648\n\tds_read_b64_tr_b16 a[44:45], v16 offset:18432\n\tds_read_b64_tr_b16 a[108:109], v16 offset:18440\n\t" \
    "ds_read_b64_tr_b16 a[46:47], v16 offset:18688\n\tds_read_b64_tr_b16 a[110:111], v16 offset:18696\n\ts_waitcnt lgkmcnt(0)\n\ts_add_u32 m0, %[lb], 81920\n\ts_mov_b32 s61, 69632\n\t" \
    "buffer_load_dwordx4 %[vo], %[ro], s61 offen lds\n\ts_add_u32 m0, %[lb], 82944\n\ts_mov_b32 s61, 77824\n\tbuffer_load_dwordx4 %[vo], %[ro], s61 offen lds\n\ts_add_u32 m0, %[lb], 83968\n\t" \
    "s_mov_b32 s61, 86016\n\tbuffer_load_dwordx4 %[vo], %[ro], s61 offen lds\n\ts_add_u32 m0, %[lb], 84992\n\ts_mov_b32 s61, 94208\n\tbuffer_load_dwordx4 %[vo], %[ro], s61 offen lds\n\t" \
    "s_waitcnt vmcnt(28)\n\tds_read_b64_tr_b16 a[48:49], v16 offset:32768\n\tds_read_b64_tr_b16 a[112:113], v16 offset:32776\n\tds_read_b64_tr_b16 a[50:51], v16 offset:33024\n\t" \
    "ds_read_b64_tr_b16 a[114:115], v16 offset:33032\n\tds_read_b64_tr_b16 a[52:53], v16 offset:34816\n\tds_read_b64_tr_b16 a[116:117], v16 offset:34824\n\tds_read_b64_tr_b16 a[54:55], v16 offset:35072\n\t" \
    "ds_read_b64_tr_b16 a[118:119], v16 offset:35080\n\ts_waitcnt lgkmcnt(0)\n\ts_add_u32 m0, %[lb], 98304\n\ts_mov_b32 s61, 98304\n\tbuffer_load_dwordx4 %[vo], %[ro], s61 offen lds\n\t" \
    "s_add_u32 m0, %[lb], 99328\n\ts_mov_b32 s61, 106496\n\tbuffer_load_dwordx4 %[vo], %[ro], s61 offen lds\n\ts_add_u32 m0, %[lb], 100352\n\ts_mov_b32 s61, 114688\n\t" \
    "buffer_load_dwordx4 %[vo], %[ro], s61 offen lds\n\ts_add_u32 m0, %[lb], 101376\n\ts_mov_b32 s61, 122880\n\tbuffer_load_dwordx4 %[vo], %[ro], s61 offen lds\n\ts_waitcnt vmcnt(28)\n\t" \
    "ds_read_b64_tr_b16 a[56:57], v16 offset:49152\n\tds_read_b64_tr_b16 a[120:121], v16 offset:49160\n\tds_read_b64_tr_b16 a[58:59], v16 offset:49408\n\tds_read_b64_tr_b16 a[122:123], v16 offset:49416\n\t" \
    "ds_read_b64_tr_b16 a[60:61], v16 offset:51200\n\tds_read_b64_tr_b16 a[124:125], v16 offset:51208\n\tds_read_b64_tr_b16 a[62:63], v16 offset:51456\n\tds_read_b64_tr_b16 a[126:127], v16 offset:51464\n\t" \
    "s_waitcnt lgkmcnt(0)\n\ts_add_u32 m0, %[lb], 114688\n\ts_mov_b32 s61, 102400\n\tbuffer_load_dwordx4 %[vo], %[ro], s61 offen lds\n\ts_add_u32 m0, %[lb], 115712\n\ts_mov_b32 s61, 110592\n\t" \
    "buffer_load_dwordx4 %[vo], %[ro], s61 offen lds\n\ts_add_u32 m0, %[lb], 116736\n\ts_mov_b32 s61, 118784\n\tbuffer_load_dwordx4 %[vo], %[ro], s61 offen lds\n\ts_add_u32 m0, %[lb], 117760\n\t" \
    "s_mov_b32 s61, 126976\n\tbuffer_load_dwordx4 %[vo], %[ro], s61 offen lds\n\ts_waitcnt vmcnt(28)\n\tds_read_b64_tr_b16 a[128:129], %[b0]\n\tds_read_b64_tr_b16 a[144:145], %[b1]\n\t" \
    "ds_read_b64_tr_b16 a[160:161], %[b2]\n\tds_read_b64_tr_b16 a[176:177], %[b3]\n\tds_read_b64_tr_b16 a[192:193], %[b0] offset:128\n\tds_read_b64_tr_b16 a[208:209], %[b1] offset:128\n\t" \
    "ds_read_b64_tr_b16 a[224:225], %[b2] offset:128\n\tds_read_b64_tr_b16 a[240:241], %[b3] offset:128\n\ts_waitcnt lgkmcnt(0)\n\ts_waitcnt vmcnt(24)\n\t" \
    "ds_read_b64_tr_b16 a[130:131], %[b0] offset:16384\n\tds_read_b64_tr_b16 a[146:147], %[b1] offset:16384\n\tds_read_b64_tr_b16 a[162:163], %[b2] offset:16384\n\t" \
    "ds_read_b64_tr_b16 a[178:179], %[b3] offset:16384\n\tds_read_b64_tr_b16 a[194:195], %[b0] offset:16512\n\tds_read_b64_tr_b16 a[210:211], %[b1] offset:16512\n\t" \
    "ds_read_b64_tr_b16 a[226:227], %[b2] offset:16512\n\tds_read_b64_tr_b16 a[242:243], %[b3] offset:16512\n\ts_waitcnt lgkmcnt(0)\n\ts_waitcnt vmcnt(20)\n\t" \
    "ds_read_b64_tr_b16 a[132:133], %[b0] offset:32768\n\tds_read_b64_tr_b16 a[148:149], %[b1] offset:32768\n\tds_read_b64_tr_b16 a[164:165], %[b2] offset:32768\n\t" \
    "ds_read_b64_tr_b16 a[180:181], %[b3] offset:32768\n\tds_read_b64_tr_b16 a[196:197], %[b0] offset:32896\n\tds_read_b64_tr_b16 a[212:213], %[b1] offset:32896\n\t" \
    "ds_read_b64_tr_b16 a[228:229], %[b2] offset:32896\n\tds_read_b64_tr_b16 a[244:245], %[b3] offset:32896\n\ts_waitcnt lgkmcnt(0)\n\ts_waitcnt vmcnt(16)\n\t" \
    "ds_read_b64_tr_b16 a[134:135], %[b0] offset:49152\n\tds_read_b64_tr_b16 a[150:151], %[b1] offset:49152\n\tds_read_b64_tr_b16 a[166:167], %[b2] offset:49152\n\t" \
    "ds_read_b64_tr_b16 a[182:183], %[b3] offset:49152\n\tds_read_b64_tr_b16 a[198:199], %[b0] offset:49280\n\tds_read_b64_tr_b16 a[214:215], %[b1] offset:49280\n\t" \
    "ds_read_b64_tr_b16 a[230:231], %[b2] offset:49280\n\tds_read_b64_tr_b16 a[246:247], %[b3] offset:49280\n\ts_waitcnt lgkmcnt(0)\n\ts_waitcnt vmcnt(12)\n\tds_read_b64_tr_b16 a[136:137], v17\n\t" \
    "ds_read_b64_tr_b16 a[152:153], v18\n\tds_read_b64_tr_b16 a[168:169], v19\n\tds_read_b64_tr_b16 a[184:185], v20\n\tds_read_b64_tr_b16 a[200:201], v17 offset:128\n\t" \
    "ds_read_b64_tr_b16 a[216:217], v18 offset:128\n\tds_read_b64_tr_b16 a[232:233], v19 offset:128\n\tds_read_b64_tr_b16 a[248:249], v20 offset:128\n\ts_waitcnt lgkmcnt(0)\n\ts_waitcnt vmcnt(8)\n\t" \
    "ds_read_b64_tr_b16 a[138:139], v17 offset:16384\n\tds_read_b64_tr_b16 a[154:155], v18 offset:16384\n\tds_read_b64_tr_b16 a[170:171], v19 offset:16384\n\t" \
    "ds_read_b64_tr_b16 a[186:187], v20 offset:16384\n\tds_read_b64_tr_b16 a[202:203], v17 offset:16512\n\tds_read_b64_tr_b16 a[218:219], v18 offset:16512\n\t" \
    "ds_read_b64_tr_b16 a[234:235], v19 offset:16512\n\tds_read_b64_tr_b16 a[250:251], v20 offset:16512\n\ts_waitcnt lgkmcnt(0)\n\ts_waitcnt vmcnt(4)\n\tds_read_b64_tr_b16 a[140:141], v17 offset:32768\n\t" \
    "ds_read_b64_tr_b16 a[156:157], v18 offset:32768\n\tds_read_b64_tr_b16 a[172:173], v19 offset:32768\n\tds_read_b64_tr_b16 a[188:189], v20 offset:32768\n\t" \
    "ds_read_b64_tr_b16 a[204:205], v17 offset:32896\n\tds_read_b64_tr_b16 a[220:221], v18 offset:32896\n\tds_read_b64_tr_b16 a[236:237], v19 offset:32896\n\t" \
    "ds_read_b64_tr_b16 a[252:253], v20 offset:32896\n\ts_waitcnt lgkmcnt(0)\n\ts_waitcnt vmcnt(0)\n\tds_read_b64_tr_b16 a[142:143], v17 offset:49152\n\tds_read_b64_tr_b16 a[158:159], v18 offset:49152\n\t" \
    "ds_read_b64_tr_b16 a[174:175], v19 offset:49152\n\tds_read_b64_tr_b16 a[190:191], v20 offset:49152\n\tds_read_b64_tr_b16 a[206:207], v17 offset:49280\n\t" \
    "ds_read_b64_tr_b16 a[222:223], v18 offset:49280\n\tds_read_b64_tr_b16 a[238:239], v19 offset:49280\n\tds_read_b64_tr_b16 a[254:255], v20 offset:49280\n\ts_waitcnt lgkmcnt(0)\n\ts_mov_b32 m0, s60\n\t"
#define ASM_VOX \
    "s_mov_b32 s60, m0\n\ts_mov_b32 s61, 0\n\tv_add_u32 v168, 0x18000, %[xb]\n\tv_xor_b32 v169, 64, v168\n\tv_bfrev_b32 v170, 1\n\tv_mov_b32 v171, 0\n\tds_read_b128 v[16:19], v168\n\t" \
    "ds_read_b128 v[20:23], v169\n\tds_read_b128 v[24:27], v168 offset:1024\n\tds_read_b128 v[28:31], v169 offset:1024\n\tds_read_b128 v[32:35], v168 offset:2048\n\ts_waitcnt lgkmcnt(4)\n\t1:\n" \
    "v_mfma_f32_16x16x32_bf16 v[48:51], a[0:3], v[16:19], 0\n\tds_read_b128 v[36:39], v169 offset:2048\n\tv_mfma_f32_16x16x32_bf16 v[52:55], a[64:67], v[16:19], 0\n\ts_waitcnt lgkmcnt(4)\n\t" \
    "s_lshl_b32 s62, s61, 15\n\tv_mfma_f32_16x16x32_bf16 v[52:55], a[68:71], v[20:23], v[52:55]\n\tds_read_b128 v[40:43], v168 offset:3072\n\tv_cvt_pk_bf16_f32 v154, v120, v121\n\t" \
    "v_mfma_f32_16x16x32_bf16 v[48:51], a[4:7], v[20:23], v[48:51]\n\ts_waitcnt lgkmcnt(4)\n\ts_and_b32 s62, s62, 0x18000\n\tv_cvt_pk_bf16_f32 v155, v122, v123\n\t" \
    "v_mfma_f32_16x16x32_bf16 v[48:51], a[8:11], v[24:27], v[48:51]\n\tds_read_b128 v[44:47], v169 offset:3072\n\tbuffer_store_dwordx4 v[152:155], v170, %[rw], %[sp] offen sc1\n\t" \
    "v_mfma_f32_16x16x32_bf16 v[52:55], a[72:75], v[24:27], v[52:55]\n\ts_waitcnt lgkmcnt(4)\n\ts_add_u32 %[sp], %[sp], 32768\n\tv_cvt_pk_bf16_f32 v158, v124, v125\n\t" \
    "v_mfma_f32_16x16x32_bf16 v[52:55], a[76:79], v[28:31], v[52:55]\n\tds_read_b128 v[16:19], v168 offset:4096\n\tv_cvt_pk_bf16_f32 v159, v126, v127\n\t" \
    "v_mfma_f32_16x16x32_bf16 v[48:51], a[12:15], v[28:31], v[48:51]\n\ts_waitcnt lgkmcnt(4)\n\tbuffer_store_dwordx4 v[156:159], v170, %[rw], %[sp] offen sc1\n\ts_add_u32 %[sp], %[sp], 32768\n\t" \
    "v_mfma_f32_16x16x32_bf16 v[48:51], a[16:19], v[32:35], v[48:51]\n\tds_read_b128 v[20:23], v169 offset:4096\n\tv_cvt_pk_bf16_f32 v162, v128, v129\n\t" \
    "v_mfma_f32_16x16x32_bf16 v[52:55], a[80:83], v[32:35], v[52:55]\n\ts_waitcnt lgkmcnt(4)\n\ts_xor_b32 s63, s62, 0x10000\n\tv_cvt_pk_bf16_f32 v163, v130, v131\n\t" \
    "v_mfma_f32_16x16x32_bf16 v[52:55], a[84:87], v[36:39], v[52:55]\n\tds_read_b128 v[24:27], v168 offset:5120\n\tbuffer_store_dwordx4 v[160:163], v170, %[rw], %[sp] offen sc1\n\t" \
    "v_mfma_f32_16x16x32_bf16 v[48:51], a[20:23], v[36:39], v[48:51]\n\ts_waitcnt lgkmcnt(4)\n\ts_add_u32 m0, s63, %[lb]\n\ts_add_u32 %[sp], %[sp], 32768\n\tv_cvt_pk_bf16_f32 v166, v132, v133\n\t" \
    "v_mfma_f32_16x16x32_bf16 v[48:51], a[24:27], v[40:43], v[48:51]\n\tds_read_b128 v[28:31], v169 offset:5120\n\tv_cvt_pk_bf16_f32 v167, v134, v135\n\t" \
    "v_mfma_f32_16x16x32_bf16 v[52:55], a[88:91], v[40:43], v[52:55]\n\ts_waitcnt lgkmcnt(4)\n\tbuffer_store_dwordx4 v[164:167], v170, %[rw], %[sp] offen sc1\n\ts_add_u32 %[sp], %[sp], 819200\n\t" \
    "v_mfma_f32_16x16x32_bf16 v[52:55], a[92:95], v[44:47], v[52:55]\n\tds_read_b128 v[32:35], v168 offset:6144\n\tv_mov_b32 v170, %[st]\n\t" \
    "v_mfma_f32_16x16x32_bf16 v[48:51], a[28:31], v[44:47], v[48:51]\n\ts_waitcnt lgkmcnt(4)\n\tv_mov_b32 v172, s61\n\tv_mfma_f32_16x16x32_bf16 v[48:51], a[32:35], v[16:19], v[48:51]\n\t" \
    "ds_read_b128 v[36:39], v169 offset:6144\n\tv_mfma_f32_16x16x32_bf16 v[52:55], a[96:99], v[16:19], v[52:55]\n\ts_waitcnt lgkmcnt(4)\n\t" \
    "v_mfma_f32_16x16x32_bf16 v[52:55], a[100:103], v[20:23], v[52:55]\n\tds_read_b128 v[40:43], v168 offset:7168\n\tv_mfma_f32_16x16x32_bf16 v[48:51], a[36:39], v[20:23], v[48:51]\n\t" \
    "s_waitcnt lgkmcnt(4)\n\tv_mfma_f32_16x16x32_bf16 v[48:51], a[40:43], v[24:27], v[48:51]\n\tds_read_b128 v[44:47], v169 offset:7168\n\t" \
    "v_mfma_f32_16x16x32_bf16 v[52:55], a[104:107], v[24:27], v[52:55]\n\ts_waitcnt lgkmcnt(4)\n\tv_mfma_f32_16x16x32_bf16 v[52:55], a[108:111], v[28:31], v[52:55]\n\t" \
    "ds_read_b128 v[16:19], v168 offset:8192\n\tv_mfma_f32_16x16x32_bf16 v[48:51], a[44:47], v[28:31], v[48:51]\n\ts_waitcnt lgkmcnt(4)\n\t" \
    "v_mfma_f32_16x16x32_bf16 v[48:51], a[48:51], v[32:35], v[48:51]\n\tds_read_b128 v[20:23], v169 offset:8192\n\tv_mfma_f32_16x16x32_bf16 v[52:55], a[112:115], v[32:35], v[52:55]\n\t" \
    "s_waitcnt lgkmcnt(4)\n\tv_mfma_f32_16x16x32_bf16 v[52:55], a[116:119], v[36:39], v[52:55]\n\tds_read_b128 v[24:27], v168 offset:9216\n\t" \
    "v_mfma_f32_16x16x32_bf16 v[48:51], a[52:55], v[36:39], v[48:51]\n\ts_waitcnt lgkmcnt(4)\n\tv_mfma_f32_16x16x32_bf16 v[48:51], a[56:59], v[40:43], v[48:51]\n\t" \
    "ds_read_b128 v[28:31], v169 offset:9216\n\tv_mfma_f32_16x16x32_bf16 v[52:55], a[120:123], v[40:43], v[52:55]\n\ts_waitcnt lgkmcnt(4)\n\t" \
    "v_mfma_f32_16x16x32_bf16 v[52:55], a[124:127], v[44:47], v[52:55]\n\tds_read_b128 v[32:35], v168 offset:10240\n\tv_mfma_f32_16x16x32_bf16 v[48:51], a[60:63], v[44:47], v[48:51]\n\t" \
    "s_waitcnt lgkmcnt(4)\n\tv_mfma_f32_16x16x32_bf16 v[56:59], a[0:3], v[16:19], 0\n\tds_read_b128 v[36:39], v169 offset:10240\n\tv_mfma_f32_16x16x32_bf16 v[60:63], a[64:67], v[16:19], 0\n\t" \
    "s_waitcnt lgkmcnt(4)\n\tv_mfma_f32_16x16x32_bf16 v[60:63], a[68:71], v[20:23], v[60:63]\n\tds_read_b128 v[40:43], v168 offset:11264\n\tv_cvt_pk_bf16_f32 v64, v48, v49\n\t" \
    "v_mfma_f32_16x16x32_bf16 v[56:59], a[4:7], v[20:23], v[56:59]\n\ts_waitcnt lgkmcnt(4)\n\tv_cvt_pk_bf16_f32 v65, v50, v51\n\tv_cvt_pk_bf16_f32 v66, v52, v53\n\t" \
    "v_mfma_f32_16x16x32_bf16 v[56:59], a[8:11], v[24:27], v[56:59]\n\tds_read_b128 v[44:47], v169 offset:11264\n\tv_cvt_pk_bf16_f32 v67, v54, v55\n\t" \
    "v_mfma_f32_16x16x32_bf16 v[60:63], a[72:75], v[24:27], v[60:63]\n\ts_waitcnt lgkmcnt(4)\n\tds_write_b128 %[vw], v[64:67]\n\tv_mfma_f32_16x16x32_bf16 v[60:63], a[76:79], v[28:31], v[60:63]\n\t" \
    "ds_read_b128 v[16:19], v168 offset:12288\n\tv_mfma_f32_16x16x32_bf16 v[56:59], a[12:15], v[28:31], v[56:59]\n\ts_waitcnt lgkmcnt(5)\n\t" \
    "v_mfma_f32_16x16x32_bf16 v[56:59], a[16:19], v[32:35], v[56:59]\n\tds_read_b128 v[20:23], v169 offset:12288\n\tv_mfma_f32_16x16x32_bf16 v[60:63], a[80:83], v[32:35], v[60:63]\n\t" \
    "s_waitcnt lgkmcnt(5)\n\tv_mfma_f32_16x16x32_bf16 v[60:63], a[84:87], v[36:39], v[60:63]\n\tds_read_b128 v[24:27], v168 offset:13312\n\t" \
    "v_mfma_f32_16x16x32_bf16 v[56:59], a[20:23], v[36:39], v[56:59]\n\ts_waitcnt lgkmcnt(5)\n\tv_mfma_f32_16x16x32_bf16 v[56:59], a[24:27], v[40:43], v[56:59]\n\ts_waitcnt lgkmcnt(3)\n\ts_barrier\n\t" \
    "ds_read_b128 v[28:31], v169 offset:13312\n\tv_mfma_f32_16x16x32_bf16 v[60:63], a[88:91], v[40:43], v[60:63]\n\tds_read_b128 v[72:75], %[vi]\n\t" \
    "v_mfma_f32_16x16x32_bf16 v[60:63], a[92:95], v[44:47], v[60:63]\n\tds_read_b128 v[32:35], v168 offset:14336\n\tv_mfma_f32_16x16x32_bf16 v[56:59], a[28:31], v[44:47], v[56:59]\n\t" \
    "s_waitcnt lgkmcnt(5)\n\tds_read_b128 v[76:79], %[vi] offset:2048\n\tv_mfma_f32_16x16x32_bf16 v[56:59], a[32:35], v[16:19], v[56:59]\n\tds_read_b128 v[36:39], v169 offset:14336\n\t" \
    "v_mfma_f32_16x16x32_bf16 v[60:63], a[96:99], v[16:19], v[60:63]\n\ts_waitcnt lgkmcnt(6)\n\tds_read_b128 v[80:83], %[vi] offset:4096\n\t" \
    "v_mfma_f32_16x16x32_bf16 v[60:63], a[100:103], v[20:23], v[60:63]\n\tds_read_b128 v[40:43], v168 offset:15360\n\tv_mfma_f32_16x16x32_bf16 v[56:59], a[36:39], v[20:23], v[56:59]\n\t" \
    "s_waitcnt lgkmcnt(7)\n\tds_read_b128 v[84:87], %[vi] offset:6144\n\tv_mfma_f32_16x16x32_bf16 v[56:59], a[40:43], v[24:27], v[56:59]\n\tds_read_b128 v[44:47], v169 offset:15360\n\t" \
    "v_mfma_f32_16x16x32_bf16 v[60:63], a[104:107], v[24:27], v[60:63]\n\ts_waitcnt lgkmcnt(8)\n\tv_mfma_f32_16x16x32_bf16 v[60:63], a[108:111], v[28:31], v[60:63]\n\t" \
    "v_mfma_f32_16x16x32_bf16 v[56:59], a[44:47], v[28:31], v[56:59]\n\ts_waitcnt lgkmcnt(6)\n\tv_mfma_f32_16x16x32_bf16 v[56:59], a[48:51], v[32:35], v[56:59]\n\t" \
    "v_mfma_f32_16x16x32_bf16 v[60:63], a[112:115], v[32:35], v[60:63]\n\ts_waitcnt lgkmcnt(4)\n\tv_mfma_f32_16x16x32_bf16 v[60:63], a[116:119], v[36:39], v[60:63]\n\t" \
    "v_mfma_f32_16x16x32_bf16 v[56:59], a[52:55], v[36:39], v[56:59]\n\ts_waitcnt lgkmcnt(2)\n\tv_mfma_f32_16x16x32_bf16 v[56:59], a[56:59], v[40:43], v[56:59]\n\t" \
    "v_mfma_f32_16x16x32_bf16 v[60:63], a[120:123], v[40:43], v[60:63]\n\ts_waitcnt lgkmcnt(0)\n\tv_mfma_f32_16x16x32_bf16 v[60:63], a[124:127], v[44:47], v[60:63]\n\t" \
    "v_mfma_f32_16x16x32_bf16 v[56:59], a[60:63], v[44:47], v[56:59]\n\tv_mfma_f32_16x16x32_bf16 v[104:107], v[72:75], a[128:131], 0\n\tv_mfma_f32_16x16x32_bf16 v[108:111], v[72:75], a[144:147], 0\n\t" \
    "v_mfma_f32_16x16x32_bf16 v[112:115], v[72:75], a[160:163], 0\n\tv_cvt_pk_bf16_f32 v68, v56, v57\n\tv_cvt_pk_bf16_f32 v69, v58, v59\n\tv_mfma_f32_16x16x32_bf16 v[116:119], v[72:75], a[176:179], 0\n\t" \
    "v_cvt_pk_bf16_f32 v70, v60, v61\n\tv_cvt_pk_bf16_f32 v71, v62, v63\n\tv_mfma_f32_16x16x32_bf16 v[116:119], v[76:79], a[180:183], v[116:119]\n\t" \
    "v_mfma_f32_16x16x32_bf16 v[112:115], v[76:79], a[164:167], v[112:115]\n\tds_write_b128 %[vw], v[68:71] offset:1024\n\tv_mfma_f32_16x16x32_bf16 v[108:111], v[76:79], a[148:151], v[108:111]\n\t" \
    "v_add_u32 v168, s62, %[xb]\n\tv_xor_b32 v169, 64, v168\n\tv_mfma_f32_16x16x32_bf16 v[104:107], v[76:79], a[132:135], v[104:107]\n\t" \
    "v_mfma_f32_16x16x32_bf16 v[104:107], v[80:83], a[136:139], v[104:107]\n\tv_mfma_f32_16x16x32_bf16 v[108:111], v[80:83], a[152:155], v[108:111]\n\t" \
    "v_mfma_f32_16x16x32_bf16 v[112:115], v[80:83], a[168:171], v[112:115]\n\tv_mfma_f32_16x16x32_bf16 v[116:119], v[80:83], a[184:187], v[116:119]\n\t" \
    "v_mfma_f32_16x16x32_bf16 v[116:119], v[84:87], a[188:191], v[116:119]\n\ts_waitcnt lgkmcnt(0)\n\ts_waitcnt vmcnt(0)\n\ts_barrier\n\t" \
    "v_mfma_f32_16x16x32_bf16 v[112:115], v[84:87], a[172:175], v[112:115]\n\tds_read_b128 v[88:91], %[vi] offset:1024\n\tv_mfma_f32_16x16x32_bf16 v[108:111], v[84:87], a[156:159], v[108:111]\n\t" \
    "s_mov_b64 exec, %[em]\n\tglobal_store_dword v171, v172, %[dn] sc1\n\ts_mov_b64 exec, -1\n\tv_mfma_f32_16x16x32_bf16 v[104:107], v[84:87], a[140:143], v[104:107]\n\t" \
    "ds_read_b128 v[92:95], %[vi] offset:3072\n\tv_mfma_f32_16x16x32_bf16 v[120:123], v[72:75], a[192:195], 0\n\tv_mfma_f32_16x16x32_bf16 v[124:127], v[72:75], a[208:211], 0\n\t" \
    "ds_read_b128 v[96:99], %[vi] offset:5120\n\tv_mfma_f32_16x16x32_bf16 v[128:131], v[72:75], a[224:227], 0\n\tv_cvt_pk_bf16_f32 v136, v104, v105\n\tv_cvt_pk_bf16_f32 v137, v106, v107\n\t" \
    "v_mfma_f32_16x16x32_bf16 v[132:135], v[72:75], a[240:243], 0\n\tds_read_b128 v[100:103], %[vi] offset:7168\n\tv_cvt_pk_bf16_f32 v140, v108, v109\n\t" \
    "v_mfma_f32_16x16x32_bf16 v[132:135], v[76:79], a[244:247], v[132:135]\n\tv_cvt_pk_bf16_f32 v141, v110, v111\n\tv_cvt_pk_bf16_f32 v144, v112, v113\n\t" \
    "v_mfma_f32_16x16x32_bf16 v[128:131], v[76:79], a[228:231], v[128:131]\n\tv_cvt_pk_bf16_f32 v145, v114, v115\n\tv_cvt_pk_bf16_f32 v148, v116, v117\n\t" \
    "v_mfma_f32_16x16x32_bf16 v[124:127], v[76:79], a[212:215], v[124:127]\n\tbuffer_load_dwordx4 %[dv], %[ra], %[so] offen lds\n\tv_cvt_pk_bf16_f32 v149, v118, v119\n\t" \
    "v_mfma_f32_16x16x32_bf16 v[120:123], v[76:79], a[196:199], v[120:123]\n\ts_add_u32 m0, m0, 896\n\tv_mfma_f32_16x16x32_bf16 v[120:123], v[80:83], a[200:203], v[120:123]\n\t" \
    "v_mfma_f32_16x16x32_bf16 v[124:127], v[80:83], a[216:219], v[124:127]\n\tbuffer_load_dwordx4 %[dv], %[ra], %[so] offen offset:128 lds\n\t" \
    "v_mfma_f32_16x16x32_bf16 v[128:131], v[80:83], a[232:235], v[128:131]\n\ts_add_u32 m0, m0, 896\n\tv_mfma_f32_16x16x32_bf16 v[132:135], v[80:83], a[248:251], v[132:135]\n\t" \
    "v_mfma_f32_16x16x32_bf16 v[132:135], v[84:87], a[252:255], v[132:135]\n\tbuffer_load_dwordx4 %[dv], %[ra], %[so] offen offset:256 lds\n\t" \
    "v_mfma_f32_16x16x32_bf16 v[128:131], v[84:87], a[236:239], v[128:131]\n\ts_add_u32 m0, m0, 896\n\tv_mfma_f32_16x16x32_bf16 v[124:127], v[84:87], a[220:223], v[124:127]\n\t" \
    "v_mfma_f32_16x16x32_bf16 v[120:123], v[84:87], a[204:207], v[120:123]\n\ts_waitcnt lgkmcnt(3)\n\tbuffer_load_dwordx4 %[dv], %[ra], %[so] offen offset:384 lds\n\t" \
    "v_mfma_f32_16x16x32_bf16 v[104:107], v[88:91], a[128:131], 0\n\ts_add_u32 m0, m0, 896\n\tv_mfma_f32_16x16x32_bf16 v[108:111], v[88:91], a[144:147], 0\n\t" \
    "v_mfma_f32_16x16x32_bf16 v[112:115], v[88:91], a[160:163], 0\n\tbuffer_load_dwordx4 %[dv], %[ra], %[so] offen offset:512 lds\n\tv_cvt_pk_bf16_f32 v152, v120, v121\n\t" \
    "v_cvt_pk_bf16_f32 v153, v122, v123\n\tv_mfma_f32_16x16x32_bf16 v[116:119], v[88:91], a[176:179], 0\n\ts_waitcnt lgkmcnt(2)\n\ts_add_u32 m0, m0, 896\n\tv_cvt_pk_bf16_f32 v156, v124, v125\n\t" \
    "v_cvt_pk_bf16_f32 v157, v126, v127\n\tv_mfma_f32_16x16x32_bf16 v[116:119], v[92:95], a[180:183], v[116:119]\n\tv_cvt_pk_bf16_f32 v160, v128, v129\n\tv_cvt_pk_bf16_f32 v161, v130, v131\n\t" \
    "v_mfma_f32_16x16x32_bf16 v[112:115], v[92:95], a[164:167], v[112:115]\n\tbuffer_load_dwordx4 %[dv], %[ra], %[so] offen offset:640 lds\n\tv_cvt_pk_bf16_f32 v164, v132, v133\n\t" \
    "v_cvt_pk_bf16_f32 v165, v134, v135\n\tv_mfma_f32_16x16x32_bf16 v[108:111], v[92:95], a[148:151], v[108:111]\n\ts_add_u32 m0, m0, 896\n\t" \
    "v_mfma_f32_16x16x32_bf16 v[104:107], v[92:95], a[132:135], v[104:107]\n\ts_waitcnt lgkmcnt(1)\n\tv_mfma_f32_16x16x32_bf16 v[104:107], v[96:99], a[136:139], v[104:107]\n\t" \
    "buffer_load_dwordx4 %[dv], %[ra], %[so] offen offset:768 lds\n\tv_mfma_f32_16x16x32_bf16 v[108:111], v[96:99], a[152:155], v[108:111]\n\ts_add_u32 m0, m0, 896\n\t" \
    "v_mfma_f32_16x16x32_bf16 v[112:115], v[96:99], a[168:171], v[112:115]\n\tv_mfma_f32_16x16x32_bf16 v[116:119], v[96:99], a[184:187], v[116:119]\n\ts_waitcnt lgkmcnt(0)\n\t" \
    "buffer_load_dwordx4 %[dv], %[ra], %[so] offen offset:896 lds\n\tv_mfma_f32_16x16x32_bf16 v[116:119], v[100:103], a[188:191], v[116:119]\n\ts_add_u32 %[so], %[so], 0x8000\n\t" \
    "v_mfma_f32_16x16x32_bf16 v[112:115], v[100:103], a[172:175], v[112:115]\n\tv_mfma_f32_16x16x32_bf16 v[108:111], v[100:103], a[156:159], v[108:111]\n\ts_min_u32 %[so], %[so], 0x1f8000\n\t" \
    "v_mfma_f32_16x16x32_bf16 v[104:107], v[100:103], a[140:143], v[104:107]\n\tds_read_b128 v[16:19], v168\n\tv_mfma_f32_16x16x32_bf16 v[120:123], v[88:91], a[192:195], 0\n\t" \
    "v_mfma_f32_16x16x32_bf16 v[124:127], v[88:91], a[208:211], 0\n\tv_mfma_f32_16x16x32_bf16 v[128:131], v[88:91], a[224:227], 0\n\tds_read_b128 v[20:23], v169\n\tv_cvt_pk_bf16_f32 v138, v104, v105\n\t" \
    "v_mfma_f32_16x16x32_bf16 v[132:135], v[88:91], a[240:243], 0\n\tv_cvt_pk_bf16_f32 v139, v106, v107\n\tv_mfma_f32_16x16x32_bf16 v[132:135], v[92:95], a[244:247], v[132:135]\n\t" \
    "buffer_store_dwordx4 v[136:139], v170, %[rw], %[sp] offen sc1\n\ts_add_u32 %[sp], %[sp], 32768\n\tv_mfma_f32_16x16x32_bf16 v[128:131], v[92:95], a[228:231], v[128:131]\n\t" \
    "ds_read_b128 v[24:27], v168 offset:1024\n\tv_cvt_pk_bf16_f32 v142, v108, v109\n\tv_mfma_f32_16x16x32_bf16 v[124:127], v[92:95], a[212:215], v[124:127]\n\tv_cvt_pk_bf16_f32 v143, v110, v111\n\t" \
    "v_mfma_f32_16x16x32_bf16 v[120:123], v[92:95], a[196:199], v[120:123]\n\tbuffer_store_dwordx4 v[140:143], v170, %[rw], %[sp] offen sc1\n\ts_add_u32 %[sp], %[sp], 32768\n\t" \
    "v_mfma_f32_16x16x32_bf16 v[120:123], v[96:99], a[200:203], v[120:123]\n\tds_read_b128 v[28:31], v169 offset:1024\n\tv_cvt_pk_bf16_f32 v146, v112, v113\n\t" \
    "v_mfma_f32_16x16x32_bf16 v[124:127], v[96:99], a[216:219], v[124:127]\n\tv_cvt_pk_bf16_f32 v147, v114, v115\n\tv_mfma_f32_16x16x32_bf16 v[128:131], v[96:99], a[232:235], v[128:131]\n\t" \
    "buffer_store_dwordx4 v[144:147], v170, %[rw], %[sp] offen sc1\n\ts_add_u32 %[sp], %[sp], 32768\n\tv_mfma_f32_16x16x32_bf16 v[132:135], v[96:99], a[248:251], v[132:135]\n\t" \
    "ds_read_b128 v[32:35], v168 offset:2048\n\tv_cvt_pk_bf16_f32 v150, v116, v117\n\tv_mfma_f32_16x16x32_bf16 v[132:135], v[100:103], a[252:255], v[132:135]\n\tv_cvt_pk_bf16_f32 v151, v118, v119\n\t" \
    "v_mfma_f32_16x16x32_bf16 v[128:131], v[100:103], a[236:239], v[128:131]\n\tbuffer_store_dwordx4 v[148:151], v170, %[rw], %[sp] offen sc1\n\ts_add_u32 %[sp], %[sp], 32768\n\t" \
    "v_mfma_f32_16x16x32_bf16 v[124:127], v[100:103], a[220:223], v[124:127]\n\tv_mfma_f32_16x16x32_bf16 v[120:123], v[100:103], a[204:207], v[120:123]\n\ts_waitcnt lgkmcnt(4)\n\ts_add_u32 s61, s61, 1\n\t" \
    "s_cmp_lt_u32 s61, 64\n\ts_cbranch_scc1 1b\n\ts_mov_b32 m0, s60\n\ts_waitcnt lgkmcnt(0)\n\ts_nop 7\n\tv_cvt_pk_bf16_f32 v154, v120, v121\n\tv_cvt_pk_bf16_f32 v155, v122, v123\n\t" \
    "buffer_store_dwordx4 v[152:155], v170, %[rw], %[sp] offen sc1\n\ts_add_u32 %[sp], %[sp], 32768\n\tv_cvt_pk_bf16_f32 v158, v124, v125\n\tv_cvt_pk_bf16_f32 v159, v126, v127\n\t" \
    "buffer_store_dwordx4 v[156:159], v170, %[rw], %[sp] offen sc1\n\ts_add_u32 %[sp], %[sp], 32768\n\tv_cvt_pk_bf16_f32 v162, v128, v129\n\tv_cvt_pk_bf16_f32 v163, v130, v131\n\t" \
    "buffer_store_dwordx4 v[160:163], v170, %[rw], %[sp] offen sc1\n\ts_add_u32 %[sp], %[sp], 32768\n\tv_cvt_pk_bf16_f32 v166, v132, v133\n\tv_cvt_pk_bf16_f32 v167, v134, v135\n\t" \
    "buffer_store_dwordx4 v[164:167], v170, %[rw], %[sp] offen sc1\n\ts_add_u32 %[sp], %[sp], 819200\n\ts_nop 1\n\t"
#define ASM_OPROJ \
    "ds_read_b128 v[16:19], %[xa]\n\tds_read_b64 v[64:65], %[fr]\n\tds_read_b128 v[20:23], %[xa] offset:1024\n\tds_read_b128 v[24:27], %[xa] offset:2048\n\tds_read_b128 v[28:31], %[xa] offset:3072\n\t" \
    "ds_read_b128 v[32:35], %[xa] offset:4096\n\ts_mov_b32 s60, m0\n\ts_mov_b64 vcc, %[em]\n\ts_cbranch_vccz 1f\n\tv_mov_b32 v68, 1\n\tv_mov_b32 v69, 0\n\ts_max_i32 s61, %[dr], 0\n\tv_mov_b32 v70, s61\n\t" \
    "s_cmp_lt_i32 %[dr], 0\n\ts_cselect_b64 exec, 0, 1\n\tglobal_atomic_add v70, v68, %[fin]\n\ts_mov_b64 exec, 1\n\tglobal_atomic_add v66, v69, v68, %[qu] sc0\n\tglobal_load_dword v67, v69, %[li] sc1\n\t" \
    "s_mov_b64 exec, -1\n\t1:\ns_waitcnt lgkmcnt(5)\n\tv_mfma_f32_16x16x32_bf16 v[48:51], a[0:3], v[16:19], 0\n\tds_read_b128 v[36:39], %[xa] offset:5120\n\t" \
    "v_mfma_f32_16x16x32_bf16 v[56:59], a[112:115], v[16:19], 0\n\ts_waitcnt lgkmcnt(4)\n\tv_mfma_f32_16x16x32_bf16 v[60:63], a[112:115], v[20:23], 0\n\tds_read_b128 v[40:43], %[xa] offset:6144\n\t" \
    "v_mfma_f32_16x16x32_bf16 v[52:55], a[0:3], v[20:23], 0\n\ts_waitcnt lgkmcnt(4)\n\tv_readfirstlane_b32 %[pos], v64\n\tv_readfirstlane_b32 %[e], v65\n\t" \
    "v_mfma_f32_16x16x32_bf16 v[48:51], a[4:7], v[24:27], v[48:51]\n\tds_read_b128 v[44:47], %[xa] offset:7168\n\tv_mfma_f32_16x16x32_bf16 v[56:59], a[116:119], v[24:27], v[56:59]\n\t" \
    "s_waitcnt lgkmcnt(4)\n\ts_mov_b32 m0, %[lb]\n\tv_mfma_f32_16x16x32_bf16 v[60:63], a[116:119], v[28:31], v[60:63]\n\ts_lshr_b32 %[nx], %[pos], 3\n\tds_read_b128 v[16:19], %[xa] offset:8192\n\t" \
    "v_mfma_f32_16x16x32_bf16 v[52:55], a[4:7], v[28:31], v[52:55]\n\ts_waitcnt lgkmcnt(4)\n\ts_lshl_b32 %[nx], %[nx], 2\n\tv_mfma_f32_16x16x32_bf16 v[48:51], a[8:11], v[32:35], v[48:51]\n\t" \
    "s_lshr_b32 %[e], %[e], %[nx]\n\tds_read_b128 v[20:23], %[xa] offset:9216\n\tv_mfma_f32_16x16x32_bf16 v[56:59], a[120:123], v[32:35], v[56:59]\n\ts_waitcnt lgkmcnt(4)\n\ts_and_b32 %[e], %[e], 15\n\t" \
    "v_mfma_f32_16x16x32_bf16 v[60:63], a[120:123], v[36:39], v[60:63]\n\ts_cmp_lt_u32 %[pos], 64\n\tds_read_b128 v[24:27], %[xa] offset:10240\n\t" \
    "v_mfma_f32_16x16x32_bf16 v[52:55], a[8:11], v[36:39], v[52:55]\n\ts_waitcnt lgkmcnt(4)\n\ts_cselect_b32 %[e], %[e], 0\n\tv_mfma_f32_16x16x32_bf16 v[48:51], a[12:15], v[40:43], v[48:51]\n\t" \
    "s_lshl_b32 %[nx], %[e], 3\n\tds_read_b128 v[28:31], %[xa] offset:11264\n\tv_mfma_f32_16x16x32_bf16 v[56:59], a[124:127], v[40:43], v[56:59]\n\ts_waitcnt lgkmcnt(4)\n\ts_and_b32 s61, %[pos], 7\n\t" \
    "v_mfma_f32_16x16x32_bf16 v[60:63], a[124:127], v[44:47], v[60:63]\n\ts_add_u32 %[nx], %[nx], s61\n\tds_read_b128 v[32:35], %[xa] offset:12288\n\t" \
    "v_mfma_f32_16x16x32_bf16 v[52:55], a[12:15], v[44:47], v[52:55]\n\ts_waitcnt lgkmcnt(4)\n\ts_sub_u32 %[nx], %[nx], 8\n\tv_mfma_f32_16x16x32_bf16 v[48:51], a[16:19], v[16:19], v[48:51]\n\t" \
    "s_cselect_b32 %[nx], %[ch], %[nx]\n\tds_read_b128 v[36:39], %[xa] offset:13312\n\tv_mfma_f32_16x16x32_bf16 v[56:59], a[128:131], v[16:19], v[56:59]\n\ts_waitcnt lgkmcnt(4)\n\t" \
    "s_lshl_b32 s61, %[nx], 18\n\tv_mfma_f32_16x16x32_bf16 v[60:63], a[128:131], v[20:23], v[60:63]\n\ts_add_u32 s61, s61, %[sb]\n\tds_read_b128 v[40:43], %[xa] offset:14336\n\t" \
    "v_mfma_f32_16x16x32_bf16 v[52:55], a[16:19], v[20:23], v[52:55]\n\ts_waitcnt lgkmcnt(4)\n\tv_mfma_f32_16x16x32_bf16 v[48:51], a[20:23], v[24:27], v[48:51]\n\t" \
    "ds_read_b128 v[44:47], %[xa] offset:15360\n\tbuffer_load_dwordx4 %[dv], %[ra], s61 offen sc1 lds\n\tv_mfma_f32_16x16x32_bf16 v[56:59], a[132:135], v[24:27], v[56:59]\n\ts_waitcnt lgkmcnt(4)\n\t" \
    "v_mfma_f32_16x16x32_bf16 v[60:63], a[132:135], v[28:31], v[60:63]\n\tds_read_b128 v[16:19], %[xa] offset:16384\n\tv_mfma_f32_16x16x32_bf16 v[52:55], a[20:23], v[28:31], v[52:55]\n\t" \
    "s_waitcnt lgkmcnt(4)\n\tv_mfma_f32_16x16x32_bf16 v[48:51], a[24:27], v[32:35], v[48:51]\n\tds_read_b128 v[20:23], %[xa] offset:17408\n\t" \
    "buffer_load_dwordx4 %[dv], %[ra], s61 offen offset:1024 sc1 lds\n\tv_mfma_f32_16x16x32_bf16 v[56:59], a[136:139], v[32:35], v[56:59]\n\ts_waitcnt lgkmcnt(4)\n\ts_add_u32 m0, m0, 0x2000\n\t" \
    "v_mfma_f32_16x16x32_bf16 v[60:63], a[136:139], v[36:39], v[60:63]\n\ts_add_u32 s61, s61, 0x2000\n\tds_read_b128 v[24:27], %[xa] offset:18432\n\t" \
    "v_mfma_f32_16x16x32_bf16 v[52:55], a[24:27], v[36:39], v[52:55]\n\ts_waitcnt lgkmcnt(4)\n\tv_mfma_f32_16x16x32_bf16 v[48:51], a[28:31], v[40:43], v[48:51]\n\t" \
    "ds_read_b128 v[28:31], %[xa] offset:19456\n\tbuffer_load_dwordx4 %[dv], %[ra], s61 offen sc1 lds\n\tv_mfma_f32_16x16x32_bf16 v[56:59], a[140:143], v[40:43], v[56:59]\n\ts_waitcnt lgkmcnt(4)\n\t" \
    "v_mfma_f32_16x16x32_bf16 v[60:63], a[140:143], v[44:47], v[60:63]\n\tds_read_b128 v[32:35], %[xa] offset:20480\n\tv_mfma_f32_16x16x32_bf16 v[52:55], a[28:31], v[44:47], v[52:55]\n\t" \
    "s_waitcnt lgkmcnt(4)\n\tv_mfma_f32_16x16x32_bf16 v[48:51], a[32:35], v[16:19], v[48:51]\n\tds_read_b128 v[36:39], %[xa] offset:21504\n\t" \
    "buffer_load_dwordx4 %[dv], %[ra], s61 offen offset:1024 sc1 lds\n\tv_mfma_f32_16x16x32_bf16 v[56:59], a[144:147], v[16:19], v[56:59]\n\ts_waitcnt lgkmcnt(4)\n\ts_add_u32 m0, m0, 0x2000\n\t" \
    "v_mfma_f32_16x16x32_bf16 v[60:63], a[144:147], v[20:23], v[60:63]\n\ts_add_u32 s61, s61, 0x2000\n\tds_read_b128 v[40:43], %[xa] offset:22528\n\t" \
    "v_mfma_f32_16x16x32_bf16 v[52:55], a[32:35], v[20:23], v[52:55]\n\ts_waitcnt lgkmcnt(4)\n\tv_mfma_f32_16x16x32_bf16 v[48:51], a[36:39], v[24:27], v[48:51]\n\t" \
    "ds_read_b128 v[44:47], %[xa] offset:23552\n\tbuffer_load_dwordx4 %[dv], %[ra], s61 offen sc1 lds\n\tv_mfma_f32_16x16x32_bf16 v[56:59], a[148:151], v[24:27], v[56:59]\n\ts_waitcnt lgkmcnt(4)\n\t" \
    "v_mfma_f32_16x16x32_bf16 v[60:63], a[148:151], v[28:31], v[60:63]\n\tds_read_b128 v[16:19], %[xa] offset:24576\n\tv_mfma_f32_16x16x32_bf16 v[52:55], a[36:39], v[28:31], v[52:55]\n\t" \
    "s_waitcnt lgkmcnt(4)\n\tv_mfma_f32_16x16x32_bf16 v[48:51], a[40:43], v[32:35], v[48:51]\n\tds_read_b128 v[20:23], %[xa] offset:25600\n\t" \
    "buffer_load_dwordx4 %[dv], %[ra], s61 offen offset:1024 sc1 lds\n\tv_mfma_f32_16x16x32_bf16 v[56:59], a[152:155], v[32:35], v[56:59]\n\ts_waitcnt lgkmcnt(4)\n\ts_add_u32 m0, m0, 0x2000\n\t" \
    "v_mfma_f32_16x16x32_bf16 v[60:63], a[152:155], v[36:39], v[60:63]\n\ts_add_u32 s61, s61, 0x2000\n\tds_read_b128 v[24:27], %[xa] offset:26624\n\t" \
    "v_mfma_f32_16x16x32_bf16 v[52:55], a[40:43], v[36:39], v[52:55]\n\ts_waitcnt lgkmcnt(4)\n\tv_mfma_f32_16x16x32_bf16 v[48:51], a[44:47], v[40:43], v[48:51]\n\t" \
    "ds_read_b128 v[28:31], %[xa] offset:27648\n\tbuffer_load_dwordx4 %[dv], %[ra], s61 offen sc1 lds\n\tv_mfma_f32_16x16x32_bf16 v[56:59], a[156:159], v[40:43], v[56:59]\n\ts_waitcnt lgkmcnt(4)\n\t" \
    "v_mfma_f32_16x16x32_bf16 v[60:63], a[156:159], v[44:47], v[60:63]\n\tds_read_b128 v[32:35], %[xa] offset:28672\n\tv_mfma_f32_16x16x32_bf16 v[52:55], a[44:47], v[44:47], v[52:55]\n\t" \
    "s_waitcnt lgkmcnt(4)\n\tv_mfma_f32_16x16x32_bf16 v[48:51], a[48:51], v[16:19], v[48:51]\n\tds_read_b128 v[36:39], %[xa] offset:29696\n\t" \
    "buffer_load_dwordx4 %[dv], %[ra], s61 offen offset:1024 sc1 lds\n\tv_mfma_f32_16x16x32_bf16 v[56:59], a[160:163], v[16:19], v[56:59]\n\ts_waitcnt lgkmcnt(4)\n\ts_add_u32 m0, m0, 0x2000\n\t" \
    "v_mfma_f32_16x16x32_bf16 v[60:63], a[160:163], v[20:23], v[60:63]\n\ts_add_u32 s61, s61, 0x2000\n\tds_read_b128 v[40:43], %[xa] offset:30720\n\t" \
    "v_mfma_f32_16x16x32_bf16 v[52:55], a[48:51], v[20:23], v[52:55]\n\ts_waitcnt lgkmcnt(4)\n\tv_mfma_f32_16x16x32_bf16 v[48:51], a[52:55], v[24:27], v[48:51]\n\t" \
    "ds_read_b128 v[44:47], %[xa] offset:31744\n\tbuffer_load_dwordx4 %[dv], %[ra], s61 offen sc1 lds\n\tv_mfma_f32_16x16x32_bf16 v[56:59], a[164:167], v[24:27], v[56:59]\n\ts_waitcnt lgkmcnt(4)\n\t" \
    "v_mfma_f32_16x16x32_bf16 v[60:63], a[164:167], v[28:31], v[60:63]\n\tds_read_b128 v[16:19], %[xa] offset:32768\n\tv_mfma_f32_16x16x32_bf16 v[52:55], a[52:55], v[28:31], v[52:55]\n\t" \
    "s_waitcnt lgkmcnt(4)\n\tv_mfma_f32_16x16x32_bf16 v[48:51], a[56:59], v[32:35], v[48:51]\n\tds_read_b128 v[20:23], %[xa] offset:33792\n\t" \
    "buffer_load_dwordx4 %[dv], %[ra], s61 offen offset:1024 sc1 lds\n\tv_mfma_f32_16x16x32_bf16 v[56:59], a[168:171], v[32:35], v[56:59]\n\ts_waitcnt lgkmcnt(4)\n\ts_add_u32 m0, m0, 0x2000\n\t" \
    "v_mfma_f32_16x16x32_bf16 v[60:63], a[168:171], v[36:39], v[60:63]\n\ts_add_u32 s61, s61, 0x2000\n\tds_read_b128 v[24:27], %[xa] offset:34816\n\t" \
    "v_mfma_f32_16x16x32_bf16 v[52:55], a[56:59], v[36:39], v[52:55]\n\ts_waitcnt lgkmcnt(4)\n\tv_mfma_f32_16x16x32_bf16 v[48:51], a[60:63], v[40:43], v[48:51]\n\t" \
    "ds_read_b128 v[28:31], %[xa] offset:35840\n\tbuffer_load_dwordx4 %[dv], %[ra], s61 offen sc1 lds\n\tv_mfma_f32_16x16x32_bf16 v[56:59], a[172:175], v[40:43], v[56:59]\n\ts_waitcnt lgkmcnt(4)\n\t" \
    "v_mfma_f32_16x16x32_bf16 v[60:63], a[172:175], v[44:47], v[60:63]\n\tds_read_b128 v[32:35], %[xa] offset:36864\n\tv_mfma_f32_16x16x32_bf16 v[52:55], a[60:63], v[44:47], v[52:55]\n\t" \
    "s_waitcnt lgkmcnt(4)\n\tv_mfma_f32_16x16x32_bf16 v[48:51], a[64:67], v[16:19], v[48:51]\n\tds_read_b128 v[36:39], %[xa] offset:37888\n\t" \
    "buffer_load_dwordx4 %[dv], %[ra], s61 offen offset:1024 sc1 lds\n\tv_mfma_f32_16x16x32_bf16 v[56:59], a[176:179], v[16:19], v[56:59]\n\ts_waitcnt lgkmcnt(4)\n\ts_add_u32 m0, m0, 0x2000\n\t" \
    "v_mfma_f32_16x16x32_bf16 v[60:63], a[176:179], v[20:23], v[60:63]\n\ts_add_u32 s61, s61, 0x2000\n\tds_read_b128 v[40:43], %[xa] offset:38912\n\t" \
    "v_mfma_f32_16x16x32_bf16 v[52:55], a[64:67], v[20:23], v[52:55]\n\ts_waitcnt lgkmcnt(4)\n\ts_cmp_lt_u32 %[kq], 3\n\tv_mfma_f32_16x16x32_bf16 v[48:51], a[68:71], v[24:27], v[48:51]\n\t" \
    "s_cbranch_scc1 2f\n\tbuffer_load_dwordx4 %[dv], %[ra], s61 offen sc1 lds\n\tbuffer_load_dwordx4 %[dv], %[ra], s61 offen offset:1024 sc1 lds\n\t2:\nds_read_b128 v[44:47], %[xa] offset:39936\n\t" \
    "v_mfma_f32_16x16x32_bf16 v[56:59], a[180:183], v[24:27], v[56:59]\n\ts_waitcnt lgkmcnt(4)\n\tv_mfma_f32_16x16x32_bf16 v[60:63], a[180:183], v[28:31], v[60:63]\n\t" \
    "ds_read_b128 v[16:19], %[xa] offset:40960\n\tv_mfma_f32_16x16x32_bf16 v[52:55], a[68:71], v[28:31], v[52:55]\n\ts_waitcnt lgkmcnt(4)\n\t" \
    "v_mfma_f32_16x16x32_bf16 v[48:51], a[72:75], v[32:35], v[48:51]\n\tds_read_b128 v[20:23], %[xa] offset:41984\n\tv_mfma_f32_16x16x32_bf16 v[56:59], a[184:187], v[32:35], v[56:59]\n\t" \
    "s_waitcnt lgkmcnt(4)\n\tv_mfma_f32_16x16x32_bf16 v[60:63], a[184:187], v[36:39], v[60:63]\n\tds_read_b128 v[24:27], %[xa] offset:43008\n\t" \
    "v_mfma_f32_16x16x32_bf16 v[52:55], a[72:75], v[36:39], v[52:55]\n\ts_waitcnt lgkmcnt(4)\n\tv_mfma_f32_16x16x32_bf16 v[48:51], a[76:79], v[40:43], v[48:51]\n\t" \
    "ds_read_b128 v[28:31], %[xa] offset:44032\n\tv_mfma_f32_16x16x32_bf16 v[56:59], a[188:191], v[40:43], v[56:59]\n\ts_waitcnt lgkmcnt(4)\n\t" \
    "v_mfma_f32_16x16x32_bf16 v[60:63], a[188:191], v[44:47], v[60:63]\n\tds_read_b128 v[32:35], %[xa] offset:45056\n\tv_mfma_f32_16x16x32_bf16 v[52:55], a[76:79], v[44:47], v[52:55]\n\t" \
    "s_waitcnt lgkmcnt(4)\n\tv_mfma_f32_16x16x32_bf16 v[48:51], a[80:83], v[16:19], v[48:51]\n\tds_read_b128 v[36:39], %[xa] offset:46080\n\t" \
    "v_mfma_f32_16x16x32_bf16 v[56:59], a[192:195], v[16:19], v[56:59]\n\ts_waitcnt lgkmcnt(4)\n\tv_mfma_f32_16x16x32_bf16 v[60:63], a[192:195], v[20:23], v[60:63]\n\t" \
    "ds_read_b128 v[40:43], %[xa] offset:47104\n\tv_mfma_f32_16x16x32_bf16 v[52:55], a[80:83], v[20:23], v[52:55]\n\ts_waitcnt lgkmcnt(4)\n\t" \
    "v_mfma_f32_16x16x32_bf16 v[48:51], a[84:87], v[24:27], v[48:51]\n\tds_read_b128 v[44:47], %[xa] offset:48128\n\tv_mfma_f32_16x16x32_bf16 v[56:59], a[196:199], v[24:27], v[56:59]\n\t" \
    "s_waitcnt lgkmcnt(4)\n\tv_mfma_f32_16x16x32_bf16 v[60:63], a[196:199], v[28:31], v[60:63]\n\tds_read_b128 v[16:19], %[xa] offset:49152\n\t" \
    "v_mfma_f32_16x16x32_bf16 v[52:55], a[84:87], v[28:31], v[52:55]\n\ts_waitcnt lgkmcnt(4)\n\tv_mfma_f32_16x16x32_bf16 v[48:51], a[88:91], v[32:35], v[48:51]\n\t" \
    "ds_read_b128 v[20:23], %[xa] offset:50176\n\tv_mfma_f32_16x16x32_bf16 v[56:59], a[200:203], v[32:35], v[56:59]\n\ts_waitcnt lgkmcnt(4)\n\t" \
    "v_mfma_f32_16x16x32_bf16 v[60:63], a[200:203], v[36:39], v[60:63]\n\tds_read_b128 v[24:27], %[xa] offset:51200\n\tv_mfma_f32_16x16x32_bf16 v[52:55], a[88:91], v[36:39], v[52:55]\n\t" \
    "s_waitcnt lgkmcnt(4)\n\tv_mfma_f32_16x16x32_bf16 v[48:51], a[92:95], v[40:43], v[48:51]\n\ts_cbranch_vccz 4f\n\ts_waitcnt vmcnt(12)\n\ts_mov_b64 exec, 1\n\tds_write_b64 %[fw], v[66:67]\n\t" \
    "s_mov_b64 exec, -1\n\ts_waitcnt lgkmcnt(0)\n\t4:\nds_read_b128 v[28:31], %[xa] offset:52224\n\tv_mfma_f32_16x16x32_bf16 v[56:59], a[204:207], v[40:43], v[56:59]\n\ts_waitcnt lgkmcnt(4)\n\t" \
    "v_mfma_f32_16x16x32_bf16 v[60:63], a[204:207], v[44:47], v[60:63]\n\ts_lshl_b32 s61, %[ch], 16\n\ts_cmp_lt_u32 %[kq], 3\n\tds_read_b128 v[32:35], %[xa] offset:53248\n\t" \
    "v_mfma_f32_16x16x32_bf16 v[52:55], a[92:95], v[44:47], v[52:55]\n\ts_waitcnt lgkmcnt(4)\n\ts_cbranch_scc1 3f\n\tv_mfma_f32_16x16x32_bf16 v[48:51], a[96:99], v[16:19], v[48:51]\n\t" \
    "ds_read_b128 v[36:39], %[xa] offset:54272\n\tv_mfma_f32_16x16x32_bf16 v[56:59], a[208:211], v[16:19], v[56:59]\n\ts_waitcnt lgkmcnt(4)\n\t" \
    "v_mfma_f32_16x16x32_bf16 v[60:63], a[208:211], v[20:23], v[60:63]\n\tds_read_b128 v[40:43], %[xa] offset:55296\n\tv_mfma_f32_16x16x32_bf16 v[52:55], a[96:99], v[20:23], v[52:55]\n\t" \
    "s_waitcnt lgkmcnt(4)\n\tv_mfma_f32_16x16x32_bf16 v[48:51], a[100:103], v[24:27], v[48:51]\n\tds_read_b128 v[44:47], %[xa] offset:56320\n\t" \
    "v_mfma_f32_16x16x32_bf16 v[56:59], a[212:215], v[24:27], v[56:59]\n\ts_waitcnt lgkmcnt(4)\n\tv_mfma_f32_16x16x32_bf16 v[60:63], a[212:215], v[28:31], v[60:63]\n\t" \
    "v_mfma_f32_16x16x32_bf16 v[52:55], a[100:103], v[28:31], v[52:55]\n\ts_waitcnt lgkmcnt(3)\n\tv_mfma_f32_16x16x32_bf16 v[48:51], a[104:107], v[32:35], v[48:51]\n\t" \
    "v_mfma_f32_16x16x32_bf16 v[56:59], a[216:219], v[32:35], v[56:59]\n\ts_waitcnt lgkmcnt(2)\n\tv_mfma_f32_16x16x32_bf16 v[60:63], a[216:219], v[36:39], v[60:63]\n\t" \
    "v_mfma_f32_16x16x32_bf16 v[52:55], a[104:107], v[36:39], v[52:55]\n\ts_waitcnt lgkmcnt(1)\n\tv_mfma_f32_16x16x32_bf16 v[48:51], a[108:111], v[40:43], v[48:51]\n\t" \
    "v_mfma_f32_16x16x32_bf16 v[56:59], a[220:223], v[40:43], v[56:59]\n\ts_waitcnt lgkmcnt(0)\n\tv_mfma_f32_16x16x32_bf16 v[60:63], a[220:223], v[44:47], v[60:63]\n\t" \
    "v_mfma_f32_16x16x32_bf16 v[52:55], a[108:111], v[44:47], v[52:55]\n\t3:\ns_mov_b32 m0, s60\n\ts_nop 7\n\tbuffer_store_dwordx4 v[48:51], %[co], %[rc], s61 offen sc1\n\t" \
    "buffer_store_dwordx4 v[52:55], %[co], %[rc], s61 offen offset:1024 sc1\n\tbuffer_store_dwordx4 v[56:59], %[co], %[rc], s61 offen offset:2048 sc1\n\t" \
    "buffer_store_dwordx4 v[60:63], %[co], %[rc], s61 offen offset:3072 sc1\n\ts_waitcnt vmcnt(4) lgkmcnt(0)\n\t"
#define ASM_OPRE \
    "s_mov_b32 s60, m0\n\tv_mov_b32 v68, 2\n\tv_mov_b32 v69, 0\n\ts_mov_b64 exec, %[em]\n\tglobal_atomic_add v64, v69, v68, %[qu] sc0\n\tglobal_load_dword v65, v69, %[li] sc1\n\ts_mov_b64 exec, -1\n\t" \
    "s_add_u32 s61, %[wb], 0\n\tbuffer_load_dwordx4 a[0:3], %[dv], %[rw], s61 offen sc1\n\tbuffer_load_dwordx4 a[4:7], %[dv], %[rw], s61 offen offset:1024 sc1\n\t" \
    "buffer_load_dwordx4 a[8:11], %[dv], %[rw], s61 offen offset:2048 sc1\n\tbuffer_load_dwordx4 a[12:15], %[dv], %[rw], s61 offen offset:3072 sc1\n\ts_add_u32 s61, s61, 0x1000\n\t" \
    "buffer_load_dwordx4 a[16:19], %[dv], %[rw], s61 offen sc1\n\tbuffer_load_dwordx4 a[20:23], %[dv], %[rw], s61 offen offset:1024 sc1\n\t" \
    "buffer_load_dwordx4 a[24:27], %[dv], %[rw], s61 offen offset:2048 sc1\n\tbuffer_load_dwordx4 a[28:31], %[dv], %[rw], s61 offen offset:3072 sc1\n\ts_add_u32 s61, s61, 0x1000\n\t" \
    "buffer_load_dwordx4 a[32:35], %[dv], %[rw], s61 offen sc1\n\tbuffer_load_dwordx4 a[36:39], %[dv], %[rw], s61 offen offset:1024 sc1\n\t" \
    "buffer_load_dwordx4 a[40:43], %[dv], %[rw], s61 offen offset:2048 sc1\n\tbuffer_load_dwordx4 a[44:47], %[dv], %[rw], s61 offen offset:3072 sc1\n\ts_add_u32 s61, s61, 0x1000\n\t" \
    "buffer_load_dwordx4 a[48:51], %[dv], %[rw], s61 offen sc1\n\tbuffer_load_dwordx4 a[52:55], %[dv], %[rw], s61 offen offset:1024 sc1\n\t" \
    "buffer_load_dwordx4 a[56:59], %[dv], %[rw], s61 offen offset:2048 sc1\n\tbuffer_load_dwordx4 a[60:63], %[dv], %[rw], s61 offen offset:3072 sc1\n\ts_add_u32 s61, s61, 0x1000\n\t" \
    "buffer_load_dwordx4 a[64:67], %[dv], %[rw], s61 offen sc1\n\tbuffer_load_dwordx4 a[68:71], %[dv], %[rw], s61 offen offset:1024 sc1\n\t" \
    "buffer_load_dwordx4 a[72:75], %[dv], %[rw], s61 offen offset:2048 sc1\n\tbuffer_load_dwordx4 a[76:79], %[dv], %[rw], s61 offen offset:3072 sc1\n\ts_add_u32 s61, s61, 0x1000\n\t" \
    "buffer_load_dwordx4 a[80:83], %[dv], %[rw], s61 offen sc1\n\tbuffer_load_dwordx4 a[84:87], %[dv], %[rw], s61 offen offset:1024 sc1\n\t" \
    "buffer_load_dwordx4 a[88:91], %[dv], %[rw], s61 offen offset:2048 sc1\n\tbuffer_load_dwordx4 a[92:95], %[dv], %[rw], s61 offen offset:3072 sc1\n\ts_add_u32 s61, s61, 0x1000\n\t" \
    "buffer_load_dwordx4 a[96:99], %[dv], %[rw], s61 offen sc1\n\tbuffer_load_dwordx4 a[100:103], %[dv], %[rw], s61 offen offset:1024 sc1\n\t" \
    "buffer_load_dwordx4 a[104:107], %[dv], %[rw], s61 offen offset:2048 sc1\n\tbuffer_load_dwordx4 a[108:111], %[dv], %[rw], s61 offen offset:3072 sc1\n\ts_add_u32 s61, %[wb], 131072\n\t" \
    "buffer_load_dwordx4 a[112:115], %[dv], %[rw], s61 offen sc1\n\tbuffer_load_dwordx4 a[116:119], %[dv], %[rw], s61 offen offset:1024 sc1\n\t" \
    "buffer_load_dwordx4 a[120:123], %[dv], %[rw], s61 offen offset:2048 sc1\n\tbuffer_load_dwordx4 a[124:127], %[dv], %[rw], s61 offen offset:3072 sc1\n\ts_add_u32 s61, s61, 0x1000\n\t" \
    "buffer_load_dwordx4 a[128:131], %[dv], %[rw], s61 offen sc1\n\tbuffer_load_dwordx4 a[132:135], %[dv], %[rw], s61 offen offset:1024 sc1\n\t" \
    "buffer_load_dwordx4 a[136:139], %[dv], %[rw], s61 offen offset:2048 sc1\n\tbuffer_load_dwordx4 a[140:143], %[dv], %[rw], s61 offen offset:3072 sc1\n\ts_add_u32 s61, s61, 0x1000\n\t" \
    "buffer_load_dwordx4 a[144:147], %[dv], %[rw], s61 offen sc1\n\tbuffer_load_dwordx4 a[148:151], %[dv], %[rw], s61 offen offset:1024 sc1\n\t" \
    "buffer_load_dwordx4 a[152:155], %[dv], %[rw], s61 offen offset:2048 sc1\n\tbuffer_load_dwordx4 a[156:159], %[dv], %[rw], s61 offen offset:3072 sc1\n\ts_add_u32 s61, s61, 0x1000\n\t" \
    "buffer_load_dwordx4 a[160:163], %[dv], %[rw], s61 offen sc1\n\tbuffer_load_dwordx4 a[164:167], %[dv], %[rw], s61 offen offset:1024 sc1\n\t" \
    "buffer_load_dwordx4 a[168:171], %[dv], %[rw], s61 offen offset:2048 sc1\n\tbuffer_load_dwordx4 a[172:175], %[dv], %[rw], s61 offen offset:3072 sc1\n\ts_add_u32 s61, s61, 0x1000\n\t" \
    "buffer_load_dwordx4 a[176:179], %[dv], %[rw], s61 offen sc1\n\tbuffer_load_dwordx4 a[180:183], %[dv], %[rw], s61 offen offset:1024 sc1\n\t" \
    "buffer_load_dwordx4 a[184:187], %[dv], %[rw], s61 offen offset:2048 sc1\n\tbuffer_load_dwordx4 a[188:191], %[dv], %[rw], s61 offen offset:3072 sc1\n\ts_add_u32 s61, s61, 0x1000\n\t" \
    "buffer_load_dwordx4 a[192:195], %[dv], %[rw], s61 offen sc1\n\tbuffer_load_dwordx4 a[196:199], %[dv], %[rw], s61 offen offset:1024 sc1\n\t" \
    "buffer_load_dwordx4 a[200:203], %[dv], %[rw], s61 offen offset:2048 sc1\n\tbuffer_load_dwordx4 a[204:207], %[dv], %[rw], s61 offen offset:3072 sc1\n\ts_add_u32 s61, s61, 0x1000\n\t" \
    "buffer_load_dwordx4 a[208:211], %[dv], %[rw], s61 offen sc1\n\tbuffer_load_dwordx4 a[212:215], %[dv], %[rw], s61 offen offset:1024 sc1\n\t" \
    "buffer_load_dwordx4 a[216:219], %[dv], %[rw], s61 offen offset:2048 sc1\n\tbuffer_load_dwordx4 a[220:223], %[dv], %[rw], s61 offen offset:3072 sc1\n\ts_waitcnt vmcnt(56)\n\tv_add_u32 v64, 1, v64\n\t" \
    "v_mov_b32 v68, 1\n\ts_mov_b64 exec, %[em]\n\tds_write_b64 %[fr], v[64:65]\n\ts_mov_b64 exec, -1\n\ts_waitcnt lgkmcnt(0)\n\ts_barrier\n\tds_read_b64 v[64:65], %[fr]\n\ts_mov_b64 exec, %[ep]\n\t" \
    "global_atomic_add %[c], v69, v68, %[qc] sc0\n\ts_mov_b64 exec, -1\n\ts_mov_b32 m0, %[lb]\n\ts_waitcnt vmcnt(48) lgkmcnt(0)\n\tv_readfirstlane_b32 %[pos], v64\n\tv_readfirstlane_b32 %[e], v65\n\t" \
    "s_nop 0\n\ts_sub_u32 %[pos], %[pos], 1\n\ts_lshr_b32 %[nx], %[pos], 3\n\ts_lshl_b32 %[nx], %[nx], 2\n\ts_lshr_b32 %[e], %[e], %[nx]\n\ts_and_b32 %[e], %[e], 15\n\ts_cmp_lt_u32 %[pos], 64\n\t" \
    "s_cselect_b32 %[e], %[e], 0\n\ts_lshl_b32 %[nx], %[e], 3\n\ts_and_b32 s61, %[pos], 7\n\ts_add_u32 %[nx], %[nx], s61\n\ts_sub_u32 %[nx], %[nx], 8\n\ts_cbranch_scc1 2f\n\ts_lshl_b32 s61, %[nx], 18\n\t" \
    "s_add_u32 s61, s61, %[sb]\n\tbuffer_load_dwordx4 %[dv], %[ra], s61 offen sc1 lds\n\tbuffer_load_dwordx4 %[dv], %[ra], s61 offen offset:1024 sc1 lds\n\ts_add_u32 m0, m0, 0x2000\n\t" \
    "s_add_u32 s61, s61, 0x2000\n\tbuffer_load_dwordx4 %[dv], %[ra], s61 offen sc1 lds\n\tbuffer_load_dwordx4 %[dv], %[ra], s61 offen offset:1024 sc1 lds\n\ts_add_u32 m0, m0, 0x2000\n\t" \
    "s_add_u32 s61, s61, 0x2000\n\tbuffer_load_dwordx4 %[dv], %[ra], s61 offen sc1 lds\n\tbuffer_load_dwordx4 %[dv], %[ra], s61 offen offset:1024 sc1 lds\n\ts_add_u32 m0, m0, 0x2000\n\t" \
    "s_add_u32 s61, s61, 0x2000\n\tbuffer_load_dwordx4 %[dv], %[ra], s61 offen sc1 lds\n\tbuffer_load_dwordx4 %[dv], %[ra], s61 offen offset:1024 sc1 lds\n\ts_add_u32 m0, m0, 0x2000\n\t" \
    "s_add_u32 s61, s61, 0x2000\n\tbuffer_load_dwordx4 %[dv], %[ra], s61 offen sc1 lds\n\tbuffer_load_dwordx4 %[dv], %[ra], s61 offen offset:1024 sc1 lds\n\ts_add_u32 m0, m0, 0x2000\n\t" \
    "s_add_u32 s61, s61, 0x2000\n\tbuffer_load_dwordx4 %[dv], %[ra], s61 offen sc1 lds\n\tbuffer_load_dwordx4 %[dv], %[ra], s61 offen offset:1024 sc1 lds\n\ts_add_u32 m0, m0, 0x2000\n\t" \
    "s_add_u32 s61, s61, 0x2000\n\ts_cmp_lt_u32 %[kq], 3\n\ts_cbranch_scc1 2f\n\tbuffer_load_dwordx4 %[dv], %[ra], s61 offen sc1 lds\n\tbuffer_load_dwordx4 %[dv], %[ra], s61 offen offset:1024 sc1 lds\n\t" \
    "2:\ns_mov_b32 m0, s60\n\ts_waitcnt vmcnt(0)\n\t"
#define ASM_STAGE_WT \
    "s_mov_b32 s60, m0\n\tv_add_u32 v16, 0x10000, %[b0]\n\tv_add_u32 v17, 0x10000, %[b1]\n\tv_add_u32 v18, 0x10000, %[b2]\n\tv_add_u32 v19, 0x10000, %[b3]\n\ts_add_u32 m0, %[lb], 0\n\ts_mov_b32 s61, 0\n\t" \
    "buffer_load_dwordx4 %[vo], %[ro], s61 offen lds\n\ts_add_u32 m0, %[lb], 1024\n\ts_mov_b32 s61, 8192\n\tbuffer_load_dwordx4 %[vo], %[ro], s61 offen lds\n\ts_add_u32 m0, %[lb], 2048\n\t" \
    "s_mov_b32 s61, 16384\n\tbuffer_load_dwordx4 %[vo], %[ro], s61 offen lds\n\ts_add_u32 m0, %[lb], 3072\n\ts_mov_b32 s61, 24576\n\tbuffer_load_dwordx4 %[vo], %[ro], s61 offen lds\n\t" \
    "s_add_u32 m0, %[lb], 16384\n\ts_mov_b32 s61, 4096\n\tbuffer_load_dwordx4 %[vo], %[ro], s61 offen lds\n\ts_add_u32 m0, %[lb], 17408\n\ts_mov_b32 s61, 12288\n\t" \
    "buffer_load_dwordx4 %[vo], %[ro], s61 offen lds\n\ts_add_u32 m0, %[lb], 18432\n\ts_mov_b32 s61, 20480\n\tbuffer_load_dwordx4 %[vo], %[ro], s61 offen lds\n\ts_add_u32 m0, %[lb], 19456\n\t" \
    "s_mov_b32 s61, 28672\n\tbuffer_load_dwordx4 %[vo], %[ro], s61 offen lds\n\ts_add_u32 m0, %[lb], 32768\n\ts_mov_b32 s61, 32768\n\tbuffer_load_dwordx4 %[vo], %[ro], s61 offen lds\n\t" \
    "s_add_u32 m0, %[lb], 33792\n\ts_mov_b32 s61, 40960\n\tbuffer_load_dwordx4 %[vo], %[ro], s61 offen lds\n\ts_add_u32 m0, %[lb], 34816\n\ts_mov_b32 s61, 49152\n\t" \
    "buffer_load_dwordx4 %[vo], %[ro], s61 offen lds\n\ts_add_u32 m0, %[lb], 35840\n\ts_mov_b32 s61, 57344\n\tbuffer_load_dwordx4 %[vo], %[ro], s61 offen lds\n\ts_add_u32 m0, %[lb], 49152\n\t" \
    "s_mov_b32 s61, 36864\n\tbuffer_load_dwordx4 %[vo], %[ro], s61 offen lds\n\ts_add_u32 m0, %[lb], 50176\n\ts_mov_b32 s61, 45056\n\tbuffer_load_dwordx4 %[vo], %[ro], s61 offen lds\n\t" \
    "s_add_u32 m0, %[lb], 51200\n\ts_mov_b32 s61, 53248\n\tbuffer_load_dwordx4 %[vo], %[ro], s61 offen lds\n\ts_add_u32 m0, %[lb], 52224\n\ts_mov_b32 s61, 61440\n\t" \
    "buffer_load_dwordx4 %[vo], %[ro], s61 offen lds\n\ts_add_u32 m0, %[lb], 65536\n\ts_mov_b32 s61, 65536\n\tbuffer_load_dwordx4 %[vo], %[ro], s61 offen lds\n\ts_add_u32 m0, %[lb], 66560\n\t" \
    "s_mov_b32 s61, 73728\n\tbuffer_load_dwordx4 %[vo], %[ro], s61 offen lds\n\ts_add_u32 m0, %[lb], 67584\n\ts_mov_b32 s61, 81920\n\tbuffer_load_dwordx4 %[vo], %[ro], s61 offen lds\n\t" \
    "s_add_u32 m0, %[lb], 68608\n\ts_mov_b32 s61, 90112\n\tbuffer_load_dwordx4 %[vo], %[ro], s61 offen lds\n\ts_add_u32 m0, %[lb], 81920\n\ts_mov_b32 s61, 69632\n\t" \
    "buffer_load_dwordx4 %[vo], %[ro], s61 offen lds\n\ts_add_u32 m0, %[lb], 82944\n\ts_mov_b32 s61, 77824\n\tbuffer_load_dwordx4 %[vo], %[ro], s61 offen lds\n\ts_add_u32 m0, %[lb], 83968\n\t" \
    "s_mov_b32 s61, 86016\n\tbuffer_load_dwordx4 %[vo], %[ro], s61 offen lds\n\ts_add_u32 m0, %[lb], 84992\n\ts_mov_b32 s61, 94208\n\tbuffer_load_dwordx4 %[vo], %[ro], s61 offen lds\n\t" \
    "s_add_u32 m0, %[lb], 98304\n\ts_mov_b32 s61, 98304\n\tbuffer_load_dwordx4 %[vo], %[ro], s61 offen lds\n\ts_add_u32 m0, %[lb], 99328\n\ts_mov_b32 s61, 106496\n\t" \
    "buffer_load_dwordx4 %[vo], %[ro], s61 offen lds\n\ts_add_u32 m0, %[lb], 100352\n\ts_mov_b32 s61, 114688\n\tbuffer_load_dwordx4 %[vo], %[ro], s61 offen lds\n\ts_add_u32 m0, %[lb], 101376\n\t" \
    "s_mov_b32 s61, 122880\n\tbuffer_load_dwordx4 %[vo], %[ro], s61 offen lds\n\ts_add_u32 m0, %[lb], 114688\n\ts_mov_b32 s61, 102400\n\tbuffer_load_dwordx4 %[vo], %[ro], s61 offen lds\n\t" \
    "s_add_u32 m0, %[lb], 115712\n\ts_mov_b32 s61, 110592\n\tbuffer_load_dwordx4 %[vo], %[ro], s61 offen lds\n\ts_add_u32 m0, %[lb], 116736\n\ts_mov_b32 s61, 118784\n\t" \
    "buffer_load_dwordx4 %[vo], %[ro], s61 offen lds\n\ts_add_u32 m0, %[lb], 117760\n\ts_mov_b32 s61, 126976\n\tbuffer_load_dwordx4 %[vo], %[ro], s61 offen lds\n\ts_waitcnt vmcnt(28)\n\t" \
    "ds_read_b64_tr_b16 a[0:1], %[b0]\n\tds_read_b64_tr_b16 a[16:17], %[b1]\n\tds_read_b64_tr_b16 a[32:33], %[b2]\n\tds_read_b64_tr_b16 a[48:49], %[b3]\n\tds_read_b64_tr_b16 a[64:65], %[b0] offset:128\n\t" \
    "ds_read_b64_tr_b16 a[80:81], %[b1] offset:128\n\tds_read_b64_tr_b16 a[96:97], %[b2] offset:128\n\tds_read_b64_tr_b16 a[112:113], %[b3] offset:128\n\ts_waitcnt lgkmcnt(0)\n\ts_waitcnt vmcnt(24)\n\t" \
    "ds_read_b64_tr_b16 a[2:3], %[b0] offset:16384\n\tds_read_b64_tr_b16 a[18:19], %[b1] offset:16384\n\tds_read_b64_tr_b16 a[34:35], %[b2] offset:16384\n\t" \
    "ds_read_b64_tr_b16 a[50:51], %[b3] offset:16384\n\tds_read_b64_tr_b16 a[66:67], %[b0] offset:16512\n\tds_read_b64_tr_b16 a[82:83], %[b1] offset:16512\n\t" \
    "ds_read_b64_tr_b16 a[98:99], %[b2] offset:16512\n\tds_read_b64_tr_b16 a[114:115], %[b3] offset:16512\n\ts_waitcnt lgkmcnt(0)\n\ts_waitcnt vmcnt(20)\n\tds_read_b64_tr_b16 a[4:5], %[b0] offset:32768\n\t" \
    "ds_read_b64_tr_b16 a[20:21], %[b1] offset:32768\n\tds_read_b64_tr_b16 a[36:37], %[b2] offset:32768\n\tds_read_b64_tr_b16 a[52:53], %[b3] offset:32768\n\t" \
    "ds_read_b64_tr_b16 a[68:69], %[b0] offset:32896\n\tds_read_b64_tr_b16 a[84:85], %[b1] offset:32896\n\tds_read_b64_tr_b16 a[100:101], %[b2] offset:32896\n\t" \
    "ds_read_b64_tr_b16 a[116:117], %[b3] offset:32896\n\ts_waitcnt lgkmcnt(0)\n\ts_waitcnt vmcnt(16)\n\tds_read_b64_tr_b16 a[6:7], %[b0] offset:49152\n\tds_read_b64_tr_b16 a[22:23], %[b1] offset:49152\n\t" \
    "ds_read_b64_tr_b16 a[38:39], %[b2] offset:49152\n\tds_read_b64_tr_b16 a[54:55], %[b3] offset:49152\n\tds_read_b64_tr_b16 a[70:71], %[b0] offset:49280\n\t" \
    "ds_read_b64_tr_b16 a[86:87], %[b1] offset:49280\n\tds_read_b64_tr_b16 a[102:103], %[b2] offset:49280\n\tds_read_b64_tr_b16 a[118:119], %[b3] offset:49280\n\ts_waitcnt lgkmcnt(0)\n\t" \
    "s_waitcnt vmcnt(12)\n\tds_read_b64_tr_b16 a[8:9], v16\n\tds_read_b64_tr_b16 a[24:25], v17\n\tds_read_b64_tr_b16 a[40:41], v18\n\tds_read_b64_tr_b16 a[56:57], v19\n\t" \
    "ds_read_b64_tr_b16 a[72:73], v16 offset:128\n\tds_read_b64_tr_b16 a[88:89], v17 offset:128\n\tds_read_b64_tr_b16 a[104:105], v18 offset:128\n\tds_read_b64_tr_b16 a[120:121], v19 offset:128\n\t" \
    "s_waitcnt lgkmcnt(0)\n\ts_waitcnt vmcnt(8)\n\tds_read_b64_tr_b16 a[10:11], v16 offset:16384\n\tds_read_b64_tr_b16 a[26:27], v17 offset:16384\n\tds_read_b64_tr_b16 a[42:43], v18 offset:16384\n\t" \
    "ds_read_b64_tr_b16 a[58:59], v19 offset:16384\n\tds_read_b64_tr_b16 a[74:75], v16 offset:16512\n\tds_read_b64_tr_b16 a[90:91], v17 offset:16512\n\tds_read_b64_tr_b16 a[106:107], v18 offset:16512\n\t" \
    "ds_read_b64_tr_b16 a[122:123], v19 offset:16512\n\ts_waitcnt lgkmcnt(0)\n\ts_waitcnt vmcnt(4)\n\tds_read_b64_tr_b16 a[12:13], v16 offset:32768\n\tds_read_b64_tr_b16 a[28:29], v17 offset:32768\n\t" \
    "ds_read_b64_tr_b16 a[44:45], v18 offset:32768\n\tds_read_b64_tr_b16 a[60:61], v19 offset:32768\n\tds_read_b64_tr_b16 a[76:77], v16 offset:32896\n\tds_read_b64_tr_b16 a[92:93], v17 offset:32896\n\t" \
    "ds_read_b64_tr_b16 a[108:109], v18 offset:32896\n\tds_read_b64_tr_b16 a[124:125], v19 offset:32896\n\ts_waitcnt lgkmcnt(0)\n\ts_waitcnt vmcnt(0)\n\tds_read_b64_tr_b16 a[14:15], v16 offset:49152\n\t" \
    "ds_read_b64_tr_b16 a[30:31], v17 offset:49152\n\tds_read_b64_tr_b16 a[46:47], v18 offset:49152\n\tds_read_b64_tr_b16 a[62:63], v19 offset:49152\n\tds_read_b64_tr_b16 a[78:79], v16 offset:49280\n\t" \
    "ds_read_b64_tr_b16 a[94:95], v17 offset:49280\n\tds_read_b64_tr_b16 a[110:111], v18 offset:49280\n\tds_read_b64_tr_b16 a[126:127], v19 offset:49280\n\ts_waitcnt lgkmcnt(0)\n\ts_mov_b32 m0, s60\n\t" \
    "s_add_u32 s61, %[wb], 0\n\tbuffer_store_dwordx4 a[0:3], %[dv], %[rw], s61 offen sc1\n\tbuffer_store_dwordx4 a[4:7], %[dv], %[rw], s61 offen offset:1024 sc1\n\t" \
    "buffer_store_dwordx4 a[8:11], %[dv], %[rw], s61 offen offset:2048 sc1\n\tbuffer_store_dwordx4 a[12:15], %[dv], %[rw], s61 offen offset:3072 sc1\n\ts_add_u32 s61, %[wb], 131072\n\t" \
    "buffer_store_dwordx4 a[16:19], %[dv], %[rw], s61 offen sc1\n\tbuffer_store_dwordx4 a[20:23], %[dv], %[rw], s61 offen offset:1024 sc1\n\t" \
    "buffer_store_dwordx4 a[24:27], %[dv], %[rw], s61 offen offset:2048 sc1\n\tbuffer_store_dwordx4 a[28:31], %[dv], %[rw], s61 offen offset:3072 sc1\n\ts_add_u32 s61, %[wb], 262144\n\t" \
    "buffer_store_dwordx4 a[32:35], %[dv], %[rw], s61 offen sc1\n\tbuffer_store_dwordx4 a[36:39], %[dv], %[rw], s61 offen offset:1024 sc1\n\t" \
    "buffer_store_dwordx4 a[40:43], %[dv], %[rw], s61 offen offset:2048 sc1\n\tbuffer_store_dwordx4 a[44:47], %[dv], %[rw], s61 offen offset:3072 sc1\n\ts_add_u32 s61, %[wb], 393216\n\t" \
    "buffer_store_dwordx4 a[48:51], %[dv], %[rw], s61 offen sc1\n\tbuffer_store_dwordx4 a[52:55], %[dv], %[rw], s61 offen offset:1024 sc1\n\t" \
    "buffer_store_dwordx4 a[56:59], %[dv], %[rw], s61 offen offset:2048 sc1\n\tbuffer_store_dwordx4 a[60:63], %[dv], %[rw], s61 offen offset:3072 sc1\n\ts_add_u32 s61, %[wb], 524288\n\t" \
    "buffer_store_dwordx4 a[64:67], %[dv], %[rw], s61 offen sc1\n\tbuffer_store_dwordx4 a[68:71], %[dv], %[rw], s61 offen offset:1024 sc1\n\t" \
    "buffer_store_dwordx4 a[72:75], %[dv], %[rw], s61 offen offset:2048 sc1\n\tbuffer_store_dwordx4 a[76:79], %[dv], %[rw], s61 offen offset:3072 sc1\n\ts_add_u32 s61, %[wb], 655360\n\t" \
    "buffer_store_dwordx4 a[80:83], %[dv], %[rw], s61 offen sc1\n\tbuffer_store_dwordx4 a[84:87], %[dv], %[rw], s61 offen offset:1024 sc1\n\t" \
    "buffer_store_dwordx4 a[88:91], %[dv], %[rw], s61 offen offset:2048 sc1\n\tbuffer_store_dwordx4 a[92:95], %[dv], %[rw], s61 offen offset:3072 sc1\n\ts_add_u32 s61, %[wb], 786432\n\t" \
    "buffer_store_dwordx4 a[96:99], %[dv], %[rw], s61 offen sc1\n\tbuffer_store_dwordx4 a[100:103], %[dv], %[rw], s61 offen offset:1024 sc1\n\t" \
    "buffer_store_dwordx4 a[104:107], %[dv], %[rw], s61 offen offset:2048 sc1\n\tbuffer_store_dwordx4 a[108:111], %[dv], %[rw], s61 offen offset:3072 sc1\n\ts_add_u32 s61, %[wb], 917504\n\t" \
    "buffer_store_dwordx4 a[112:115], %[dv], %[rw], s61 offen sc1\n\tbuffer_store_dwordx4 a[116:119], %[dv], %[rw], s61 offen offset:1024 sc1\n\t" \
    "buffer_store_dwordx4 a[120:123], %[dv], %[rw], s61 offen offset:2048 sc1\n\tbuffer_store_dwordx4 a[124:127], %[dv], %[rw], s61 offen offset:3072 sc1\n\ts_waitcnt vmcnt(0)\n\t"
#define ASM_STAGE_H \
    "s_mov_b32 s60, m0\n\tv_add_u32 v16, 0x10000, %[a0]\n\ts_add_u32 m0, %[lb], 0\n\ts_mov_b32 s61, 0\n\tbuffer_load_dwordx4 %[vv], %[rq], s61 offen lds\n\ts_add_u32 m0, %[lb], 1024\n\t" \
    "s_mov_b32 s61, 131072\n\tbuffer_load_dwordx4 %[vv], %[rq], s61 offen lds\n\ts_add_u32 m0, %[lb], 2048\n\ts_mov_b32 s61, 262144\n\tbuffer_load_dwordx4 %[vv], %[rq], s61 offen lds\n\t" \
    "s_add_u32 m0, %[lb], 3072\n\ts_mov_b32 s61, 393216\n\tbuffer_load_dwordx4 %[vv], %[rq], s61 offen lds\n\ts_add_u32 m0, %[lb], 16384\n\ts_mov_b32 s61, 524288\n\t" \
    "buffer_load_dwordx4 %[vv], %[rq], s61 offen lds\n\ts_add_u32 m0, %[lb], 17408\n\ts_mov_b32 s61, 655360\n\tbuffer_load_dwordx4 %[vv], %[rq], s61 offen lds\n\ts_add_u32 m0, %[lb], 18432\n\t" \
    "s_mov_b32 s61, 786432\n\tbuffer_load_dwordx4 %[vv], %[rq], s61 offen lds\n\ts_add_u32 m0, %[lb], 19456\n\ts_mov_b32 s61, 917504\n\tbuffer_load_dwordx4 %[vv], %[rq], s61 offen lds\n\t" \
    "s_add_u32 m0, %[lb], 32768\n\ts_mov_b32 s61, 1048576\n\tbuffer_load_dwordx4 %[vv], %[rq], s61 offen lds\n\ts_add_u32 m0, %[lb], 33792\n\ts_mov_b32 s61, 1179648\n\t" \
    "buffer_load_dwordx4 %[vv], %[rq], s61 offen lds\n\ts_add_u32 m0, %[lb], 34816\n\ts_mov_b32 s61, 1310720\n\tbuffer_load_dwordx4 %[vv], %[rq], s61 offen lds\n\ts_add_u32 m0, %[lb], 35840\n\t" \
    "s_mov_b32 s61, 1441792\n\tbuffer_load_dwordx4 %[vv], %[rq], s61 offen lds\n\ts_add_u32 m0, %[lb], 49152\n\ts_mov_b32 s61, 1572864\n\tbuffer_load_dwordx4 %[vv], %[rq], s61 offen lds\n\t" \
    "s_add_u32 m0, %[lb], 50176\n\ts_mov_b32 s61, 1703936\n\tbuffer_load_dwordx4 %[vv], %[rq], s61 offen lds\n\ts_add_u32 m0, %[lb], 51200\n\ts_mov_b32 s61, 1835008\n\t" \
    "buffer_load_dwordx4 %[vv], %[rq], s61 offen lds\n\ts_add_u32 m0, %[lb], 52224\n\ts_mov_b32 s61, 1966080\n\tbuffer_load_dwordx4 %[vv], %[rq], s61 offen lds\n\ts_add_u32 m0, %[lb], 65536\n\t" \
    "s_mov_b32 s61, 2097152\n\tbuffer_load_dwordx4 %[vv], %[rq], s61 offen lds\n\ts_add_u32 m0, %[lb], 66560\n\ts_mov_b32 s61, 2228224\n\tbuffer_load_dwordx4 %[vv], %[rq], s61 offen lds\n\t" \
    "s_add_u32 m0, %[lb], 67584\n\ts_mov_b32 s61, 2359296\n\tbuffer_load_dwordx4 %[vv], %[rq], s61 offen lds\n\ts_add_u32 m0, %[lb], 68608\n\ts_mov_b32 s61, 2490368\n\t" \
    "buffer_load_dwordx4 %[vv], %[rq], s61 offen lds\n\ts_add_u32 m0, %[lb], 81920\n\ts_mov_b32 s61, 2621440\n\tbuffer_load_dwordx4 %[vv], %[rq], s61 offen lds\n\ts_add_u32 m0, %[lb], 82944\n\t" \
    "s_mov_b32 s61, 2752512\n\tbuffer_load_dwordx4 %[vv], %[rq], s61 offen lds\n\ts_add_u32 m0, %[lb], 83968\n\ts_mov_b32 s61, 2883584\n\tbuffer_load_dwordx4 %[vv], %[rq], s61 offen lds\n\t" \
    "s_add_u32 m0, %[lb], 84992\n\ts_mov_b32 s61, 3014656\n\tbuffer_load_dwordx4 %[vv], %[rq], s61 offen lds\n\ts_add_u32 m0, %[lb], 98304\n\ts_mov_b32 s61, 3145728\n\t" \
    "buffer_load_dwordx4 %[vv], %[rq], s61 offen lds\n\ts_add_u32 m0, %[lb], 99328\n\ts_mov_b32 s61, 3276800\n\tbuffer_load_dwordx4 %[vv], %[rq], s61 offen lds\n\ts_add_u32 m0, %[lb], 100352\n\t" \
    "s_mov_b32 s61, 3407872\n\tbuffer_load_dwordx4 %[vv], %[rq], s61 offen lds\n\ts_add_u32 m0, %[lb], 101376\n\ts_mov_b32 s61, 3538944\n\tbuffer_load_dwordx4 %[vv], %[rq], s61 offen lds\n\t" \
    "s_add_u32 m0, %[lb], 114688\n\ts_mov_b32 s61, 3670016\n\tbuffer_load_dwordx4 %[vv], %[rq], s61 offen lds\n\ts_add_u32 m0, %[lb], 115712\n\ts_mov_b32 s61, 3801088\n\t" \
    "buffer_load_dwordx4 %[vv], %[rq], s61 offen lds\n\ts_add_u32 m0, %[lb], 116736\n\ts_mov_b32 s61, 3932160\n\tbuffer_load_dwordx4 %[vv], %[rq], s61 offen lds\n\ts_add_u32 m0, %[lb], 117760\n\t" \
    "s_mov_b32 s61, 4063232\n\tbuffer_load_dwordx4 %[vv], %[rq], s61 offen lds\n\ts_waitcnt vmcnt(28)\n\tds_read_b64_tr_b16 a[0:1], %[a0]\n\tds_read_b64_tr_b16 a[64:65], %[a0] offset:32\n\t" \
    "ds_read_b64_tr_b16 a[2:3], %[a0] offset:256\n\tds_read_b64_tr_b16 a[66:67], %[a0] offset:288\n\tds_read_b64_tr_b16 a[4:5], %[a0] offset:1024\n\tds_read_b64_tr_b16 a[68:69], %[a0] offset:1056\n\t" \
    "ds_read_b64_tr_b16 a[6:7], %[a0] offset:1280\n\tds_read_b64_tr_b16 a[70:71], %[a0] offset:1312\n\ts_waitcnt lgkmcnt(0)\n\ts_add_u32 m0, %[lb], 0\n\ts_mov_b32 s61, 0\n\t" \
    "buffer_load_dwordx4 %[vv], %[rk], s61 offen lds\n\ts_add_u32 m0, %[lb], 1024\n\ts_mov_b32 s61, 131072\n\tbuffer_load_dwordx4 %[vv], %[rk], s61 offen lds\n\ts_add_u32 m0, %[lb], 2048\n\t" \
    "s_mov_b32 s61, 262144\n\tbuffer_load_dwordx4 %[vv], %[rk], s61 offen lds\n\ts_add_u32 m0, %[lb], 3072\n\ts_mov_b32 s61, 393216\n\tbuffer_load_dwordx4 %[vv], %[rk], s61 offen lds\n\t" \
    "s_waitcnt vmcnt(28)\n\tds_read_b64_tr_b16 a[8:9], %[a0] offset:16384\n\tds_read_b64_tr_b16 a[72:73], %[a0] offset:16416\n\tds_read_b64_tr_b16 a[10:11], %[a0] offset:16640\n\t" \
    "ds_read_b64_tr_b16 a[74:75], %[a0] offset:16672\n\tds_read_b64_tr_b16 a[12:13], %[a0] offset:17408\n\tds_read_b64_tr_b16 a[76:77], %[a0] offset:17440\n\t" \
    "ds_read_b64_tr_b16 a[14:15], %[a0] offset:17664\n\tds_read_b64_tr_b16 a[78:79], %[a0] offset:17696\n\ts_waitcnt lgkmcnt(0)\n\ts_add_u32 m0, %[lb], 16384\n\ts_mov_b32 s61, 524288\n\t" \
    "buffer_load_dwordx4 %[vv], %[rk], s61 offen lds\n\ts_add_u32 m0, %[lb], 17408\n\ts_mov_b32 s61, 655360\n\tbuffer_load_dwordx4 %[vv], %[rk], s61 offen lds\n\ts_add_u32 m0, %[lb], 18432\n\t" \
    "s_mov_b32 s61, 786432\n\tbuffer_load_dwordx4 %[vv], %[rk], s61 offen lds\n\ts_add_u32 m0, %[lb], 19456\n\ts_mov_b32 s61, 917504\n\tbuffer_load_dwordx4 %[vv], %[rk], s61 offen lds\n\t" \
    "s_waitcnt vmcnt(28)\n\tds_read_b64_tr_b16 a[16:17], %[a0] offset:32768\n\tds_read_b64_tr_b16 a[80:81], %[a0] offset:32800\n\tds_read_b64_tr_b16 a[18:19], %[a0] offset:33024\n\t" \
    "ds_read_b64_tr_b16 a[82:83], %[a0] offset:33056\n\tds_read_b64_tr_b16 a[20:21], %[a0] offset:33792\n\tds_read_b64_tr_b16 a[84:85], %[a0] offset:33824\n\t" \
    "ds_read_b64_tr_b16 a[22:23], %[a0] offset:34048\n\tds_read_b64_tr_b16 a[86:87], %[a0] offset:34080\n\ts_waitcnt lgkmcnt(0)\n\ts_add_u32 m0, %[lb], 32768\n\ts_mov_b32 s61, 1048576\n\t" \
    "buffer_load_dwordx4 %[vv], %[rk], s61 offen lds\n\ts_add_u32 m0, %[lb], 33792\n\ts_mov_b32 s61, 1179648\n\tbuffer_load_dwordx4 %[vv], %[rk], s61 offen lds\n\ts_add_u32 m0, %[lb], 34816\n\t" \
    "s_mov_b32 s61, 1310720\n\tbuffer_load_dwordx4 %[vv], %[rk], s61 offen lds\n\ts_add_u32 m0, %[lb], 35840\n\ts_mov_b32 s61, 1441792\n\tbuffer_load_dwordx4 %[vv], %[rk], s61 offen lds\n\t" \
    "s_waitcnt vmcnt(28)\n\tds_read_b64_tr_b16 a[24:25], %[a0] offset:49152\n\tds_read_b64_tr_b16 a[88:89], %[a0] offset:49184\n\tds_read_b64_tr_b16 a[26:27], %[a0] offset:49408\n\t" \
    "ds_read_b64_tr_b16 a[90:91], %[a0] offset:49440\n\tds_read_b64_tr_b16 a[28:29], %[a0] offset:50176\n\tds_read_b64_tr_b16 a[92:93], %[a0] offset:50208\n\t" \
    "ds_read_b64_tr_b16 a[30:31], %[a0] offset:50432\n\tds_read_b64_tr_b16 a[94:95], %[a0] offset:50464\n\ts_waitcnt lgkmcnt(0)\n\ts_add_u32 m0, %[lb], 49152\n\ts_mov_b32 s61, 1572864\n\t" \
    "buffer_load_dwordx4 %[vv], %[rk], s61 offen lds\n\ts_add_u32 m0, %[lb], 50176\n\ts_mov_b32 s61, 1703936\n\tbuffer_load_dwordx4 %[vv], %[rk], s61 offen lds\n\ts_add_u32 m0, %[lb], 51200\n\t" \
    "s_mov_b32 s61, 1835008\n\tbuffer_load_dwordx4 %[vv], %[rk], s61 offen lds\n\ts_add_u32 m0, %[lb], 52224\n\ts_mov_b32 s61, 1966080\n\tbuffer_load_dwordx4 %[vv], %[rk], s61 offen lds\n\t" \
    "s_waitcnt vmcnt(28)\n\tds_read_b64_tr_b16 a[32:33], v16\n\tds_read_b64_tr_b16 a[96:97], v16 offset:32\n\tds_read_b64_tr_b16 a[34:35], v16 offset:256\n\tds_read_b64_tr_b16 a[98:99], v16 offset:288\n\t" \
    "ds_read_b64_tr_b16 a[36:37], v16 offset:1024\n\tds_read_b64_tr_b16 a[100:101], v16 offset:1056\n\tds_read_b64_tr_b16 a[38:39], v16 offset:1280\n\tds_read_b64_tr_b16 a[102:103], v16 offset:1312\n\t" \
    "s_waitcnt lgkmcnt(0)\n\ts_add_u32 m0, %[lb], 65536\n\ts_mov_b32 s61, 2097152\n\tbuffer_load_dwordx4 %[vv], %[rk], s61 offen lds\n\ts_add_u32 m0, %[lb], 66560\n\ts_mov_b32 s61, 2228224\n\t" \
    "buffer_load_dwordx4 %[vv], %[rk], s61 offen lds\n\ts_add_u32 m0, %[lb], 67584\n\ts_mov_b32 s61, 2359296\n\tbuffer_load_dwordx4 %[vv], %[rk], s61 offen lds\n\ts_add_u32 m0, %[lb], 68608\n\t" \
    "s_mov_b32 s61, 2490368\n\tbuffer_load_dwordx4 %[vv], %[rk], s61 offen lds\n\ts_waitcnt vmcnt(28)\n\tds_read_b64_tr_b16 a[40:41], v16 offset:16384\n\tds_read_b64_tr_b16 a[104:105], v16 offset:16416\n\t" \
    "ds_read_b64_tr_b16 a[42:43], v16 offset:16640\n\tds_read_b64_tr_b16 a[106:107], v16 offset:16672\n\tds_read_b64_tr_b16 a[44:45], v16 offset:17408\n\tds_read_b64_tr_b16 a[108:109], v16 offset:17440\n\t" \
    "ds_read_b64_tr_b16 a[46:47], v16 offset:17664\n\tds_read_b64_tr_b16 a[110:111], v16 offset:17696\n\ts_waitcnt lgkmcnt(0)\n\ts_add_u32 m0, %[lb], 81920\n\ts_mov_b32 s61, 2621440\n\t" \
    "buffer_load_dwordx4 %[vv], %[rk], s61 offen lds\n\ts_add_u32 m0, %[lb], 82944\n\ts_mov_b32 s61, 2752512\n\tbuffer_load_dwordx4 %[vv], %[rk], s61 offen lds\n\ts_add_u32 m0, %[lb], 83968\n\t" \
    "s_mov_b32 s61, 2883584\n\tbuffer_load_dwordx4 %[vv], %[rk], s61 offen lds\n\ts_add_u32 m0, %[lb], 84992\n\ts_mov_b32 s61, 3014656\n\tbuffer_load_dwordx4 %[vv], %[rk], s61 offen lds\n\t" \
    "s_waitcnt vmcnt(28)\n\tds_read_b64_tr_b16 a[48:49], v16 offset:32768\n\tds_read_b64_tr_b16 a[112:113], v16 offset:32800\n\tds_read_b64_tr_b16 a[50:51], v16 offset:33024\n\t" \
    "ds_read_b64_tr_b16 a[114:115], v16 offset:33056\n\tds_read_b64_tr_b16 a[52:53], v16 offset:33792\n\tds_read_b64_tr_b16 a[116:117], v16 offset:33824\n\tds_read_b64_tr_b16 a[54:55], v16 offset:34048\n\t" \
    "ds_read_b64_tr_b16 a[118:119], v16 offset:34080\n\ts_waitcnt lgkmcnt(0)\n\ts_add_u32 m0, %[lb], 98304\n\ts_mov_b32 s61, 3145728\n\tbuffer_load_dwordx4 %[vv], %[rk], s61 offen lds\n\t" \
    "s_add_u32 m0, %[lb], 99328\n\ts_mov_b32 s61, 3276800\n\tbuffer_load_dwordx4 %[vv], %[rk], s61 offen lds\n\ts_add_u32 m0, %[lb], 100352\n\ts_mov_b32 s61, 3407872\n\t" \
    "buffer_load_dwordx4 %[vv], %[rk], s61 offen lds\n\ts_add_u32 m0, %[lb], 101376\n\ts_mov_b32 s61, 3538944\n\tbuffer_load_dwordx4 %[vv], %[rk], s61 offen lds\n\ts_waitcnt vmcnt(28)\n\t" \
    "ds_read_b64_tr_b16 a[56:57], v16 offset:49152\n\tds_read_b64_tr_b16 a[120:121], v16 offset:49184\n\tds_read_b64_tr_b16 a[58:59], v16 offset:49408\n\tds_read_b64_tr_b16 a[122:123], v16 offset:49440\n\t" \
    "ds_read_b64_tr_b16 a[60:61], v16 offset:50176\n\tds_read_b64_tr_b16 a[124:125], v16 offset:50208\n\tds_read_b64_tr_b16 a[62:63], v16 offset:50432\n\tds_read_b64_tr_b16 a[126:127], v16 offset:50464\n\t" \
    "s_waitcnt lgkmcnt(0)\n\ts_add_u32 m0, %[lb], 114688\n\ts_mov_b32 s61, 3670016\n\tbuffer_load_dwordx4 %[vv], %[rk], s61 offen lds\n\ts_add_u32 m0, %[lb], 115712\n\ts_mov_b32 s61, 3801088\n\t" \
    "buffer_load_dwordx4 %[vv], %[rk], s61 offen lds\n\ts_add_u32 m0, %[lb], 116736\n\ts_mov_b32 s61, 3932160\n\tbuffer_load_dwordx4 %[vv], %[rk], s61 offen lds\n\ts_add_u32 m0, %[lb], 117760\n\t" \
    "s_mov_b32 s61, 4063232\n\tbuffer_load_dwordx4 %[vv], %[rk], s61 offen lds\n\ts_waitcnt vmcnt(28)\n\tds_read_b64_tr_b16 a[128:129], %[a0]\n\tds_read_b64_tr_b16 a[192:193], %[a0] offset:32\n\t" \
    "ds_read_b64_tr_b16 a[130:131], %[a0] offset:256\n\tds_read_b64_tr_b16 a[194:195], %[a0] offset:288\n\tds_read_b64_tr_b16 a[132:133], %[a0] offset:1024\n\t" \
    "ds_read_b64_tr_b16 a[196:197], %[a0] offset:1056\n\tds_read_b64_tr_b16 a[134:135], %[a0] offset:1280\n\tds_read_b64_tr_b16 a[198:199], %[a0] offset:1312\n\ts_waitcnt lgkmcnt(0)\n\t" \
    "s_waitcnt vmcnt(24)\n\tds_read_b64_tr_b16 a[136:137], %[a0] offset:16384\n\tds_read_b64_tr_b16 a[200:201], %[a0] offset:16416\n\tds_read_b64_tr_b16 a[138:139], %[a0] offset:16640\n\t" \
    "ds_read_b64_tr_b16 a[202:203], %[a0] offset:16672\n\tds_read_b64_tr_b16 a[140:141], %[a0] offset:17408\n\tds_read_b64_tr_b16 a[204:205], %[a0] offset:17440\n\t" \
    "ds_read_b64_tr_b16 a[142:143], %[a0] offset:17664\n\tds_read_b64_tr_b16 a[206:207], %[a0] offset:17696\n\ts_waitcnt lgkmcnt(0)\n\ts_waitcnt vmcnt(20)\n\t" \
    "ds_read_b64_tr_b16 a[144:145], %[a0] offset:32768\n\tds_read_b64_tr_b16 a[208:209], %[a0] offset:32800\n\tds_read_b64_tr_b16 a[146:147], %[a0] offset:33024\n\t" \
    "ds_read_b64_tr_b16 a[210:211], %[a0] offset:33056\n\tds_read_b64_tr_b16 a[148:149], %[a0] offset:33792\n\tds_read_b64_tr_b16 a[212:213], %[a0] offset:33824\n\t" \
    "ds_read_b64_tr_b16 a[150:151], %[a0] offset:34048\n\tds_read_b64_tr_b16 a[214:215], %[a0] offset:34080\n\ts_waitcnt lgkmcnt(0)\n\ts_waitcnt vmcnt(16)\n\t" \
    "ds_read_b64_tr_b16 a[152:153], %[a0] offset:49152\n\tds_read_b64_tr_b16 a[216:217], %[a0] offset:49184\n\tds_read_b64_tr_b16 a[154:155], %[a0] offset:49408\n\t" \
    "ds_read_b64_tr_b16 a[218:219], %[a0] offset:49440\n\tds_read_b64_tr_b16 a[156:157], %[a0] offset:50176\n\tds_read_b64_tr_b16 a[220:221], %[a0] offset:50208\n\t" \
    "ds_read_b64_tr_b16 a[158:159], %[a0] offset:50432\n\tds_read_b64_tr_b16 a[222:223], %[a0] offset:50464\n\ts_waitcnt lgkmcnt(0)\n\ts_waitcnt vmcnt(12)\n\tds_read_b64_tr_b16 a[160:161], v16\n\t" \
    "ds_read_b64_tr_b16 a[224:225], v16 offset:32\n\tds_read_b64_tr_b16 a[162:163], v16 offset:256\n\tds_read_b64_tr_b16 a[226:227], v16 offset:288\n\tds_read_b64_tr_b16 a[164:165], v16 offset:1024\n\t" \
    "ds_read_b64_tr_b16 a[228:229], v16 offset:1056\n\tds_read_b64_tr_b16 a[166:167], v16 offset:1280\n\tds_read_b64_tr_b16 a[230:231], v16 offset:1312\n\ts_waitcnt lgkmcnt(0)\n\ts_waitcnt vmcnt(8)\n\t" \
    "ds_read_b64_tr_b16 a[168:169], v16 offset:16384\n\tds_read_b64_tr_b16 a[232:233], v16 offset:16416\n\tds_read_b64_tr_b16 a[170:171], v16 offset:16640\n\t" \
    "ds_read_b64_tr_b16 a[234:235], v16 offset:16672\n\tds_read_b64_tr_b16 a[172:173], v16 offset:17408\n\tds_read_b64_tr_b16 a[236:237], v16 offset:17440\n\t" \
    "ds_read_b64_tr_b16 a[174:175], v16 offset:17664\n\tds_read_b64_tr_b16 a[238:239], v16 offset:17696\n\ts_waitcnt lgkmcnt(0)\n\ts_waitcnt vmcnt(4)\n\tds_read_b64_tr_b16 a[176:177], v16 offset:32768\n\t" \
    "ds_read_b64_tr_b16 a[240:241], v16 offset:32800\n\tds_read_b64_tr_b16 a[178:179], v16 offset:33024\n\tds_read_b64_tr_b16 a[242:243], v16 offset:33056\n\t" \
    "ds_read_b64_tr_b16 a[180:181], v16 offset:33792\n\tds_read_b64_tr_b16 a[244:245], v16 offset:33824\n\tds_read_b64_tr_b16 a[182:183], v16 offset:34048\n\t" \
    "ds_read_b64_tr_b16 a[246:247], v16 offset:34080\n\ts_waitcnt lgkmcnt(0)\n\ts_waitcnt vmcnt(0)\n\tds_read_b64_tr_b16 a[184:185], v16 offset:49152\n\tds_read_b64_tr_b16 a[248:249], v16 offset:49184\n\t" \
    "ds_read_b64_tr_b16 a[186:187], v16 offset:49408\n\tds_read_b64_tr_b16 a[250:251], v16 offset:49440\n\tds_read_b64_tr_b16 a[188:189], v16 offset:50176\n\t" \
    "ds_read_b64_tr_b16 a[252:253], v16 offset:50208\n\tds_read_b64_tr_b16 a[190:191], v16 offset:50432\n\tds_read_b64_tr_b16 a[254:255], v16 offset:50464\n\ts_waitcnt lgkmcnt(0)\n\ts_mov_b32 m0, s60\n\t"
#define ASM_STAGE_V \
    "s_mov_b32 s60, m0\n\tv_add_u32 v16, 0x10000, %[a0]\n\tv_add_u32 v17, 0x10000, %[b0]\n\tv_add_u32 v18, 0x10000, %[b1]\n\tv_add_u32 v19, 0x10000, %[b2]\n\tv_add_u32 v20, 0x10000, %[b3]\n\t" \
    "s_add_u32 m0, %[lb], 0\n\ts_mov_b32 s61, 0\n\tbuffer_load_dwordx4 %[vv], %[rv], s61 offen lds\n\ts_add_u32 m0, %[lb], 1024\n\ts_mov_b32 s61, 131072\n\t" \
    "buffer_load_dwordx4 %[vv], %[rv], s61 offen lds\n\ts_add_u32 m0, %[lb], 2048\n\ts_mov_b32 s61, 262144\n\tbuffer_load_dwordx4 %[vv], %[rv], s61 offen lds\n\ts_add_u32 m0, %[lb], 3072\n\t" \
    "s_mov_b32 s61, 393216\n\tbuffer_load_dwordx4 %[vv], %[rv], s61 offen lds\n\ts_add_u32 m0, %[lb], 16384\n\ts_mov_b32 s61, 524288\n\tbuffer_load_dwordx4 %[vv], %[rv], s61 offen lds\n\t" \
    "s_add_u32 m0, %[lb], 17408\n\ts_mov_b32 s61, 655360\n\tbuffer_load_dwordx4 %[vv], %[rv], s61 offen lds\n\ts_add_u32 m0, %[lb], 18432\n\ts_mov_b32 s61, 786432\n\t" \
    "buffer_load_dwordx4 %[vv], %[rv], s61 offen lds\n\ts_add_u32 m0, %[lb], 19456\n\ts_mov_b32 s61, 917504\n\tbuffer_load_dwordx4 %[vv], %[rv], s61 offen lds\n\ts_add_u32 m0, %[lb], 32768\n\t" \
    "s_mov_b32 s61, 1048576\n\tbuffer_load_dwordx4 %[vv], %[rv], s61 offen lds\n\ts_add_u32 m0, %[lb], 33792\n\ts_mov_b32 s61, 1179648\n\tbuffer_load_dwordx4 %[vv], %[rv], s61 offen lds\n\t" \
    "s_add_u32 m0, %[lb], 34816\n\ts_mov_b32 s61, 1310720\n\tbuffer_load_dwordx4 %[vv], %[rv], s61 offen lds\n\ts_add_u32 m0, %[lb], 35840\n\ts_mov_b32 s61, 1441792\n\t" \
    "buffer_load_dwordx4 %[vv], %[rv], s61 offen lds\n\ts_add_u32 m0, %[lb], 49152\n\ts_mov_b32 s61, 1572864\n\tbuffer_load_dwordx4 %[vv], %[rv], s61 offen lds\n\ts_add_u32 m0, %[lb], 50176\n\t" \
    "s_mov_b32 s61, 1703936\n\tbuffer_load_dwordx4 %[vv], %[rv], s61 offen lds\n\ts_add_u32 m0, %[lb], 51200\n\ts_mov_b32 s61, 1835008\n\tbuffer_load_dwordx4 %[vv], %[rv], s61 offen lds\n\t" \
    "s_add_u32 m0, %[lb], 52224\n\ts_mov_b32 s61, 1966080\n\tbuffer_load_dwordx4 %[vv], %[rv], s61 offen lds\n\ts_add_u32 m0, %[lb], 65536\n\ts_mov_b32 s61, 2097152\n\t" \
    "buffer_load_dwordx4 %[vv], %[rv], s61 offen lds\n\ts_add_u32 m0, %[lb], 66560\n\ts_mov_b32 s61, 2228224\n\tbuffer_load_dwordx4 %[vv], %[rv], s61 offen lds\n\ts_add_u32 m0, %[lb], 67584\n\t" \
    "s_mov_b32 s61, 2359296\n\tbuffer_load_dwordx4 %[vv], %[rv], s61 offen lds\n\ts_add_u32 m0, %[lb], 68608\n\ts_mov_b32 s61, 2490368\n\tbuffer_load_dwordx4 %[vv], %[rv], s61 offen lds\n\t" \
    "s_add_u32 m0, %[lb], 81920\n\ts_mov_b32 s61, 2621440\n\tbuffer_load_dwordx4 %[vv], %[rv], s61 offen lds\n\ts_add_u32 m0, %[lb], 82944\n\ts_mov_b32 s61, 2752512\n\t" \
    "buffer_load_dwordx4 %[vv], %[rv], s61 offen lds\n\ts_add_u32 m0, %[lb], 83968\n\ts_mov_b32 s61, 2883584\n\tbuffer_load_dwordx4 %[vv], %[rv], s61 offen lds\n\ts_add_u32 m0, %[lb], 84992\n\t" \
    "s_mov_b32 s61, 3014656\n\tbuffer_load_dwordx4 %[vv], %[rv], s61 offen lds\n\ts_add_u32 m0, %[lb], 98304\n\ts_mov_b32 s61, 3145728\n\tbuffer_load_dwordx4 %[vv], %[rv], s61 offen lds\n\t" \
    "s_add_u32 m0, %[lb], 99328\n\ts_mov_b32 s61, 3276800\n\tbuffer_load_dwordx4 %[vv], %[rv], s61 offen lds\n\ts_add_u32 m0, %[lb], 100352\n\ts_mov_b32 s61, 3407872\n\t" \
    "buffer_load_dwordx4 %[vv], %[rv], s61 offen lds\n\ts_add_u32 m0, %[lb], 101376\n\ts_mov_b32 s61, 3538944\n\tbuffer_load_dwordx4 %[vv], %[rv], s61 offen lds\n\ts_add_u32 m0, %[lb], 114688\n\t" \
    "s_mov_b32 s61, 3670016\n\tbuffer_load_dwordx4 %[vv], %[rv], s61 offen lds\n\ts_add_u32 m0, %[lb], 115712\n\ts_mov_b32 s61, 3801088\n\tbuffer_load_dwordx4 %[vv], %[rv], s61 offen lds\n\t" \
    "s_add_u32 m0, %[lb], 116736\n\ts_mov_b32 s61, 3932160\n\tbuffer_load_dwordx4 %[vv], %[rv], s61 offen lds\n\ts_add_u32 m0, %[lb], 117760\n\ts_mov_b32 s61, 4063232\n\t" \
    "buffer_load_dwordx4 %[vv], %[rv], s61 offen lds\n\ts_waitcnt vmcnt(28)\n\tds_read_b64_tr_b16 a[0:1], %[a0]\n\tds_read_b64_tr_b16 a[64:65], %[a0] offset:32\n\t" \
    "ds_read_b64_tr_b16 a[2:3], %[a0] offset:256\n\tds_read_b64_tr_b16 a[66:67], %[a0] offset:288\n\tds_read_b64_tr_b16 a[4:5], %[a0] offset:1024\n\tds_read_b64_tr_b16 a[68:69], %[a0] offset:1056\n\t" \
    "ds_read_b64_tr_b16 a[6:7], %[a0] offset:1280\n\tds_read_b64_tr_b16 a[70:71], %[a0] offset:1312\n\ts_waitcnt lgkmcnt(0)\n\ts_add_u32 m0, %[lb], 0\n\ts_mov_b32 s61, 0\n\t" \
    "buffer_load_dwordx4 %[vo], %[ro], s61 offen lds\n\ts_add_u32 m0, %[lb], 1024\n\ts_mov_b32 s61, 4096\n\tbuffer_load_dwordx4 %[vo], %[ro], s61 offen lds\n\ts_add_u32 m0, %[lb], 2048\n\t" \
    "s_mov_b32 s61, 8192\n\tbuffer_load_dwordx4 %[vo], %[ro], s61 offen lds\n\ts_add_u32 m0, %[lb], 3072\n\ts_mov_b32 s61, 12288\n\tbuffer_load_dwordx4 %[vo], %[ro], s61 offen lds\n\t" \
    "s_waitcnt vmcnt(28)\n\tds_read_b64_tr_b16 a[8:9], %[a0] offset:16384\n\tds_read_b64_tr_b16 a[72:73], %[a0] offset:16416\n\tds_read_b64_tr_b16 a[10:11], %[a0] offset:16640\n\t" \
    "ds_read_b64_tr_b16 a[74:75], %[a0] offset:16672\n\tds_read_b64_tr_b16 a[12:13], %[a0] offset:17408\n\tds_read_b64_tr_b16 a[76:77], %[a0] offset:17440\n\t" \
    "ds_read_b64_tr_b16 a[14:15], %[a0] offset:17664\n\tds_read_b64_tr_b16 a[78:79], %[a0] offset:17696\n\ts_waitcnt lgkmcnt(0)\n\ts_add_u32 m0, %[lb], 16384\n\ts_mov_b32 s61, 32768\n\t" \
    "buffer_load_dwordx4 %[vo], %[ro], s61 offen lds\n\ts_add_u32 m0, %[lb], 17408\n\ts_mov_b32 s61, 36864\n\tbuffer_load_dwordx4 %[vo], %[ro], s61 offen lds\n\ts_add_u32 m0, %[lb], 18432\n\t" \
    "s_mov_b32 s61, 40960\n\tbuffer_load_dwordx4 %[vo], %[ro], s61 offen lds\n\ts_add_u32 m0, %[lb], 19456\n\ts_mov_b32 s61, 45056\n\tbuffer_load_dwordx4 %[vo], %[ro], s61 offen lds\n\t" \
    "s_waitcnt vmcnt(28)\n\tds_read_b64_tr_b16 a[16:17], %[a0] offset:32768\n\tds_read_b64_tr_b16 a[80:81], %[a0] offset:32800\n\tds_read_b64_tr_b16 a[18:19], %[a0] offset:33024\n\t" \
    "ds_read_b64_tr_b16 a[82:83], %[a0] offset:33056\n\tds_read_b64_tr_b16 a[20:21], %[a0] offset:33792\n\tds_read_b64_tr_b16 a[84:85], %[a0] offset:33824\n\t" \
    "ds_read_b64_tr_b16 a[22:23], %[a0] offset:34048\n\tds_read_b64_tr_b16 a[86:87], %[a0] offset:34080\n\ts_waitcnt lgkmcnt(0)\n\ts_add_u32 m0, %[lb], 32768\n\ts_mov_b32 s61, 65536\n\t" \
    "buffer_load_dwordx4 %[vo], %[ro], s61 offen lds\n\ts_add_u32 m0, %[lb], 33792\n\ts_mov_b32 s61, 69632\n\tbuffer_load_dwordx4 %[vo], %[ro], s61 offen lds\n\ts_add_u32 m0, %[lb], 34816\n\t" \
    "s_mov_b32 s61, 73728\n\tbuffer_load_dwordx4 %[vo], %[ro], s61 offen lds\n\ts_add_u32 m0, %[lb], 35840\n\ts_mov_b32 s61, 77824\n\tbuffer_load_dwordx4 %[vo], %[ro], s61 offen lds\n\t" \
    "s_waitcnt vmcnt(28)\n\tds_read_b64_tr_b16 a[24:25], %[a0] offset:49152\n\tds_read_b64_tr_b16 a[88:89], %[a0] offset:49184\n\tds_read_b64_tr_b16 a[26:27], %[a0] offset:49408\n\t" \
    "ds_read_b64_tr_b16 a[90:91], %[a0] offset:49440\n\tds_read_b64_tr_b16 a[28:29], %[a0] offset:50176\n\tds_read_b64_tr_b16 a[92:93], %[a0] offset:50208\n\t" \
    "ds_read_b64_tr_b16 a[30:31], %[a0] offset:50432\n\tds_read_b64_tr_b16 a[94:95], %[a0] offset:50464\n\ts_waitcnt lgkmcnt(0)\n\ts_add_u32 m0, %[lb], 49152\n\ts_mov_b32 s61, 98304\n\t" \
    "buffer_load_dwordx4 %[vo], %[ro], s61 offen lds\n\ts_add_u32 m0, %[lb], 50176\n\ts_mov_b32 s61, 102400\n\tbuffer_load_dwordx4 %[vo], %[ro], s61 offen lds\n\ts_add_u32 m0, %[lb], 51200\n\t" \
    "s_mov_b32 s61, 106496\n\tbuffer_load_dwordx4 %[vo], %[ro], s61 offen lds\n\ts_add_u32 m0, %[lb], 52224\n\ts_mov_b32 s61, 110592\n\tbuffer_load_dwordx4 %[vo], %[ro], s61 offen lds\n\t" \
    "s_waitcnt vmcnt(28)\n\tds_read_b64_tr_b16 a[32:33], v16\n\tds_read_b64_tr_b16 a[96:97], v16 offset:32\n\tds_read_b64_tr_b16 a[34:35], v16 offset:256\n\tds_read_b64_tr_b16 a[98:99], v16 offset:288\n\t" \
    "ds_read_b64_tr_b16 a[36:37], v16 offset:1024\n\tds_read_b64_tr_b16 a[100:101], v16 offset:1056\n\tds_read_b64_tr_b16 a[38:39], v16 offset:1280\n\tds_read_b64_tr_b16 a[102:103], v16 offset:1312\n\t" \
    "s_waitcnt lgkmcnt(0)\n\ts_add_u32 m0, %[lb], 65536\n\ts_mov_b32 s61, 16384\n\tbuffer_load_dwordx4 %[vo], %[ro], s61 offen lds\n\ts_add_u32 m0, %[lb], 66560\n\ts_mov_b32 s61, 20480\n\t" \
    "buffer_load_dwordx4 %[vo], %[ro], s61 offen lds\n\ts_add_u32 m0, %[lb], 67584\n\ts_mov_b32 s61, 24576\n\tbuffer_load_dwordx4 %[vo], %[ro], s61 offen lds\n\ts_add_u32 m0, %[lb], 68608\n\t" \
    "s_mov_b32 s61, 28672\n\tbuffer_load_dwordx4 %[vo], %[ro], s61 offen lds\n\ts_waitcnt vmcnt(28)\n\tds_read_b64_tr_b16 a[40:41], v16 offset:16384\n\tds_read_b64_tr_b16 a[104:105], v16 offset:16416\n\t" \
    "ds_read_b64_tr_b16 a[42:43], v16 offset:16640\n\tds_read_b64_tr_b16 a[106:107], v16 offset:16672\n\tds_read_b64_tr_b16 a[44:45], v16 offset:17408\n\tds_read_b64_tr_b16 a[108:109], v16 offset:17440\n\t" \
    "ds_read_b64_tr_b16 a[46:47], v16 offset:17664\n\tds_read_b64_tr_b16 a[110:111], v16 offset:17696\n\ts_waitcnt lgkmcnt(0)\n\ts_add_u32 m0, %[lb], 81920\n\ts_mov_b32 s61, 49152\n\t" \
    "buffer_load_dwordx4 %[vo], %[ro], s61 offen lds\n\ts_add_u32 m0, %[lb], 82944\n\ts_mov_b32 s61, 53248\n\tbuffer_load_dwordx4 %[vo], %[ro], s61 offen lds\n\ts_add_u32 m0, %[lb], 83968\n\t" \
    "s_mov_b32 s61, 57344\n\tbuffer_load_dwordx4 %[vo], %[ro], s61 offen lds\n\ts_add_u32 m0, %[lb], 84992\n\ts_mov_b32 s61, 61440\n\tbuffer_load_dwordx4 %[vo], %[ro], s61 offen lds\n\t" \
    "s_waitcnt vmcnt(28)\n\tds_read_b64_tr_b16 a[48:49], v16 offset:32768\n\tds_read_b64_tr_b16 a[112:113], v16 offset:32800\n\tds_read_b64_tr_b16 a[50:51], v16 offset:33024\n\t" \
    "ds_read_b64_tr_b16 a[114:115], v16 offset:33056\n\tds_read_b64_tr_b16 a[52:53], v16 offset:33792\n\tds_read_b64_tr_b16 a[116:117], v16 offset:33824\n\tds_read_b64_tr_b16 a[54:55], v16 offset:34048\n\t" \
    "ds_read_b64_tr_b16 a[118:119], v16 offset:34080\n\ts_waitcnt lgkmcnt(0)\n\ts_add_u32 m0, %[lb], 98304\n\ts_mov_b32 s61, 81920\n\tbuffer_load_dwordx4 %[vo], %[ro], s61 offen lds\n\t" \
    "s_add_u32 m0, %[lb], 99328\n\ts_mov_b32 s61, 86016\n\tbuffer_load_dwordx4 %[vo], %[ro], s61 offen lds\n\ts_add_u32 m0, %[lb], 100352\n\ts_mov_b32 s61, 90112\n\t" \
    "buffer_load_dwordx4 %[vo], %[ro], s61 offen lds\n\ts_add_u32 m0, %[lb], 101376\n\ts_mov_b32 s61, 94208\n\tbuffer_load_dwordx4 %[vo], %[ro], s61 offen lds\n\ts_waitcnt vmcnt(28)\n\t" \
    "ds_read_b64_tr_b16 a[56:57], v16 offset:49152\n\tds_read_b64_tr_b16 a[120:121], v16 offset:49184\n\tds_read_b64_tr_b16 a[58:59], v16 offset:49408\n\tds_read_b64_tr_b16 a[122:123], v16 offset:49440\n\t" \
    "ds_read_b64_tr_b16 a[60:61], v16 offset:50176\n\tds_read_b64_tr_b16 a[124:125], v16 offset:50208\n\tds_read_b64_tr_b16 a[62:63], v16 offset:50432\n\tds_read_b64_tr_b16 a[126:127], v16 offset:50464\n\t" \
    "s_waitcnt lgkmcnt(0)\n\ts_add_u32 m0, %[lb], 114688\n\ts_mov_b32 s61, 114688\n\tbuffer_load_dwordx4 %[vo], %[ro], s61 offen lds\n\ts_add_u32 m0, %[lb], 115712\n\ts_mov_b32 s61, 118784\n\t" \
    "buffer_load_dwordx4 %[vo], %[ro], s61 offen lds\n\ts_add_u32 m0, %[lb], 116736\n\ts_mov_b32 s61, 122880\n\tbuffer_load_dwordx4 %[vo], %[ro], s61 offen lds\n\ts_add_u32 m0, %[lb], 117760\n\t" \
    "s_mov_b32 s61, 126976\n\tbuffer_load_dwordx4 %[vo], %[ro], s61 offen lds\n\ts_waitcnt vmcnt(28)\n\tds_read_b64_tr_b16 v[24:25], %[b0]\n\tds_read_b64_tr_b16 v[26:27], %[b1]\n\t" \
    "ds_read_b64_tr_b16 v[28:29], %[b2]\n\tds_read_b64_tr_b16 v[30:31], %[b3]\n\tds_read_b64_tr_b16 v[32:33], %[b0] offset:128\n\tds_read_b64_tr_b16 v[34:35], %[b1] offset:128\n\t" \
    "ds_read_b64_tr_b16 v[36:37], %[b2] offset:128\n\tds_read_b64_tr_b16 v[38:39], %[b3] offset:128\n\ts_waitcnt lgkmcnt(0)\n\tv_accvgpr_write_b32 a128, v24\n\tv_accvgpr_write_b32 a130, v25\n\t" \
    "v_accvgpr_write_b32 a129, v26\n\tv_accvgpr_write_b32 a131, v27\n\tv_accvgpr_write_b32 a160, v28\n\tv_accvgpr_write_b32 a162, v29\n\tv_accvgpr_write_b32 a161, v30\n\tv_accvgpr_write_b32 a163, v31\n\t" \
    "v_accvgpr_write_b32 a192, v32\n\tv_accvgpr_write_b32 a194, v33\n\tv_accvgpr_write_b32 a193, v34\n\tv_accvgpr_write_b32 a195, v35\n\tv_accvgpr_write_b32 a224, v36\n\tv_accvgpr_write_b32 a226, v37\n\t" \
    "v_accvgpr_write_b32 a225, v38\n\tv_accvgpr_write_b32 a227, v39\n\ts_waitcnt vmcnt(24)\n\tds_read_b64_tr_b16 v[24:25], %[b0] offset:16384\n\tds_read_b64_tr_b16 v[26:27], %[b1] offset:16384\n\t" \
    "ds_read_b64_tr_b16 v[28:29], %[b2] offset:16384\n\tds_read_b64_tr_b16 v[30:31], %[b3] offset:16384\n\tds_read_b64_tr_b16 v[32:33], %[b0] offset:16512\n\t" \
    "ds_read_b64_tr_b16 v[34:35], %[b1] offset:16512\n\tds_read_b64_tr_b16 v[36:37], %[b2] offset:16512\n\tds_read_b64_tr_b16 v[38:39], %[b3] offset:16512\n\ts_waitcnt lgkmcnt(0)\n\t" \
    "v_accvgpr_write_b32 a132, v24\n\tv_accvgpr_write_b32 a134, v25\n\tv_accvgpr_write_b32 a133, v26\n\tv_accvgpr_write_b32 a135, v27\n\tv_accvgpr_write_b32 a164, v28\n\tv_accvgpr_write_b32 a166, v29\n\t" \
    "v_accvgpr_write_b32 a165, v30\n\tv_accvgpr_write_b32 a167, v31\n\tv_accvgpr_write_b32 a196, v32\n\tv_accvgpr_write_b32 a198, v33\n\tv_accvgpr_write_b32 a197, v34\n\tv_accvgpr_write_b32 a199, v35\n\t" \
    "v_accvgpr_write_b32 a228, v36\n\tv_accvgpr_write_b32 a230, v37\n\tv_accvgpr_write_b32 a229, v38\n\tv_accvgpr_write_b32 a231, v39\n\ts_waitcnt vmcnt(20)\n\t" \
    "ds_read_b64_tr_b16 v[24:25], %[b0] offset:32768\n\tds_read_b64_tr_b16 v[26:27], %[b1] offset:32768\n\tds_read_b64_tr_b16 v[28:29], %[b2] offset:32768\n\t" \
    "ds_read_b64_tr_b16 v[30:31], %[b3] offset:32768\n\tds_read_b64_tr_b16 v[32:33], %[b0] offset:32896\n\tds_read_b64_tr_b16 v[34:35], %[b1] offset:32896\n\t" \
    "ds_read_b64_tr_b16 v[36:37], %[b2] offset:32896\n\tds_read_b64_tr_b16 v[38:39], %[b3] offset:32896\n\ts_waitcnt lgkmcnt(0)\n\tv_accvgpr_write_b32 a136, v24\n\tv_accvgpr_write_b32 a138, v25\n\t" \
    "v_accvgpr_write_b32 a137, v26\n\tv_accvgpr_write_b32 a139, v27\n\tv_accvgpr_write_b32 a168, v28\n\tv_accvgpr_write_b32 a170, v29\n\tv_accvgpr_write_b32 a169, v30\n\tv_accvgpr_write_b32 a171, v31\n\t" \
    "v_accvgpr_write_b32 a200, v32\n\tv_accvgpr_write_b32 a202, v33\n\tv_accvgpr_write_b32 a201, v34\n\tv_accvgpr_write_b32 a203, v35\n\tv_accvgpr_write_b32 a232, v36\n\tv_accvgpr_write_b32 a234, v37\n\t" \
    "v_accvgpr_write_b32 a233, v38\n\tv_accvgpr_write_b32 a235, v39\n\ts_waitcnt vmcnt(16)\n\tds_read_b64_tr_b16 v[24:25], %[b0] offset:49152\n\tds_read_b64_tr_b16 v[26:27], %[b1] offset:49152\n\t" \
    "ds_read_b64_tr_b16 v[28:29], %[b2] offset:49152\n\tds_read_b64_tr_b16 v[30:31], %[b3] offset:49152\n\tds_read_b64_tr_b16 v[32:33], %[b0] offset:49280\n\t" \
    "ds_read_b64_tr_b16 v[34:35], %[b1] offset:49280\n\tds_read_b64_tr_b16 v[36:37], %[b2] offset:49280\n\tds_read_b64_tr_b16 v[38:39], %[b3] offset:49280\n\ts_waitcnt lgkmcnt(0)\n\t" \
    "v_accvgpr_write_b32 a140, v24\n\tv_accvgpr_write_b32 a142, v25\n\tv_accvgpr_write_b32 a141, v26\n\tv_accvgpr_write_b32 a143, v27\n\tv_accvgpr_write_b32 a172, v28\n\tv_accvgpr_write_b32 a174, v29\n\t" \
    "v_accvgpr_write_b32 a173, v30\n\tv_accvgpr_write_b32 a175, v31\n\tv_accvgpr_write_b32 a204, v32\n\tv_accvgpr_write_b32 a206, v33\n\tv_accvgpr_write_b32 a205, v34\n\tv_accvgpr_write_b32 a207, v35\n\t" \
    "v_accvgpr_write_b32 a236, v36\n\tv_accvgpr_write_b32 a238, v37\n\tv_accvgpr_write_b32 a237, v38\n\tv_accvgpr_write_b32 a239, v39\n\ts_waitcnt vmcnt(12)\n\tds_read_b64_tr_b16 v[24:25], v17\n\t" \
    "ds_read_b64_tr_b16 v[26:27], v18\n\tds_read_b64_tr_b16 v[28:29], v19\n\tds_read_b64_tr_b16 v[30:31], v20\n\tds_read_b64_tr_b16 v[32:33], v17 offset:128\n\t" \
    "ds_read_b64_tr_b16 v[34:35], v18 offset:128\n\tds_read_b64_tr_b16 v[36:37], v19 offset:128\n\tds_read_b64_tr_b16 v[38:39], v20 offset:128\n\ts_waitcnt lgkmcnt(0)\n\tv_accvgpr_write_b32 a144, v24\n\t" \
    "v_accvgpr_write_b32 a146, v25\n\tv_accvgpr_write_b32 a145, v26\n\tv_accvgpr_write_b32 a147, v27\n\tv_accvgpr_write_b32 a176, v28\n\tv_accvgpr_write_b32 a178, v29\n\tv_accvgpr_write_b32 a177, v30\n\t" \
    "v_accvgpr_write_b32 a179, v31\n\tv_accvgpr_write_b32 a208, v32\n\tv_accvgpr_write_b32 a210, v33\n\tv_accvgpr_write_b32 a209, v34\n\tv_accvgpr_write_b32 a211, v35\n\tv_accvgpr_write_b32 a240, v36\n\t" \
    "v_accvgpr_write_b32 a242, v37\n\tv_accvgpr_write_b32 a241, v38\n\tv_accvgpr_write_b32 a243, v39\n\ts_waitcnt vmcnt(8)\n\tds_read_b64_tr_b16 v[24:25], v17 offset:16384\n\t" \
    "ds_read_b64_tr_b16 v[26:27], v18 offset:16384\n\tds_read_b64_tr_b16 v[28:29], v19 offset:16384\n\tds_read_b64_tr_b16 v[30:31], v20 offset:16384\n\tds_read_b64_tr_b16 v[32:33], v17 offset:16512\n\t" \
    "ds_read_b64_tr_b16 v[34:35], v18 offset:16512\n\tds_read_b64_tr_b16 v[36:37], v19 offset:16512\n\tds_read_b64_tr_b16 v[38:39], v20 offset:16512\n\ts_waitcnt lgkmcnt(0)\n\t" \
    "v_accvgpr_write_b32 a148, v24\n\tv_accvgpr_write_b32 a150, v25\n\tv_accvgpr_write_b32 a149, v26\n\tv_accvgpr_write_b32 a151, v27\n\tv_accvgpr_write_b32 a180, v28\n\tv_accvgpr_write_b32 a182, v29\n\t" \
    "v_accvgpr_write_b32 a181, v30\n\tv_accvgpr_write_b32 a183, v31\n\tv_accvgpr_write_b32 a212, v32\n\tv_accvgpr_write_b32 a214, v33\n\tv_accvgpr_write_b32 a213, v34\n\tv_accvgpr_write_b32 a215, v35\n\t" \
    "v_accvgpr_write_b32 a244, v36\n\tv_accvgpr_write_b32 a246, v37\n\tv_accvgpr_write_b32 a245, v38\n\tv_accvgpr_write_b32 a247, v39\n\ts_waitcnt vmcnt(4)\n\t" \
    "ds_read_b64_tr_b16 v[24:25], v17 offset:32768\n\tds_read_b64_tr_b16 v[26:27], v18 offset:32768\n\tds_read_b64_tr_b16 v[28:29], v19 offset:32768\n\tds_read_b64_tr_b16 v[30:31], v20 offset:32768\n\t" \
    "ds_read_b64_tr_b16 v[32:33], v17 offset:32896\n\tds_read_b64_tr_b16 v[34:35], v18 offset:32896\n\tds_read_b64_tr_b16 v[36:37], v19 offset:32896\n\tds_read_b64_tr_b16 v[38:39], v20 offset:32896\n\t" \
    "s_waitcnt lgkmcnt(0)\n\tv_accvgpr_write_b32 a152, v24\n\tv_accvgpr_write_b32 a154, v25\n\tv_accvgpr_write_b32 a153, v26\n\tv_accvgpr_write_b32 a155, v27\n\tv_accvgpr_write_b32 a184, v28\n\t" \
    "v_accvgpr_write_b32 a186, v29\n\tv_accvgpr_write_b32 a185, v30\n\tv_accvgpr_write_b32 a187, v31\n\tv_accvgpr_write_b32 a216, v32\n\tv_accvgpr_write_b32 a218, v33\n\tv_accvgpr_write_b32 a217, v34\n\t" \
    "v_accvgpr_write_b32 a219, v35\n\tv_accvgpr_write_b32 a248, v36\n\tv_accvgpr_write_b32 a250, v37\n\tv_accvgpr_write_b32 a249, v38\n\tv_accvgpr_write_b32 a251, v39\n\ts_waitcnt vmcnt(0)\n\t" \
    "ds_read_b64_tr_b16 v[24:25], v17 offset:49152\n\tds_read_b64_tr_b16 v[26:27], v18 offset:49152\n\tds_read_b64_tr_b16 v[28:29], v19 offset:49152\n\tds_read_b64_tr_b16 v[30:31], v20 offset:49152\n\t" \
    "ds_read_b64_tr_b16 v[32:33], v17 offset:49280\n\tds_read_b64_tr_b16 v[34:35], v18 offset:49280\n\tds_read_b64_tr_b16 v[36:37], v19 offset:49280\n\tds_read_b64_tr_b16 v[38:39], v20 offset:49280\n\t" \
    "s_waitcnt lgkmcnt(0)\n\tv_accvgpr_write_b32 a156, v24\n\tv_accvgpr_write_b32 a158, v25\n\tv_accvgpr_write_b32 a157, v26\n\tv_accvgpr_write_b32 a159, v27\n\tv_accvgpr_write_b32 a188, v28\n\t" \
    "v_accvgpr_write_b32 a190, v29\n\tv_accvgpr_write_b32 a189, v30\n\tv_accvgpr_write_b32 a191, v31\n\tv_accvgpr_write_b32 a220, v32\n\tv_accvgpr_write_b32 a222, v33\n\tv_accvgpr_write_b32 a221, v34\n\t" \
    "v_accvgpr_write_b32 a223, v35\n\tv_accvgpr_write_b32 a252, v36\n\tv_accvgpr_write_b32 a254, v37\n\tv_accvgpr_write_b32 a253, v38\n\tv_accvgpr_write_b32 a255, v39\n\ts_mov_b32 m0, s60\n\t"
#define ASM_HST0 \
    "s_mov_b32 s60, m0\n\ts_mov_b32 %[cnt], 0\n\ts_sub_u32 s62, 63, %[s]\n\ts_cmp_eq_u32 %[s], 0\n\ts_cbranch_scc1 13f\n\t20:\ns_memrealtime s[70:71]\n\ts_waitcnt lgkmcnt(0)\n\ts_lshl_b32 s70, s70, 4\n\t" \
    "s_sub_u32 s70, %[due], s70\n\ts_sub_u32 s70, s70, 512\n\ts_cmp_lt_u32 s70, 0x4000\n\ts_cbranch_scc0 21f\n\ts_sleep 1\n\ts_branch 20b\n\t21:\ns_memtime s[70:71]\n\ts_memrealtime s[72:73]\n\ts_nop 2\n\t" \
    "s_memrealtime s[74:75]\n\ts_nop 2\n\ts_memrealtime s[76:77]\n\ts_nop 2\n\ts_memrealtime s[78:79]\n\ts_nop 2\n\ts_memrealtime s[80:81]\n\ts_nop 2\n\ts_memrealtime s[82:83]\n\ts_nop 2\n\t" \
    "s_memrealtime s[84:85]\n\ts_waitcnt lgkmcnt(0)\n\ts_add_u32 s71, s72, 4\n\ts_cmp_eq_u32 s84, s72\n\ts_cselect_b32 s71, s71, s84\n\ts_cselect_b32 s73, 16, 0\n\ts_add_u32 s70, s70, s73\n\t" \
    "s_cmp_eq_u32 s82, s72\n\ts_cselect_b32 s71, s71, s82\n\ts_cselect_b32 s73, 16, 0\n\ts_add_u32 s70, s70, s73\n\ts_cmp_eq_u32 s80, s72\n\ts_cselect_b32 s71, s71, s80\n\ts_cselect_b32 s73, 16, 0\n\t" \
    "s_add_u32 s70, s70, s73\n\ts_cmp_eq_u32 s78, s72\n\ts_cselect_b32 s71, s71, s78\n\ts_cselect_b32 s73, 16, 0\n\ts_add_u32 s70, s70, s73\n\ts_cmp_eq_u32 s76, s72\n\ts_cselect_b32 s71, s71, s76\n\t" \
    "s_cselect_b32 s73, 16, 0\n\ts_add_u32 s70, s70, s73\n\ts_cmp_eq_u32 s74, s72\n\ts_cselect_b32 s71, s71, s74\n\ts_cselect_b32 s73, 16, 0\n\ts_add_u32 s70, s70, s73\n\ts_sub_u32 s72, s70, %[pc]\n\t" \
    "s_sub_u32 s73, s71, %[pr]\n\ts_mov_b32 %[pc], s70\n\ts_mov_b32 %[pr], s71\n\ts_lshl_b32 s72, s72, 4\n\ts_mul_i32 s74, s73, %[f]\n\ts_sub_u32 s72, s72, s74\n\ts_ashr_i32 s72, s72, 8\n\t" \
    "s_cmp_lt_u32 s73, 320\n\ts_cselect_b32 s72, s72, 0\n\ts_add_u32 %[f], %[f], s72\n\ts_max_i32 %[f], %[f], 240\n\ts_min_i32 %[f], %[f], 400\n\ts_lshl_b32 s71, s71, 4\n\ts_sub_u32 s72, %[due], s71\n\t" \
    "s_mul_i32 s72, s72, %[f]\n\ts_ashr_i32 s72, s72, 8\n\ts_add_u32 s72, s72, s70\n\ts_memtime s[74:75]\n\ts_getpc_b64 s[76:77]\n\t22:\ns_add_u32 s76, s76, 23f-22b\n\ts_addc_u32 s77, s77, 0\n\t" \
    "s_sub_u32 s72, s72, 120\n\ts_waitcnt lgkmcnt(0)\n\ts_sub_u32 s72, s72, s74\n\ts_max_i32 s72, s72, 0\n\ts_min_i32 s72, s72, 640\n\ts_and_b32 s72, s72, -4\n\ts_sub_u32 s76, s76, s72\n\t" \
    "s_subb_u32 s77, s77, 0\n\ts_setpc_b64 s[76:77]\n\t.rept 160\n\ts_nop 0\n\t.endr\n\t23:\ns_mov_b32 m0, %[lm]\n\ts_add_u32 s61, %[fb], 0x1000\n\tbuffer_load_dwordx4 %[dv], %[rs], %[fb] offen sc1 lds\n\t" \
    "buffer_load_dwordx4 %[dv], %[rs], %[fb] offen offset:1024 sc1 lds\n\tbuffer_load_dwordx4 %[dv], %[rs], %[fb] offen offset:2048 sc1 lds\n\t" \
    "buffer_load_dwordx4 %[dv], %[rs], %[fb] offen offset:3072 sc1 lds\n\ts_add_u32 m0, m0, 0x1000\n\ts_nop 0\n\tbuffer_load_dwordx4 %[dv], %[rs], s61 offen sc1 lds\n\t" \
    "buffer_load_dwordx4 %[dv], %[rs], s61 offen offset:1024 sc1 lds\n\tbuffer_load_dwordx4 %[dv], %[rs], s61 offen offset:2048 sc1 lds\n\t" \
    "buffer_load_dwordx4 %[dv], %[rs], s61 offen offset:3072 sc1 lds\n\t13:\ns_cmp_lg_u32 s62, 0\n\ts_cbranch_scc1 14f\n\ts_waitcnt vmcnt(0)\n\ts_branch 1f\n\t14:\ns_mov_b32 m0, %[lk]\n\t" \
    "s_add_u32 s61, %[ko], 0x1000000\n\tbuffer_load_dwordx4 %[dv], %[rk], s61 offen sc1 lds\n\tbuffer_load_dwordx4 %[dv], %[rk], s61 offen offset:1024 sc1 lds\n\t" \
    "buffer_load_dwordx4 %[dv], %[rk], s61 offen offset:2048 sc1 lds\n\tbuffer_load_dwordx4 %[dv], %[rk], s61 offen offset:3072 sc1 lds\n\ts_add_u32 m0, m0, 0x1000\n\ts_add_u32 s61, s61, 0x1000\n\t" \
    "buffer_load_dwordx4 %[dv], %[rk], s61 offen sc1 lds\n\tbuffer_load_dwordx4 %[dv], %[rk], s61 offen offset:1024 sc1 lds\n\tbuffer_load_dwordx4 %[dv], %[rk], s61 offen offset:2048 sc1 lds\n\t" \
    "buffer_load_dwordx4 %[dv], %[rk], s61 offen offset:3072 sc1 lds\n\ts_add_u32 m0, m0, 0x1000\n\ts_mov_b32 s61, %[ko]\n\tbuffer_load_dwordx4 %[dv], %[rk], s61 offen sc1 lds\n\t" \
    "buffer_load_dwordx4 %[dv], %[rk], s61 offen offset:1024 sc1 lds\n\tbuffer_load_dwordx4 %[dv], %[rk], s61 offen offset:2048 sc1 lds\n\t" \
    "buffer_load_dwordx4 %[dv], %[rk], s61 offen offset:3072 sc1 lds\n\ts_add_u32 m0, m0, 0x1000\n\ts_add_u32 s61, s61, 0x1000\n\tbuffer_load_dwordx4 %[dv], %[rk], s61 offen sc1 lds\n\t" \
    "buffer_load_dwordx4 %[dv], %[rk], s61 offen offset:1024 sc1 lds\n\tbuffer_load_dwordx4 %[dv], %[rk], s61 offen offset:2048 sc1 lds\n\t" \
    "buffer_load_dwordx4 %[dv], %[rk], s61 offen offset:3072 sc1 lds\n\t1:\nv_mov_b64 v[48:49], 0\n\tv_mov_b64 v[50:51], 0\n\tv_mov_b64 v[52:53], 0\n\tv_mov_b64 v[54:55], 0\n\tv_mov_b64 v[56:57], 0\n\t" \
    "v_mov_b64 v[58:59], 0\n\tv_mov_b64 v[60:61], 0\n\tv_mov_b64 v[62:63], 0\n\ts_add_u32 %[tg], %[tg], 1\n\tv_mov_b32 v198, %[tg]\n\ts_waitcnt vmcnt(16) lgkmcnt(0)\n\ts_barrier\n\t" \
    "ds_read_b64_tr_b16 v[16:17], %[ma]\n\tds_read_b64_tr_b16 v[18:19], %[ma] offset:512\n\tds_read_b64_tr_b16 v[20:21], %[ma] offset:1024\n\tds_read_b64_tr_b16 v[22:23], %[ma] offset:1536\n\t" \
    "ds_read_b64_tr_b16 v[24:25], %[ma] offset:2048\n\tds_read_b64_tr_b16 v[26:27], %[ma] offset:2560\n\tds_read_b64_tr_b16 v[28:29], %[ma] offset:3072\n\tds_read_b64_tr_b16 v[30:31], %[ma] offset:3584\n\t" \
    "s_waitcnt lgkmcnt(6)\n\tv_smfmac_f32_16x16x64_bf16 v[48:51], v[16:19], a[0:7], %[ix]\n\ts_waitcnt lgkmcnt(4)\n\tds_read_b64_tr_b16 v[32:33], %[ma] offset:4096\n\t" \
    "ds_read_b64_tr_b16 v[34:35], %[ma] offset:4608\n\tv_smfmac_f32_16x16x64_bf16 v[48:51], v[20:23], a[8:15], %[ix]\n\ts_waitcnt lgkmcnt(4)\n\tds_read_b64_tr_b16 v[36:37], %[ma] offset:5120\n\t" \
    "ds_read_b64_tr_b16 v[38:39], %[ma] offset:5632\n\tv_smfmac_f32_16x16x64_bf16 v[48:51], v[24:27], a[16:23], %[ix]\n\ts_waitcnt lgkmcnt(4)\n\tds_read_b64_tr_b16 v[40:41], %[ma] offset:6144\n\t" \
    "ds_read_b64_tr_b16 v[42:43], %[ma] offset:6656\n\tv_smfmac_f32_16x16x64_bf16 v[48:51], v[28:31], a[24:31], %[ix]\n\ts_waitcnt lgkmcnt(4)\n\tds_read_b64_tr_b16 v[44:45], %[ma] offset:7168\n\t" \
    "ds_read_b64_tr_b16 v[46:47], %[ma] offset:7680\n\tv_smfmac_f32_16x16x64_bf16 v[48:51], v[32:35], a[32:39], %[ix]\n\ts_waitcnt lgkmcnt(4)\n\t" \
    "v_smfmac_f32_16x16x64_bf16 v[48:51], v[36:39], a[40:47], %[ix]\n\ts_waitcnt lgkmcnt(2)\n\tv_smfmac_f32_16x16x64_bf16 v[48:51], v[40:43], a[48:55], %[ix]\n\ts_waitcnt lgkmcnt(0)\n\t" \
    "v_smfmac_f32_16x16x64_bf16 v[48:51], v[44:47], a[56:63], %[ix]\n\tv_smfmac_f32_16x16x64_bf16 v[52:55], v[16:19], a[64:71], %[ix]\n\tv_smfmac_f32_16x16x64_bf16 v[52:55], v[20:23], a[72:79], %[ix]\n\t" \
    "v_add_f32 v48, v48, v50\n\tv_smfmac_f32_16x16x64_bf16 v[52:55], v[24:27], a[80:87], %[ix]\n\tv_add_f32 v49, v49, v51\n\tv_smfmac_f32_16x16x64_bf16 v[52:55], v[28:31], a[88:95], %[ix]\n\t" \
    "v_mul_f32 v48, %[c2], v48\n\tv_smfmac_f32_16x16x64_bf16 v[52:55], v[32:35], a[96:103], %[ix]\n\tv_mul_f32 v49, %[c2], v49\n\tv_smfmac_f32_16x16x64_bf16 v[52:55], v[36:39], a[104:111], %[ix]\n\t" \
    "v_cvt_pk_bf16_f32 v50, v48, v49\n\tv_smfmac_f32_16x16x64_bf16 v[52:55], v[40:43], a[112:119], %[ix]\n\tds_write_b32 %[qw], v50\n\tv_smfmac_f32_16x16x64_bf16 v[52:55], v[44:47], a[120:127], %[ix]\n\t" \
    "v_cmp_u_f32 vcc, v48, v49\n\tv_smfmac_f32_16x16x64_bf16 v[56:59], v[16:19], a[128:135], %[ix]\n\tv_smfmac_f32_16x16x64_bf16 v[56:59], v[20:23], a[136:143], %[ix]\n\tv_add_f32 v52, v52, v54\n\t" \
    "v_smfmac_f32_16x16x64_bf16 v[56:59], v[24:27], a[144:151], %[ix]\n\ts_cbranch_vccnz 3f\n\t2:\nv_add_f32 v53, v53, v55\n\tv_smfmac_f32_16x16x64_bf16 v[56:59], v[28:31], a[152:159], %[ix]\n\t" \
    "v_mul_f32 v52, %[c2], v52\n\tv_smfmac_f32_16x16x64_bf16 v[56:59], v[32:35], a[160:167], %[ix]\n\tv_mul_f32 v53, %[c2], v53\n\tv_smfmac_f32_16x16x64_bf16 v[56:59], v[36:39], a[168:175], %[ix]\n\t" \
    "v_cvt_pk_bf16_f32 v54, v52, v53\n\tv_smfmac_f32_16x16x64_bf16 v[56:59], v[40:43], a[176:183], %[ix]\n\tds_write_b32 %[qw], v54 offset:256\n\tds_write_b32 %[qw], v198 offset:4096\n\t" \
    "v_smfmac_f32_16x16x64_bf16 v[56:59], v[44:47], a[184:191], %[ix]\n\ts_barrier\n\tv_smfmac_f32_16x16x64_bf16 v[60:63], v[16:19], a[192:199], %[ix]\n\t" \
    "v_smfmac_f32_16x16x64_bf16 v[60:63], v[20:23], a[200:207], %[ix]\n\tv_add_f32 v56, v56, v58\n\tv_smfmac_f32_16x16x64_bf16 v[60:63], v[24:27], a[208:215], %[ix]\n\tv_add_f32 v57, v57, v59\n\t" \
    "v_smfmac_f32_16x16x64_bf16 v[60:63], v[28:31], a[216:223], %[ix]\n\tv_cvt_pk_bf16_f32 v58, v56, v57\n\tv_smfmac_f32_16x16x64_bf16 v[60:63], v[32:35], a[224:231], %[ix]\n\tds_write_b32 %[kw], v58\n\t" \
    "v_smfmac_f32_16x16x64_bf16 v[60:63], v[36:39], a[232:239], %[ix]\n\tv_smfmac_f32_16x16x64_bf16 v[60:63], v[40:43], a[240:247], %[ix]\n\t" \
    "v_smfmac_f32_16x16x64_bf16 v[60:63], v[44:47], a[248:255], %[ix]\n\ts_nop 7\n\tv_add_f32 v60, v60, v62\n\tv_add_f32 v61, v61, v63\n\tv_cvt_pk_bf16_f32 v62, v60, v61\n\t" \
    "ds_write_b32 %[kw], v62 offset:256\n\tds_write_b32 %[kw], v198 offset:4096\n\ts_barrier\n\ts_mov_b32 m0, s60\n\ts_branch 9f\n\t3:\ns_waitcnt lgkmcnt(0)\n\tds_read_b128 v[160:163], %[ml]\n\t" \
    "ds_read_b128 v[164:167], %[ml] offset:1024\n\tds_read_b128 v[168:171], %[ml] offset:2048\n\tds_read_b128 v[172:175], %[ml] offset:3072\n\tds_read_b128 v[176:179], %[ml] offset:4096\n\t" \
    "ds_read_b128 v[180:183], %[ml] offset:5120\n\tds_read_b128 v[184:187], %[ml] offset:6144\n\tds_read_b128 v[188:191], %[ml] offset:7168\n\ts_waitcnt lgkmcnt(0)\n\tv_max3_u32 v160, v160, v161, v162\n\t" \
    "v_max3_u32 v160, v160, v163, v164\n\tv_max3_u32 v160, v160, v165, v166\n\tv_max3_u32 v160, v160, v167, v168\n\tv_max3_u32 v160, v160, v169, v170\n\tv_max3_u32 v160, v160, v171, v172\n\t" \
    "v_max3_u32 v160, v160, v173, v174\n\tv_max3_u32 v160, v160, v175, v176\n\tv_max3_u32 v160, v160, v177, v178\n\tv_max3_u32 v160, v160, v179, v180\n\tv_max3_u32 v160, v160, v181, v182\n\t" \
    "v_max3_u32 v160, v160, v183, v184\n\tv_max3_u32 v160, v160, v185, v186\n\tv_max3_u32 v160, v160, v187, v188\n\tv_max3_u32 v160, v160, v189, v190\n\tv_max_u32 v160, v160, v191\n\t" \
    "v_cmp_eq_u32 vcc, -1, v160\n\ts_cmp_eq_u32 %[s], 0\n\ts_cbranch_scc1 2b\n\ts_cbranch_vccz 2b\n\ts_add_u32 %[cnt], %[cnt], 1\n\ts_cmp_gt_u32 %[cnt], %[cap]\n\ts_cselect_b32 %[cap], 0, %[cap]\n\t" \
    "s_cbranch_scc1 2b\n\ts_barrier\n\ts_mov_b32 s63, 0\n\t4:\ns_mov_b32 m0, %[lm]\n\ts_add_u32 s61, %[fb], 0x1000\n\tbuffer_load_dwordx4 %[dv], %[rs], %[fb] offen sc1 lds\n\t" \
    "buffer_load_dwordx4 %[dv], %[rs], %[fb] offen offset:1024 sc1 lds\n\tbuffer_load_dwordx4 %[dv], %[rs], %[fb] offen offset:2048 sc1 lds\n\t" \
    "buffer_load_dwordx4 %[dv], %[rs], %[fb] offen offset:3072 sc1 lds\n\ts_add_u32 m0, m0, 0x1000\n\ts_nop 0\n\tbuffer_load_dwordx4 %[dv], %[rs], s61 offen sc1 lds\n\t" \
    "buffer_load_dwordx4 %[dv], %[rs], s61 offen offset:1024 sc1 lds\n\tbuffer_load_dwordx4 %[dv], %[rs], s61 offen offset:2048 sc1 lds\n\t" \
    "buffer_load_dwordx4 %[dv], %[rs], s61 offen offset:3072 sc1 lds\n\ts_waitcnt vmcnt(0)\n\ts_waitcnt lgkmcnt(0)\n\tds_read_b128 v[160:163], %[ml]\n\tds_read_b128 v[164:167], %[ml] offset:1024\n\t" \
    "ds_read_b128 v[168:171], %[ml] offset:2048\n\tds_read_b128 v[172:175], %[ml] offset:3072\n\tds_read_b128 v[176:179], %[ml] offset:4096\n\tds_read_b128 v[180:183], %[ml] offset:5120\n\t" \
    "ds_read_b128 v[184:187], %[ml] offset:6144\n\tds_read_b128 v[188:191], %[ml] offset:7168\n\ts_waitcnt lgkmcnt(0)\n\tv_max3_u32 v160, v160, v161, v162\n\tv_max3_u32 v160, v160, v163, v164\n\t" \
    "v_max3_u32 v160, v160, v165, v166\n\tv_max3_u32 v160, v160, v167, v168\n\tv_max3_u32 v160, v160, v169, v170\n\tv_max3_u32 v160, v160, v171, v172\n\tv_max3_u32 v160, v160, v173, v174\n\t" \
    "v_max3_u32 v160, v160, v175, v176\n\tv_max3_u32 v160, v160, v177, v178\n\tv_max3_u32 v160, v160, v179, v180\n\tv_max3_u32 v160, v160, v181, v182\n\tv_max3_u32 v160, v160, v183, v184\n\t" \
    "v_max3_u32 v160, v160, v185, v186\n\tv_max3_u32 v160, v160, v187, v188\n\tv_max3_u32 v160, v160, v189, v190\n\tv_max_u32 v160, v160, v191\n\tv_cmp_eq_u32 vcc, -1, v160\n\ts_add_u32 s63, s63, 1\n\t" \
    "s_cmp_lt_u32 s63, 64\n\ts_cbranch_scc0 1b\n\ts_cbranch_vccnz 4b\n\ts_branch 1b\n\t9:\n"
#define ASM_HST1 \
    "s_mov_b32 s60, m0\n\ts_mov_b32 %[cnt], 0\n\t1:\nv_mov_b64 v[48:49], 0\n\tv_mov_b64 v[50:51], 0\n\tv_mov_b64 v[52:53], 0\n\tv_mov_b64 v[54:55], 0\n\tv_mov_b64 v[56:57], 0\n\tv_mov_b64 v[58:59], 0\n\t" \
    "v_mov_b64 v[60:61], 0\n\tv_mov_b64 v[62:63], 0\n\ts_add_u32 %[tg], %[tg], 1\n\tv_mov_b32 v198, %[tg]\n\ts_waitcnt vmcnt(0) lgkmcnt(0)\n\ts_barrier\n\tds_read_b64_tr_b16 v[16:17], %[ma]\n\t" \
    "ds_read_b64_tr_b16 v[18:19], %[ma] offset:512\n\tds_read_b64_tr_b16 v[20:21], %[ma] offset:1024\n\tds_read_b64_tr_b16 v[22:23], %[ma] offset:1536\n\tds_read_b64_tr_b16 v[24:25], %[ma] offset:2048\n\t" \
    "ds_read_b64_tr_b16 v[26:27], %[ma] offset:2560\n\tds_read_b64_tr_b16 v[28:29], %[ma] offset:3072\n\tds_read_b64_tr_b16 v[30:31], %[ma] offset:3584\n\ts_waitcnt lgkmcnt(6)\n\t" \
    "v_smfmac_f32_16x16x64_bf16 v[48:51], v[16:19], a[0:7], %[ix]\n\ts_waitcnt lgkmcnt(4)\n\tds_read_b64_tr_b16 v[32:33], %[ma] offset:4096\n\tds_read_b64_tr_b16 v[34:35], %[ma] offset:4608\n\t" \
    "v_smfmac_f32_16x16x64_bf16 v[48:51], v[20:23], a[8:15], %[ix]\n\ts_waitcnt lgkmcnt(4)\n\tds_read_b64_tr_b16 v[36:37], %[ma] offset:5120\n\tds_read_b64_tr_b16 v[38:39], %[ma] offset:5632\n\t" \
    "v_smfmac_f32_16x16x64_bf16 v[48:51], v[24:27], a[16:23], %[ix]\n\ts_waitcnt lgkmcnt(4)\n\tds_read_b64_tr_b16 v[40:41], %[ma] offset:6144\n\tds_read_b64_tr_b16 v[42:43], %[ma] offset:6656\n\t" \
    "v_smfmac_f32_16x16x64_bf16 v[48:51], v[28:31], a[24:31], %[ix]\n\ts_waitcnt lgkmcnt(4)\n\tds_read_b64_tr_b16 v[44:45], %[ma] offset:7168\n\tds_read_b64_tr_b16 v[46:47], %[ma] offset:7680\n\t" \
    "v_smfmac_f32_16x16x64_bf16 v[48:51], v[32:35], a[32:39], %[ix]\n\ts_waitcnt lgkmcnt(4)\n\tv_smfmac_f32_16x16x64_bf16 v[48:51], v[36:39], a[40:47], %[ix]\n\ts_waitcnt lgkmcnt(2)\n\t" \
    "v_smfmac_f32_16x16x64_bf16 v[48:51], v[40:43], a[48:55], %[ix]\n\ts_waitcnt lgkmcnt(0)\n\tds_read_b128 v[64:67], %[kx]\n\tds_read_b128 v[68:71], %[kx] offset:64\n\t" \
    "v_smfmac_f32_16x16x64_bf16 v[48:51], v[44:47], a[56:63], %[ix]\n\tds_read_b128 v[72:75], %[kx] offset:2048\n\tds_read_b128 v[76:79], %[kx] offset:2112\n\t" \
    "v_smfmac_f32_16x16x64_bf16 v[52:55], v[16:19], a[64:71], %[ix]\n\tds_read_b128 v[80:83], %[kx] offset:4096\n\tds_read_b128 v[84:87], %[kx] offset:4160\n\t" \
    "v_smfmac_f32_16x16x64_bf16 v[52:55], v[20:23], a[72:79], %[ix]\n\tv_add_f32 v48, v48, v50\n\tv_smfmac_f32_16x16x64_bf16 v[52:55], v[24:27], a[80:87], %[ix]\n\tv_add_f32 v49, v49, v51\n\t" \
    "v_smfmac_f32_16x16x64_bf16 v[52:55], v[28:31], a[88:95], %[ix]\n\tv_mul_f32 v48, %[c2], v48\n\tv_smfmac_f32_16x16x64_bf16 v[52:55], v[32:35], a[96:103], %[ix]\n\tv_mul_f32 v49, %[c2], v49\n\t" \
    "v_smfmac_f32_16x16x64_bf16 v[52:55], v[36:39], a[104:111], %[ix]\n\tv_cvt_pk_bf16_f32 v50, v48, v49\n\tv_smfmac_f32_16x16x64_bf16 v[52:55], v[40:43], a[112:119], %[ix]\n\tds_write_b32 %[qw], v50\n\t" \
    "v_smfmac_f32_16x16x64_bf16 v[52:55], v[44:47], a[120:127], %[ix]\n\tv_cmp_u_f32 vcc, v48, v49\n\tv_smfmac_f32_16x16x64_bf16 v[56:59], v[16:19], a[128:135], %[ix]\n\t" \
    "ds_read_b128 v[88:91], %[kx] offset:6144\n\tds_read_b128 v[92:95], %[kx] offset:6208\n\tv_smfmac_f32_16x16x64_bf16 v[56:59], v[20:23], a[136:143], %[ix]\n\tv_add_f32 v52, v52, v54\n\t" \
    "v_smfmac_f32_16x16x64_bf16 v[56:59], v[24:27], a[144:151], %[ix]\n\ts_cbranch_vccnz 3f\n\t2:\nv_add_f32 v53, v53, v55\n\tv_smfmac_f32_16x16x64_bf16 v[56:59], v[28:31], a[152:159], %[ix]\n\t" \
    "v_mul_f32 v52, %[c2], v52\n\tv_smfmac_f32_16x16x64_bf16 v[56:59], v[32:35], a[160:167], %[ix]\n\tv_mul_f32 v53, %[c2], v53\n\tv_smfmac_f32_16x16x64_bf16 v[56:59], v[36:39], a[168:175], %[ix]\n\t" \
    "s_waitcnt lgkmcnt(0)\n\tv_cvt_pk_bf16_f32 v54, v52, v53\n\tv_smfmac_f32_16x16x64_bf16 v[56:59], v[40:43], a[176:183], %[ix]\n\tds_write_b32 %[qw], v54 offset:256\n\t" \
    "ds_write_b32 %[qw], v198 offset:4096\n\tv_smfmac_f32_16x16x64_bf16 v[56:59], v[44:47], a[184:191], %[ix]\n\ts_barrier\n\tds_read_b32 v140, %[ta]\n\tds_read_b64_tr_b16 v[96:97], %[fr]\n\t" \
    "v_smfmac_f32_16x16x64_bf16 v[60:63], v[16:19], a[192:199], %[ix]\n\tds_read_b64_tr_b16 v[98:99], %[fr] offset:64\n\tds_read_b64_tr_b16 v[100:101], %[fr] offset:512\n\t" \
    "v_smfmac_f32_16x16x64_bf16 v[60:63], v[20:23], a[200:207], %[ix]\n\tv_add_f32 v56, v56, v58\n\tv_smfmac_f32_16x16x64_bf16 v[60:63], v[24:27], a[208:215], %[ix]\n\tv_add_f32 v57, v57, v59\n\t" \
    "v_smfmac_f32_16x16x64_bf16 v[60:63], v[28:31], a[216:223], %[ix]\n\tv_cvt_pk_bf16_f32 v58, v56, v57\n\tv_smfmac_f32_16x16x64_bf16 v[60:63], v[32:35], a[224:231], %[ix]\n\t" \
    "ds_read_b64_tr_b16 v[102:103], %[fr] offset:576\n\tv_smfmac_f32_16x16x64_bf16 v[60:63], v[36:39], a[232:239], %[ix]\n\tds_read_b64_tr_b16 v[104:105], %[fr] offset:1024\n\t" \
    "ds_read_b64_tr_b16 v[106:107], %[fr] offset:1088\n\tv_smfmac_f32_16x16x64_bf16 v[60:63], v[40:43], a[240:247], %[ix]\n\tds_read_b64_tr_b16 v[108:109], %[fr] offset:1536\n\t" \
    "ds_read_b64_tr_b16 v[110:111], %[fr] offset:1600\n\tv_smfmac_f32_16x16x64_bf16 v[60:63], v[44:47], a[248:255], %[ix]\n\tds_write_b32 %[kw], v58\n\ts_waitcnt lgkmcnt(7)\n\t" \
    "v_mfma_f32_16x16x32_bf16 v[128:131], v[64:67], v[96:99], 0\n\tv_mfma_f32_16x16x32_bf16 v[132:135], v[68:71], v[96:99], 0\n\ts_waitcnt lgkmcnt(5)\n\tv_add_f32 v60, v60, v62\n\t" \
    "v_add_f32 v61, v61, v63\n\tv_cvt_pk_bf16_f32 v62, v60, v61\n\tv_mfma_f32_16x16x32_bf16 v[128:131], v[72:75], v[100:103], v[128:131]\n\tds_write_b32 %[kw], v62 offset:256\n\t" \
    "ds_write_b32 %[kw], v198 offset:4096\n\tv_mfma_f32_16x16x32_bf16 v[132:135], v[76:79], v[100:103], v[132:135]\n\ts_waitcnt lgkmcnt(3)\n\ts_barrier\n\tds_read_b32 v141, %[ta] offset:2048\n\t" \
    "ds_read_b64_tr_b16 v[112:113], %[fr] offset:2048\n\tds_read_b64_tr_b16 v[114:115], %[fr] offset:2112\n\tv_mfma_f32_16x16x32_bf16 v[128:131], v[80:83], v[104:107], v[128:131]\n\t" \
    "ds_read_b64_tr_b16 v[116:117], %[fr] offset:2560\n\tds_read_b64_tr_b16 v[118:119], %[fr] offset:2624\n\tv_mfma_f32_16x16x32_bf16 v[132:135], v[84:87], v[104:107], v[132:135]\n\t" \
    "ds_read_b64_tr_b16 v[120:121], %[fr] offset:3072\n\tds_read_b64_tr_b16 v[122:123], %[fr] offset:3136\n\tv_mfma_f32_16x16x32_bf16 v[128:131], v[88:91], v[108:111], v[128:131]\n\t" \
    "ds_read_b64_tr_b16 v[124:125], %[fr] offset:3584\n\tds_read_b64_tr_b16 v[126:127], %[fr] offset:3648\n\tv_mfma_f32_16x16x32_bf16 v[132:135], v[92:95], v[108:111], v[132:135]\n\t" \
    "s_waitcnt lgkmcnt(6)\n\t12:\nv_mfma_f32_16x16x32_bf16 v[136:139], v[112:115], v[96:99], 0\n\ts_waitcnt lgkmcnt(4)\n\tv_min_u32 v140, v140, v141\n\tv_cmp_ne_u32 vcc, v140, v198\n\t" \
    "v_mfma_f32_16x16x32_bf16 v[136:139], v[116:119], v[100:103], v[136:139]\n\ts_waitcnt lgkmcnt(2)\n\tv_mov_b32_dpp v128, v132 quad_perm:[0,1,2,3] row_mask:0xf bank_mask:0xc\n\t" \
    "v_mov_b32_dpp v129, v133 quad_perm:[0,1,2,3] row_mask:0xf bank_mask:0xc\n\tv_mfma_f32_16x16x32_bf16 v[136:139], v[120:123], v[104:107], v[136:139]\n\ts_waitcnt lgkmcnt(0)\n\t" \
    "v_mov_b32_dpp v130, v134 quad_perm:[0,1,2,3] row_mask:0xf bank_mask:0xc\n\tv_mov_b32_dpp v131, v135 quad_perm:[0,1,2,3] row_mask:0xf bank_mask:0xc\n\t" \
    "v_mfma_f32_16x16x32_bf16 v[136:139], v[124:127], v[108:111], v[136:139]\n\ts_cmp_eq_u64 vcc, 0\n\tv_exp_f32 v128, v128\n\tv_exp_f32 v129, v129\n\tv_exp_f32 v130, v130\n\tv_exp_f32 v131, v131\n\t" \
    "s_cselect_b64 s[62:63], -1, 0\n\tv_pk_add_f32 v[142:143], v[128:129], v[130:131]\n\ts_nop 0\n\tv_exp_f32 v136, v136\n\tv_exp_f32 v137, v137\n\tv_exp_f32 v138, v138\n\tv_exp_f32 v139, v139\n\t" \
    "v_pk_fma_f32 v[142:143], v[136:137], %[qt], v[142:143]\n\tv_pk_fma_f32 v[142:143], v[138:139], %[qt], v[142:143]\n\tv_add_f32 v144, v142, v143\n\tv_add_f32 v145, v142, v143\n\ts_nop 1\n\t" \
    "v_permlane16_swap_b32 v144, v145\n\tv_add_f32 v142, v144, v145\n\tv_add_f32 v143, v144, v145\n\ts_nop 1\n\tv_permlane32_swap_b32 v142, v143\n\tv_add_f32 v142, v142, v143\n\ts_nop 1\n\t" \
    "v_add_f32_dpp v142, v142, v142 row_ror:8 row_mask:0xf bank_mask:0xf\n\tv_rcp_f32 v144, v142\n\tv_subrev_u32 v142, 0x1f800000, v142\n\tv_cmp_gt_u32 vcc, 2.0, v142\n\t" \
    "v_pk_mul_f32 v[128:129], v[144:145], v[128:129] op_sel_hi:[0,1]\n\tv_pk_mul_f32 v[130:131], v[144:145], v[130:131] op_sel_hi:[0,1]\n\tv_cvt_pk_bf16_f32 v128, v128, v129\n\t" \
    "v_cvt_pk_bf16_f32 v129, v130, v131\n\ts_and_b64 exec, vcc, s[62:63]\n\tbuffer_store_dwordx2 v[128:129], %[px], %[rs], %[o0] offen sc1\n\t" \
    "buffer_store_dwordx2 v[128:129], %[px], %[rs], %[o1] offen sc1\n\tbuffer_store_dwordx2 v[128:129], %[px], %[rs], %[o2] offen sc1\n\tv_pk_mul_f32 v[136:137], v[144:145], v[136:137] op_sel_hi:[0,1]\n\t" \
    "v_pk_mul_f32 v[138:139], v[144:145], v[138:139] op_sel_hi:[0,1]\n\tv_cvt_pk_bf16_f32 v136, v136, v137\n\tv_cvt_pk_bf16_f32 v137, v138, v139\n\t" \
    "buffer_store_dwordx2 v[136:137], %[pm], %[rs], 0 offen sc1\n\ts_mov_b64 exec, -1\n\ts_not_b64 %[bad], vcc\n\ts_cmp_eq_u32 s62, 0\n\ts_cbranch_scc1 6f\n\ts_branch 9f\n\t6:\nds_read_b32 v140, %[ta]\n\t" \
    "ds_read_b64_tr_b16 v[96:97], %[fr]\n\tds_read_b64_tr_b16 v[98:99], %[fr] offset:64\n\tds_read_b64_tr_b16 v[100:101], %[fr] offset:512\n\tds_read_b64_tr_b16 v[102:103], %[fr] offset:576\n\t" \
    "ds_read_b64_tr_b16 v[104:105], %[fr] offset:1024\n\tds_read_b64_tr_b16 v[106:107], %[fr] offset:1088\n\tds_read_b64_tr_b16 v[108:109], %[fr] offset:1536\n\t" \
    "ds_read_b64_tr_b16 v[110:111], %[fr] offset:1600\n\ts_waitcnt lgkmcnt(0)\n\tds_read_b32 v141, %[ta] offset:2048\n\tds_read_b64_tr_b16 v[112:113], %[fr] offset:2048\n\t" \
    "ds_read_b64_tr_b16 v[114:115], %[fr] offset:2112\n\tds_read_b64_tr_b16 v[116:117], %[fr] offset:2560\n\tds_read_b64_tr_b16 v[118:119], %[fr] offset:2624\n\t" \
    "ds_read_b64_tr_b16 v[120:121], %[fr] offset:3072\n\tds_read_b64_tr_b16 v[122:123], %[fr] offset:3136\n\tds_read_b64_tr_b16 v[124:125], %[fr] offset:3584\n\t" \
    "ds_read_b64_tr_b16 v[126:127], %[fr] offset:3648\n\tv_mfma_f32_16x16x32_bf16 v[128:131], v[64:67], v[96:99], 0\n\tv_mfma_f32_16x16x32_bf16 v[132:135], v[68:71], v[96:99], 0\n\t" \
    "v_mfma_f32_16x16x32_bf16 v[128:131], v[72:75], v[100:103], v[128:131]\n\tv_mfma_f32_16x16x32_bf16 v[132:135], v[76:79], v[100:103], v[132:135]\n\t" \
    "v_mfma_f32_16x16x32_bf16 v[128:131], v[80:83], v[104:107], v[128:131]\n\tv_mfma_f32_16x16x32_bf16 v[132:135], v[84:87], v[104:107], v[132:135]\n\t" \
    "v_mfma_f32_16x16x32_bf16 v[128:131], v[88:91], v[108:111], v[128:131]\n\tv_mfma_f32_16x16x32_bf16 v[132:135], v[92:95], v[108:111], v[132:135]\n\ts_sub_u32 %[cn], %[cn], 1\n\ts_cmp_lt_i32 %[cn], 0\n\t" \
    "s_waitcnt lgkmcnt(0)\n\ts_cbranch_scc0 12b\n\tv_mov_b32 v140, v198\n\tv_mov_b32 v141, v198\n\ts_branch 12b\n\t3:\ns_waitcnt lgkmcnt(0)\n\tds_read_b128 v[160:163], %[ml]\n\t" \
    "ds_read_b128 v[164:167], %[ml] offset:1024\n\tds_read_b128 v[168:171], %[ml] offset:2048\n\tds_read_b128 v[172:175], %[ml] offset:3072\n\tds_read_b128 v[176:179], %[ml] offset:4096\n\t" \
    "ds_read_b128 v[180:183], %[ml] offset:5120\n\tds_read_b128 v[184:187], %[ml] offset:6144\n\tds_read_b128 v[188:191], %[ml] offset:7168\n\ts_waitcnt lgkmcnt(0)\n\tv_max3_u32 v160, v160, v161, v162\n\t" \
    "v_max3_u32 v160, v160, v163, v164\n\tv_max3_u32 v160, v160, v165, v166\n\tv_max3_u32 v160, v160, v167, v168\n\tv_max3_u32 v160, v160, v169, v170\n\tv_max3_u32 v160, v160, v171, v172\n\t" \
    "v_max3_u32 v160, v160, v173, v174\n\tv_max3_u32 v160, v160, v175, v176\n\tv_max3_u32 v160, v160, v177, v178\n\tv_max3_u32 v160, v160, v179, v180\n\tv_max3_u32 v160, v160, v181, v182\n\t" \
    "v_max3_u32 v160, v160, v183, v184\n\tv_max3_u32 v160, v160, v185, v186\n\tv_max3_u32 v160, v160, v187, v188\n\tv_max3_u32 v160, v160, v189, v190\n\tv_max_u32 v160, v160, v191\n\t" \
    "v_cmp_eq_u32 vcc, -1, v160\n\ts_cmp_eq_u32 %[s], 0\n\ts_cbranch_scc1 2b\n\ts_cbranch_vccz 2b\n\ts_add_u32 %[cnt], %[cnt], 1\n\ts_cmp_gt_u32 %[cnt], %[cap]\n\ts_cselect_b32 %[cap], 0, %[cap]\n\t" \
    "s_cbranch_scc1 2b\n\ts_barrier\n\ts_branch 1b\n\t9:\n"
#define ASM_HST2 \
    "s_mov_b32 s60, m0\n\ts_mov_b32 %[cnt], 0\n\t1:\nv_mov_b64 v[48:49], 0\n\tv_mov_b64 v[50:51], 0\n\tv_mov_b64 v[52:53], 0\n\tv_mov_b64 v[54:55], 0\n\tv_mov_b64 v[56:57], 0\n\tv_mov_b64 v[58:59], 0\n\t" \
    "v_mov_b64 v[60:61], 0\n\tv_mov_b64 v[62:63], 0\n\ts_add_u32 %[tg], %[tg], 1\n\tv_mov_b32 v198, %[tg]\n\ts_waitcnt vmcnt(0) lgkmcnt(0)\n\ts_barrier\n\tds_read_b64_tr_b16 v[16:17], %[ma]\n\t" \
    "ds_read_b64_tr_b16 v[18:19], %[ma] offset:512\n\tds_read_b64_tr_b16 v[20:21], %[ma] offset:1024\n\tds_read_b64_tr_b16 v[22:23], %[ma] offset:1536\n\tds_read_b64_tr_b16 v[24:25], %[ma] offset:2048\n\t" \
    "ds_read_b64_tr_b16 v[26:27], %[ma] offset:2560\n\tds_read_b64_tr_b16 v[28:29], %[ma] offset:3072\n\tds_read_b64_tr_b16 v[30:31], %[ma] offset:3584\n\ts_waitcnt lgkmcnt(6)\n\t" \
    "v_smfmac_f32_16x16x64_bf16 v[48:51], v[16:19], a[0:7], %[ix]\n\ts_waitcnt lgkmcnt(4)\n\tds_read_b64_tr_b16 v[32:33], %[ma] offset:4096\n\tds_read_b64_tr_b16 v[34:35], %[ma] offset:4608\n\t" \
    "v_smfmac_f32_16x16x64_bf16 v[48:51], v[20:23], a[8:15], %[ix]\n\ts_waitcnt lgkmcnt(4)\n\tds_read_b64_tr_b16 v[36:37], %[ma] offset:5120\n\tds_read_b64_tr_b16 v[38:39], %[ma] offset:5632\n\t" \
    "v_smfmac_f32_16x16x64_bf16 v[48:51], v[24:27], a[16:23], %[ix]\n\ts_waitcnt lgkmcnt(4)\n\tds_read_b64_tr_b16 v[40:41], %[ma] offset:6144\n\tds_read_b64_tr_b16 v[42:43], %[ma] offset:6656\n\t" \
    "v_smfmac_f32_16x16x64_bf16 v[48:51], v[28:31], a[24:31], %[ix]\n\ts_waitcnt lgkmcnt(4)\n\tds_read_b64_tr_b16 v[44:45], %[ma] offset:7168\n\tds_read_b64_tr_b16 v[46:47], %[ma] offset:7680\n\t" \
    "v_smfmac_f32_16x16x64_bf16 v[48:51], v[32:35], a[32:39], %[ix]\n\ts_waitcnt lgkmcnt(4)\n\tv_smfmac_f32_16x16x64_bf16 v[48:51], v[36:39], a[40:47], %[ix]\n\ts_waitcnt lgkmcnt(2)\n\t" \
    "v_smfmac_f32_16x16x64_bf16 v[48:51], v[40:43], a[48:55], %[ix]\n\ts_waitcnt lgkmcnt(0)\n\tv_smfmac_f32_16x16x64_bf16 v[48:51], v[44:47], a[56:63], %[ix]\n\t" \
    "v_smfmac_f32_16x16x64_bf16 v[52:55], v[16:19], a[64:71], %[ix]\n\tv_smfmac_f32_16x16x64_bf16 v[52:55], v[20:23], a[72:79], %[ix]\n\tv_add_f32 v48, v48, v50\n\t" \
    "v_smfmac_f32_16x16x64_bf16 v[52:55], v[24:27], a[80:87], %[ix]\n\tv_add_f32 v49, v49, v51\n\tv_smfmac_f32_16x16x64_bf16 v[52:55], v[28:31], a[88:95], %[ix]\n\tv_mul_f32 v48, %[c2], v48\n\t" \
    "v_smfmac_f32_16x16x64_bf16 v[52:55], v[32:35], a[96:103], %[ix]\n\tv_mul_f32 v49, %[c2], v49\n\tv_smfmac_f32_16x16x64_bf16 v[52:55], v[36:39], a[104:111], %[ix]\n\tv_cvt_pk_bf16_f32 v50, v48, v49\n\t" \
    "v_smfmac_f32_16x16x64_bf16 v[52:55], v[40:43], a[112:119], %[ix]\n\tds_write_b32 %[qw], v50\n\tv_smfmac_f32_16x16x64_bf16 v[52:55], v[44:47], a[120:127], %[ix]\n\tv_cmp_u_f32 vcc, v48, v49\n\t" \
    "v_smfmac_f32_16x16x64_bf16 v[56:59], v[16:19], a[128:135], %[ix]\n\tv_smfmac_f32_16x16x64_bf16 v[56:59], v[20:23], a[136:143], %[ix]\n\tv_add_f32 v52, v52, v54\n\t" \
    "v_smfmac_f32_16x16x64_bf16 v[56:59], v[24:27], a[144:151], %[ix]\n\ts_cbranch_vccnz 3f\n\t2:\nv_add_f32 v53, v53, v55\n\tv_smfmac_f32_16x16x64_bf16 v[56:59], v[28:31], a[152:159], %[ix]\n\t" \
    "v_mul_f32 v52, %[c2], v52\n\tv_smfmac_f32_16x16x64_bf16 v[56:59], v[32:35], a[160:167], %[ix]\n\tv_mul_f32 v53, %[c2], v53\n\tv_smfmac_f32_16x16x64_bf16 v[56:59], v[36:39], a[168:175], %[ix]\n\t" \
    "v_cvt_pk_bf16_f32 v54, v52, v53\n\tv_smfmac_f32_16x16x64_bf16 v[56:59], v[40:43], a[176:183], %[ix]\n\tds_write_b32 %[qw], v54 offset:256\n\tds_write_b32 %[qw], v198 offset:4096\n\t" \
    "v_smfmac_f32_16x16x64_bf16 v[56:59], v[44:47], a[184:191], %[ix]\n\ts_barrier\n\tv_smfmac_f32_16x16x64_bf16 v[60:63], v[16:19], a[192:199], %[ix]\n\t" \
    "v_smfmac_f32_16x16x64_bf16 v[60:63], v[20:23], a[200:207], %[ix]\n\tv_add_f32 v56, v56, v58\n\tv_smfmac_f32_16x16x64_bf16 v[60:63], v[24:27], a[208:215], %[ix]\n\tv_add_f32 v57, v57, v59\n\t" \
    "v_smfmac_f32_16x16x64_bf16 v[60:63], v[28:31], a[216:223], %[ix]\n\tv_cvt_pk_bf16_f32 v58, v56, v57\n\tv_smfmac_f32_16x16x64_bf16 v[60:63], v[32:35], a[224:231], %[ix]\n\tds_write_b32 %[kw], v58\n\t" \
    "v_smfmac_f32_16x16x64_bf16 v[60:63], v[36:39], a[232:239], %[ix]\n\tv_smfmac_f32_16x16x64_bf16 v[60:63], v[40:43], a[240:247], %[ix]\n\t" \
    "v_smfmac_f32_16x16x64_bf16 v[60:63], v[44:47], a[248:255], %[ix]\n\ts_nop 7\n\tv_add_f32 v60, v60, v62\n\tv_add_f32 v61, v61, v63\n\tv_cvt_pk_bf16_f32 v62, v60, v61\n\t" \
    "ds_write_b32 %[kw], v62 offset:256\n\tds_write_b32 %[kw], v198 offset:4096\n\ts_barrier\n\ts_mov_b32 m0, s60\n\ts_branch 9f\n\t3:\ns_waitcnt lgkmcnt(0)\n\tds_read_b128 v[160:163], %[ml]\n\t" \
    "ds_read_b128 v[164:167], %[ml] offset:1024\n\tds_read_b128 v[168:171], %[ml] offset:2048\n\tds_read_b128 v[172:175], %[ml] offset:3072\n\tds_read_b128 v[176:179], %[ml] offset:4096\n\t" \
    "ds_read_b128 v[180:183], %[ml] offset:5120\n\tds_read_b128 v[184:187], %[ml] offset:6144\n\tds_read_b128 v[188:191], %[ml] offset:7168\n\ts_waitcnt lgkmcnt(0)\n\tv_max3_u32 v160, v160, v161, v162\n\t" \
    "v_max3_u32 v160, v160, v163, v164\n\tv_max3_u32 v160, v160, v165, v166\n\tv_max3_u32 v160, v160, v167, v168\n\tv_max3_u32 v160, v160, v169, v170\n\tv_max3_u32 v160, v160, v171, v172\n\t" \
    "v_max3_u32 v160, v160, v173, v174\n\tv_max3_u32 v160, v160, v175, v176\n\tv_max3_u32 v160, v160, v177, v178\n\tv_max3_u32 v160, v160, v179, v180\n\tv_max3_u32 v160, v160, v181, v182\n\t" \
    "v_max3_u32 v160, v160, v183, v184\n\tv_max3_u32 v160, v160, v185, v186\n\tv_max3_u32 v160, v160, v187, v188\n\tv_max3_u32 v160, v160, v189, v190\n\tv_max_u32 v160, v160, v191\n\t" \
    "v_cmp_eq_u32 vcc, -1, v160\n\ts_cmp_eq_u32 %[s], 0\n\ts_cbranch_scc1 2b\n\ts_cbranch_vccz 2b\n\ts_add_u32 %[cnt], %[cnt], 1\n\ts_cmp_gt_u32 %[cnt], %[cap]\n\ts_cselect_b32 %[cap], 0, %[cap]\n\t" \
    "s_cbranch_scc1 2b\n\ts_barrier\n\ts_branch 1b\n\t9:\n"
#define ASM_VST0 \
    "s_mov_b32 s60, m0\n\ts_mov_b32 %[cnt], 0\n\ts_mov_b32 s62, %[pf]\n\ts_memrealtime s[78:79]\n\ts_cmp_eq_u32 %[s], 0\n\ts_cbranch_scc1 13f\n\t20:\ns_memrealtime s[70:71]\n\ts_waitcnt lgkmcnt(0)\n\t" \
    "s_lshl_b32 s70, s70, 4\n\ts_sub_u32 s70, %[due], s70\n\ts_sub_u32 s70, s70, 512\n\ts_cmp_lt_u32 s70, 0x4000\n\ts_cbranch_scc0 21f\n\ts_sleep 1\n\ts_branch 20b\n\t21:\ns_memtime s[70:71]\n\t" \
    "s_memrealtime s[72:73]\n\ts_nop 2\n\ts_memrealtime s[74:75]\n\ts_nop 2\n\ts_memrealtime s[76:77]\n\ts_nop 2\n\ts_memrealtime s[78:79]\n\ts_nop 2\n\ts_memrealtime s[80:81]\n\ts_nop 2\n\t" \
    "s_memrealtime s[82:83]\n\ts_nop 2\n\ts_memrealtime s[84:85]\n\ts_waitcnt lgkmcnt(0)\n\ts_add_u32 s71, s72, 4\n\ts_cmp_eq_u32 s84, s72\n\ts_cselect_b32 s71, s71, s84\n\ts_cselect_b32 s73, 16, 0\n\t" \
    "s_add_u32 s70, s70, s73\n\ts_cmp_eq_u32 s82, s72\n\ts_cselect_b32 s71, s71, s82\n\ts_cselect_b32 s73, 16, 0\n\ts_add_u32 s70, s70, s73\n\ts_cmp_eq_u32 s80, s72\n\ts_cselect_b32 s71, s71, s80\n\t" \
    "s_cselect_b32 s73, 16, 0\n\ts_add_u32 s70, s70, s73\n\ts_cmp_eq_u32 s78, s72\n\ts_cselect_b32 s71, s71, s78\n\ts_cselect_b32 s73, 16, 0\n\ts_add_u32 s70, s70, s73\n\ts_cmp_eq_u32 s76, s72\n\t" \
    "s_cselect_b32 s71, s71, s76\n\ts_cselect_b32 s73, 16, 0\n\ts_add_u32 s70, s70, s73\n\ts_cmp_eq_u32 s74, s72\n\ts_cselect_b32 s71, s71, s74\n\ts_cselect_b32 s73, 16, 0\n\ts_add_u32 s70, s70, s73\n\t" \
    "s_sub_u32 s72, s70, %[pc]\n\ts_sub_u32 s73, s71, %[pr]\n\ts_mov_b32 %[pc], s70\n\ts_mov_b32 %[pr], s71\n\ts_lshl_b32 s72, s72, 4\n\ts_mul_i32 s74, s73, %[f]\n\ts_sub_u32 s72, s72, s74\n\t" \
    "s_ashr_i32 s72, s72, 8\n\ts_cmp_lt_u32 s73, 320\n\ts_cselect_b32 s72, s72, 0\n\ts_add_u32 %[f], %[f], s72\n\ts_max_i32 %[f], %[f], 240\n\ts_min_i32 %[f], %[f], 400\n\ts_lshl_b32 s71, s71, 4\n\t" \
    "s_sub_u32 s72, %[due], s71\n\ts_mul_i32 s72, s72, %[f]\n\ts_ashr_i32 s72, s72, 8\n\ts_add_u32 s72, s72, s70\n\ts_memtime s[74:75]\n\ts_getpc_b64 s[76:77]\n\t22:\ns_add_u32 s76, s76, 23f-22b\n\t" \
    "s_addc_u32 s77, s77, 0\n\ts_sub_u32 s72, s72, 120\n\ts_waitcnt lgkmcnt(0)\n\ts_sub_u32 s72, s72, s74\n\ts_max_i32 s72, s72, 0\n\ts_min_i32 s72, s72, 640\n\ts_and_b32 s72, s72, -4\n\t" \
    "s_sub_u32 s76, s76, s72\n\ts_subb_u32 s77, s77, 0\n\ts_setpc_b64 s[76:77]\n\t.rept 160\n\ts_nop 0\n\t.endr\n\t23:\ns_memrealtime s[78:79]\n\ts_mov_b32 m0, %[lm]\n\ts_add_u32 s61, %[fb], 0x1000\n\t" \
    "buffer_load_dwordx4 %[dv], %[rs], %[fb] offen sc1 lds\n\tbuffer_load_dwordx4 %[dv], %[rs], %[fb] offen offset:1024 sc1 lds\n\tbuffer_load_dwordx4 %[dv], %[rs], %[fb] offen offset:2048 sc1 lds\n\t" \
    "buffer_load_dwordx4 %[dv], %[rs], %[fb] offen offset:3072 sc1 lds\n\ts_add_u32 m0, m0, 0x1000\n\ts_nop 0\n\tbuffer_load_dwordx4 %[dv], %[rs], s61 offen sc1 lds\n\t" \
    "buffer_load_dwordx4 %[dv], %[rs], s61 offen offset:1024 sc1 lds\n\tbuffer_load_dwordx4 %[dv], %[rs], s61 offen offset:2048 sc1 lds\n\t" \
    "buffer_load_dwordx4 %[dv], %[rs], s61 offen offset:3072 sc1 lds\n\t13:\ns_mov_b32 m0, %[lk]\n\ts_nop 0\n\tbuffer_load_dword %[pv], %[rk], s62 offen sc1 lds\n\ts_add_u32 s62, s62, 0x2000\n\t" \
    "buffer_load_dword %[pv], %[rk], s62 offen sc1 lds\n\ts_nop 0\n\tbuffer_load_dword %[pv], %[rs], %[pw] offen sc1 lds\n\t1:\nv_mov_b64 v[48:49], 0\n\tv_mov_b64 v[50:51], 0\n\tv_mov_b64 v[52:53], 0\n\t" \
    "v_mov_b64 v[54:55], 0\n\tv_mov_b64 v[72:73], 0\n\tv_mov_b64 v[74:75], 0\n\tv_mov_b64 v[76:77], 0\n\tv_mov_b64 v[78:79], 0\n\tv_mov_b64 v[80:81], 0\n\tv_mov_b64 v[82:83], 0\n\tv_mov_b64 v[84:85], 0\n\t" \
    "v_mov_b64 v[86:87], 0\n\ts_waitcnt lgkmcnt(0)\n\ts_lshl_b32 s78, s78, 4\n\ts_sub_u32 s79, s78, %[due]\n\ts_add_u32 s79, s79, 176\n\ts_add_u32 s78, s78, %[fm]\n\ts_cmp_lt_u32 s79, 240\n\t" \
    "s_cselect_b32 s78, %[due], s78\n\ts_min_u32 s78, s78, -2\n\ts_add_u32 s79, s78, %[pe]\n\tv_mov_b32 v98, s78\n\tv_mov_b32 v99, s79\n\ts_add_u32 %[tg], %[tg], 1\n\tv_mov_b32 v198, %[tg]\n\t" \
    "s_waitcnt vmcnt(3) lgkmcnt(0)\n\ts_barrier\n\tds_read_b64_tr_b16 v[16:17], %[ma]\n\tds_read_b64_tr_b16 v[18:19], %[ma] offset:512\n\tds_read_b64_tr_b16 v[20:21], %[ma] offset:1024\n\t" \
    "ds_read_b64_tr_b16 v[22:23], %[ma] offset:1536\n\tds_read_b64_tr_b16 v[24:25], %[ma] offset:2048\n\tds_read_b64_tr_b16 v[26:27], %[ma] offset:2560\n\tds_read_b64_tr_b16 v[28:29], %[ma] offset:3072\n\t" \
    "ds_read_b64_tr_b16 v[30:31], %[ma] offset:3584\n\ts_waitcnt lgkmcnt(6)\n\tv_smfmac_f32_16x16x64_bf16 v[48:51], v[16:19], a[0:7], %[ix]\n\ts_waitcnt lgkmcnt(4)\n\t" \
    "ds_read_b64_tr_b16 v[32:33], %[ma] offset:4096\n\tds_read_b64_tr_b16 v[34:35], %[ma] offset:4608\n\tv_smfmac_f32_16x16x64_bf16 v[48:51], v[20:23], a[8:15], %[ix]\n\ts_waitcnt lgkmcnt(4)\n\t" \
    "ds_read_b64_tr_b16 v[36:37], %[ma] offset:5120\n\tds_read_b64_tr_b16 v[38:39], %[ma] offset:5632\n\tv_smfmac_f32_16x16x64_bf16 v[48:51], v[24:27], a[16:23], %[ix]\n\ts_waitcnt lgkmcnt(4)\n\t" \
    "ds_read_b64_tr_b16 v[40:41], %[ma] offset:6144\n\tds_read_b64_tr_b16 v[42:43], %[ma] offset:6656\n\tv_smfmac_f32_16x16x64_bf16 v[48:51], v[28:31], a[24:31], %[ix]\n\ts_waitcnt lgkmcnt(4)\n\t" \
    "ds_read_b64_tr_b16 v[44:45], %[ma] offset:7168\n\tds_read_b64_tr_b16 v[46:47], %[ma] offset:7680\n\tv_smfmac_f32_16x16x64_bf16 v[48:51], v[32:35], a[32:39], %[ix]\n\ts_waitcnt lgkmcnt(4)\n\t" \
    "v_smfmac_f32_16x16x64_bf16 v[48:51], v[36:39], a[40:47], %[ix]\n\ts_waitcnt lgkmcnt(2)\n\tv_smfmac_f32_16x16x64_bf16 v[48:51], v[40:43], a[48:55], %[ix]\n\ts_waitcnt lgkmcnt(0)\n\t" \
    "v_smfmac_f32_16x16x64_bf16 v[48:51], v[44:47], a[56:63], %[ix]\n\tv_smfmac_f32_16x16x64_bf16 v[52:55], v[16:19], a[64:71], %[ix]\n\tv_smfmac_f32_16x16x64_bf16 v[52:55], v[20:23], a[72:79], %[ix]\n\t" \
    "v_add_f32 v48, v48, v50\n\tv_add_f32 v49, v49, v51\n\tv_cvt_pk_bf16_f32 v50, v48, v49\n\tv_smfmac_f32_16x16x64_bf16 v[52:55], v[24:27], a[80:87], %[ix]\n\tds_write_b32 %[vw], v50\n\t" \
    "ds_write_b32 %[vw], v198 offset:2048\n\tv_smfmac_f32_16x16x64_bf16 v[52:55], v[28:31], a[88:95], %[ix]\n\ts_barrier\n\tds_read_b32 v96, %[ta]\n\tds_read_b64_tr_b16 v[56:57], %[fr]\n\t" \
    "ds_read_b64_tr_b16 v[58:59], %[fr] offset:64\n\tv_smfmac_f32_16x16x64_bf16 v[52:55], v[32:35], a[96:103], %[ix]\n\tds_read_b64_tr_b16 v[60:61], %[fr] offset:512\n\t" \
    "ds_read_b64_tr_b16 v[62:63], %[fr] offset:576\n\tv_smfmac_f32_16x16x64_bf16 v[52:55], v[36:39], a[104:111], %[ix]\n\tv_cmp_u_f32 vcc, v48, v49\n\t" \
    "v_smfmac_f32_16x16x64_bf16 v[52:55], v[40:43], a[112:119], %[ix]\n\tv_smfmac_f32_16x16x64_bf16 v[52:55], v[44:47], a[120:127], %[ix]\n\ts_waitcnt lgkmcnt(2)\n\ts_cbranch_vccnz 3f\n\t2:\n" \
    "s_mov_b64 exec, %[am]\n\tbuffer_store_dwordx2 v[98:99], %[av], %[rs], %[ao] offen sc1\n\ts_mov_b64 exec, -1\n\tv_smfmac_f32_16x16x64_bf16 v[72:75], v[56:59], a[128:135], %[iy]\n\t" \
    "v_smfmac_f32_16x16x64_bf16 v[76:79], v[56:59], a[160:167], %[iy]\n\tv_add_f32 v52, v52, v54\n\tv_add_f32 v53, v53, v55\n\tv_cvt_pk_bf16_f32 v54, v52, v53\n\t" \
    "v_smfmac_f32_16x16x64_bf16 v[80:83], v[56:59], a[192:199], %[iy]\n\tds_write_b32 %[vw], v54 offset:1024\n\tds_write_b32 %[vw], v198 offset:3072\n\t" \
    "v_smfmac_f32_16x16x64_bf16 v[84:87], v[56:59], a[224:231], %[iy]\n\ts_waitcnt lgkmcnt(2)\n\ts_barrier\n\tds_read_b32 v97, %[ta] offset:1024\n\tds_read_b64_tr_b16 v[64:65], %[fr] offset:1024\n\t" \
    "ds_read_b64_tr_b16 v[66:67], %[fr] offset:1088\n\tv_smfmac_f32_16x16x64_bf16 v[84:87], v[60:63], a[232:239], %[iy]\n\tds_read_b64_tr_b16 v[68:69], %[fr] offset:1536\n\t" \
    "ds_read_b64_tr_b16 v[70:71], %[fr] offset:1600\n\tv_smfmac_f32_16x16x64_bf16 v[80:83], v[60:63], a[200:207], %[iy]\n\tv_smfmac_f32_16x16x64_bf16 v[76:79], v[60:63], a[168:175], %[iy]\n\t" \
    "v_smfmac_f32_16x16x64_bf16 v[72:75], v[60:63], a[136:143], %[iy]\n\ts_waitcnt lgkmcnt(2)\n\t12:\nv_smfmac_f32_16x16x64_bf16 v[72:75], v[64:67], a[144:151], %[iy]\n\tv_min_u32 v96, v96, v97\n\t" \
    "v_smfmac_f32_16x16x64_bf16 v[76:79], v[64:67], a[176:183], %[iy]\n\ts_waitcnt lgkmcnt(0)\n\tv_cmp_eq_u32 vcc, v96, v198\n\tv_smfmac_f32_16x16x64_bf16 v[72:75], v[68:71], a[152:159], %[iy]\n\t" \
    "v_smfmac_f32_16x16x64_bf16 v[76:79], v[68:71], a[184:191], %[iy]\n\tv_smfmac_f32_16x16x64_bf16 v[80:83], v[64:67], a[208:215], %[iy]\n\ts_cmp_eq_u64 vcc, -1\n\tv_cvt_pk_bf16_f32 v88, v72, v73\n\t" \
    "v_smfmac_f32_16x16x64_bf16 v[84:87], v[64:67], a[240:247], %[iy]\n\ts_cselect_b64 s[62:63], -1, 0\n\tv_cvt_pk_bf16_f32 v89, v74, v75\n\t" \
    "v_smfmac_f32_16x16x64_bf16 v[80:83], v[68:71], a[216:223], %[iy]\n\tv_cvt_pk_bf16_f32 v90, v76, v77\n\tv_smfmac_f32_16x16x64_bf16 v[84:87], v[68:71], a[248:255], %[iy]\n\t" \
    "v_cvt_pk_bf16_f32 v91, v78, v79\n\tv_permlane16_swap_b32 v88, v90\n\ts_nop 0\n\tv_permlane16_swap_b32 v89, v91\n\ts_mov_b64 exec, s[62:63]\n\t" \
    "buffer_store_dwordx4 v[88:91], %[so0], %[rs], 0 offen sc1\n\tv_cvt_pk_bf16_f32 v92, v80, v81\n\tv_cvt_pk_bf16_f32 v93, v82, v83\n\tv_cvt_pk_bf16_f32 v94, v84, v85\n\t" \
    "v_cvt_pk_bf16_f32 v95, v86, v87\n\ts_nop 0\n\tv_permlane16_swap_b32 v92, v94\n\tv_permlane16_swap_b32 v93, v95\n\tbuffer_store_dwordx4 v[92:95], %[so1], %[rs], 0 offen sc1\n\ts_mov_b64 exec, -1\n\t" \
    "s_cmp_eq_u32 s62, 0\n\ts_cbranch_scc1 6f\n\tv_max3_u32 v96, v88, v89, v90\n\tv_max3_u32 v96, v96, v91, v92\n\tv_max3_u32 v96, v96, v93, v94\n\tv_max_u32 v96, v96, v95\n\tv_cmp_eq_u32 vcc, -1, v96\n\t" \
    "s_mov_b32 m0, s60\n\ts_cbranch_vccnz 7f\n\ts_branch 9f\n\t6:\nds_read_b32 v96, %[ta]\n\tds_read_b64_tr_b16 v[56:57], %[fr]\n\tds_read_b64_tr_b16 v[58:59], %[fr] offset:64\n\t" \
    "ds_read_b64_tr_b16 v[60:61], %[fr] offset:512\n\tds_read_b64_tr_b16 v[62:63], %[fr] offset:576\n\tds_read_b32 v97, %[ta] offset:1024\n\tds_read_b64_tr_b16 v[64:65], %[fr] offset:1024\n\t" \
    "ds_read_b64_tr_b16 v[66:67], %[fr] offset:1088\n\tds_read_b64_tr_b16 v[68:69], %[fr] offset:1536\n\tds_read_b64_tr_b16 v[70:71], %[fr] offset:1600\n\tv_mov_b64 v[72:73], 0\n\tv_mov_b64 v[74:75], 0\n\t" \
    "v_mov_b64 v[76:77], 0\n\tv_mov_b64 v[78:79], 0\n\tv_mov_b64 v[80:81], 0\n\tv_mov_b64 v[82:83], 0\n\tv_mov_b64 v[84:85], 0\n\tv_mov_b64 v[86:87], 0\n\ts_sub_u32 %[cn], %[cn], 1\n\t" \
    "s_cmp_lt_i32 %[cn], 0\n\ts_waitcnt lgkmcnt(0)\n\tv_smfmac_f32_16x16x64_bf16 v[72:75], v[56:59], a[128:135], %[iy]\n\tv_smfmac_f32_16x16x64_bf16 v[76:79], v[56:59], a[160:167], %[iy]\n\t" \
    "v_smfmac_f32_16x16x64_bf16 v[80:83], v[56:59], a[192:199], %[iy]\n\tv_smfmac_f32_16x16x64_bf16 v[84:87], v[56:59], a[224:231], %[iy]\n\t" \
    "v_smfmac_f32_16x16x64_bf16 v[84:87], v[60:63], a[232:239], %[iy]\n\tv_smfmac_f32_16x16x64_bf16 v[80:83], v[60:63], a[200:207], %[iy]\n\t" \
    "v_smfmac_f32_16x16x64_bf16 v[76:79], v[60:63], a[168:175], %[iy]\n\tv_smfmac_f32_16x16x64_bf16 v[72:75], v[60:63], a[136:143], %[iy]\n\ts_cbranch_scc0 12b\n\tv_mov_b32 v96, v198\n\t" \
    "v_mov_b32 v97, v198\n\ts_branch 12b\n\t7:\nv_min_u32 v88, -2, v88\n\tv_min_u32 v89, -2, v89\n\tv_min_u32 v90, -2, v90\n\tv_min_u32 v91, -2, v91\n\tv_min_u32 v92, -2, v92\n\tv_min_u32 v93, -2, v93\n\t" \
    "v_min_u32 v94, -2, v94\n\tv_min_u32 v95, -2, v95\n\tbuffer_store_dwordx4 v[88:91], %[so0], %[rs], 0 offen sc1\n\tbuffer_store_dwordx4 v[92:95], %[so1], %[rs], 0 offen sc1\n\ts_branch 9f\n\t3:\n" \
    "s_waitcnt lgkmcnt(0)\n\tds_read_b128 v[100:103], %[ml]\n\tds_read_b128 v[104:107], %[ml] offset:1024\n\tds_read_b128 v[108:111], %[ml] offset:2048\n\tds_read_b128 v[112:115], %[ml] offset:3072\n\t" \
    "ds_read_b128 v[116:119], %[ml] offset:4096\n\tds_read_b128 v[120:123], %[ml] offset:5120\n\tds_read_b128 v[124:127], %[ml] offset:6144\n\tds_read_b128 v[128:131], %[ml] offset:7168\n\t" \
    "s_waitcnt lgkmcnt(0)\n\tv_max3_u32 v100, v100, v101, v102\n\tv_max3_u32 v100, v100, v103, v104\n\tv_max3_u32 v100, v100, v105, v106\n\tv_max3_u32 v100, v100, v107, v108\n\t" \
    "v_max3_u32 v100, v100, v109, v110\n\tv_max3_u32 v100, v100, v111, v112\n\tv_max3_u32 v100, v100, v113, v114\n\tv_max3_u32 v100, v100, v115, v116\n\tv_max3_u32 v100, v100, v117, v118\n\t" \
    "v_max3_u32 v100, v100, v119, v120\n\tv_max3_u32 v100, v100, v121, v122\n\tv_max3_u32 v100, v100, v123, v124\n\tv_max3_u32 v100, v100, v125, v126\n\tv_max3_u32 v100, v100, v127, v128\n\t" \
    "v_max3_u32 v100, v100, v129, v130\n\tv_max_u32 v100, v100, v131\n\tv_cmp_eq_u32 vcc, -1, v100\n\ts_cmp_eq_u32 %[s], 0\n\ts_cbranch_scc1 2b\n\ts_cbranch_vccz 2b\n\ts_add_u32 %[cnt], %[cnt], 1\n\t" \
    "s_cmp_gt_u32 %[cnt], %[cap]\n\ts_cselect_b32 %[cap], 0, %[cap]\n\ts_cbranch_scc1 2b\n\ts_barrier\n\ts_mov_b32 s63, 0\n\t4:\ns_memrealtime s[78:79]\n\ts_mov_b32 m0, %[lm]\n\t" \
    "s_add_u32 s61, %[fb], 0x1000\n\tbuffer_load_dwordx4 %[dv], %[rs], %[fb] offen sc1 lds\n\tbuffer_load_dwordx4 %[dv], %[rs], %[fb] offen offset:1024 sc1 lds\n\t" \
    "buffer_load_dwordx4 %[dv], %[rs], %[fb] offen offset:2048 sc1 lds\n\tbuffer_load_dwordx4 %[dv], %[rs], %[fb] offen offset:3072 sc1 lds\n\ts_add_u32 m0, m0, 0x1000\n\ts_nop 0\n\t" \
    "buffer_load_dwordx4 %[dv], %[rs], s61 offen sc1 lds\n\tbuffer_load_dwordx4 %[dv], %[rs], s61 offen offset:1024 sc1 lds\n\tbuffer_load_dwordx4 %[dv], %[rs], s61 offen offset:2048 sc1 lds\n\t" \
    "buffer_load_dwordx4 %[dv], %[rs], s61 offen offset:3072 sc1 lds\n\ts_waitcnt vmcnt(0)\n\ts_waitcnt lgkmcnt(0)\n\tds_read_b128 v[100:103], %[ml]\n\tds_read_b128 v[104:107], %[ml] offset:1024\n\t" \
    "ds_read_b128 v[108:111], %[ml] offset:2048\n\tds_read_b128 v[112:115], %[ml] offset:3072\n\tds_read_b128 v[116:119], %[ml] offset:4096\n\tds_read_b128 v[120:123], %[ml] offset:5120\n\t" \
    "ds_read_b128 v[124:127], %[ml] offset:6144\n\tds_read_b128 v[128:131], %[ml] offset:7168\n\ts_waitcnt lgkmcnt(0)\n\tv_max3_u32 v100, v100, v101, v102\n\tv_max3_u32 v100, v100, v103, v104\n\t" \
    "v_max3_u32 v100, v100, v105, v106\n\tv_max3_u32 v100, v100, v107, v108\n\tv_max3_u32 v100, v100, v109, v110\n\tv_max3_u32 v100, v100, v111, v112\n\tv_max3_u32 v100, v100, v113, v114\n\t" \
    "v_max3_u32 v100, v100, v115, v116\n\tv_max3_u32 v100, v100, v117, v118\n\tv_max3_u32 v100, v100, v119, v120\n\tv_max3_u32 v100, v100, v121, v122\n\tv_max3_u32 v100, v100, v123, v124\n\t" \
    "v_max3_u32 v100, v100, v125, v126\n\tv_max3_u32 v100, v100, v127, v128\n\tv_max3_u32 v100, v100, v129, v130\n\tv_max_u32 v100, v100, v131\n\tv_cmp_eq_u32 vcc, -1, v100\n\ts_add_u32 s63, s63, 1\n\t" \
    "s_cmp_lt_u32 s63, 64\n\ts_cbranch_scc0 1b\n\ts_cbranch_vccnz 4b\n\ts_branch 1b\n\t9:\n"
#define ASM_VST1 \
    "s_mov_b32 s60, m0\n\ts_mov_b32 %[cnt], 0\n\t1:\nv_mov_b64 v[48:49], 0\n\tv_mov_b64 v[50:51], 0\n\tv_mov_b64 v[52:53], 0\n\tv_mov_b64 v[54:55], 0\n\tv_mov_b64 v[72:73], 0\n\tv_mov_b64 v[74:75], 0\n\t" \
    "v_mov_b64 v[76:77], 0\n\tv_mov_b64 v[78:79], 0\n\tv_mov_b64 v[80:81], 0\n\tv_mov_b64 v[82:83], 0\n\tv_mov_b64 v[84:85], 0\n\tv_mov_b64 v[86:87], 0\n\ts_add_u32 %[tg], %[tg], 1\n\t" \
    "v_mov_b32 v198, %[tg]\n\ts_waitcnt vmcnt(0) lgkmcnt(0)\n\ts_barrier\n\tds_read_b64_tr_b16 v[16:17], %[ma]\n\tds_read_b64_tr_b16 v[18:19], %[ma] offset:512\n\t" \
    "ds_read_b64_tr_b16 v[20:21], %[ma] offset:1024\n\tds_read_b64_tr_b16 v[22:23], %[ma] offset:1536\n\tds_read_b64_tr_b16 v[24:25], %[ma] offset:2048\n\tds_read_b64_tr_b16 v[26:27], %[ma] offset:2560\n\t" \
    "ds_read_b64_tr_b16 v[28:29], %[ma] offset:3072\n\tds_read_b64_tr_b16 v[30:31], %[ma] offset:3584\n\ts_waitcnt lgkmcnt(6)\n\tv_smfmac_f32_16x16x64_bf16 v[48:51], v[16:19], a[0:7], %[ix]\n\t" \
    "s_waitcnt lgkmcnt(4)\n\tds_read_b64_tr_b16 v[32:33], %[ma] offset:4096\n\tds_read_b64_tr_b16 v[34:35], %[ma] offset:4608\n\tv_smfmac_f32_16x16x64_bf16 v[48:51], v[20:23], a[8:15], %[ix]\n\t" \
    "s_waitcnt lgkmcnt(4)\n\tds_read_b64_tr_b16 v[36:37], %[ma] offset:5120\n\tds_read_b64_tr_b16 v[38:39], %[ma] offset:5632\n\tv_smfmac_f32_16x16x64_bf16 v[48:51], v[24:27], a[16:23], %[ix]\n\t" \
    "s_waitcnt lgkmcnt(4)\n\tds_read_b64_tr_b16 v[40:41], %[ma] offset:6144\n\tds_read_b64_tr_b16 v[42:43], %[ma] offset:6656\n\tv_smfmac_f32_16x16x64_bf16 v[48:51], v[28:31], a[24:31], %[ix]\n\t" \
    "s_waitcnt lgkmcnt(4)\n\tds_read_b64_tr_b16 v[44:45], %[ma] offset:7168\n\tds_read_b64_tr_b16 v[46:47], %[ma] offset:7680\n\tv_smfmac_f32_16x16x64_bf16 v[48:51], v[32:35], a[32:39], %[ix]\n\t" \
    "s_waitcnt lgkmcnt(4)\n\tv_smfmac_f32_16x16x64_bf16 v[48:51], v[36:39], a[40:47], %[ix]\n\ts_waitcnt lgkmcnt(2)\n\tv_smfmac_f32_16x16x64_bf16 v[48:51], v[40:43], a[48:55], %[ix]\n\t" \
    "s_waitcnt lgkmcnt(0)\n\tv_smfmac_f32_16x16x64_bf16 v[48:51], v[44:47], a[56:63], %[ix]\n\tv_smfmac_f32_16x16x64_bf16 v[52:55], v[16:19], a[64:71], %[ix]\n\t" \
    "v_smfmac_f32_16x16x64_bf16 v[52:55], v[20:23], a[72:79], %[ix]\n\tv_add_f32 v48, v48, v50\n\tv_add_f32 v49, v49, v51\n\tv_cvt_pk_bf16_f32 v50, v48, v49\n\t" \
    "v_smfmac_f32_16x16x64_bf16 v[52:55], v[24:27], a[80:87], %[ix]\n\tds_write_b32 %[vw], v50\n\tds_write_b32 %[vw], v198 offset:2048\n\tv_smfmac_f32_16x16x64_bf16 v[52:55], v[28:31], a[88:95], %[ix]\n\t" \
    "s_barrier\n\tds_read_b32 v96, %[ta]\n\tds_read_b64_tr_b16 v[56:57], %[fr]\n\tds_read_b64_tr_b16 v[58:59], %[fr] offset:64\n\tv_smfmac_f32_16x16x64_bf16 v[52:55], v[32:35], a[96:103], %[ix]\n\t" \
    "ds_read_b64_tr_b16 v[60:61], %[fr] offset:512\n\tds_read_b64_tr_b16 v[62:63], %[fr] offset:576\n\tv_smfmac_f32_16x16x64_bf16 v[52:55], v[36:39], a[104:111], %[ix]\n\tv_cmp_u_f32 vcc, v48, v49\n\t" \
    "v_smfmac_f32_16x16x64_bf16 v[52:55], v[40:43], a[112:119], %[ix]\n\tv_smfmac_f32_16x16x64_bf16 v[52:55], v[44:47], a[120:127], %[ix]\n\ts_waitcnt lgkmcnt(2)\n\ts_cbranch_vccnz 3f\n\t2:\n" \
    "v_smfmac_f32_16x16x64_bf16 v[72:75], v[56:59], a[128:135], %[iy]\n\tv_smfmac_f32_16x16x64_bf16 v[76:79], v[56:59], a[160:167], %[iy]\n\tv_add_f32 v52, v52, v54\n\tv_add_f32 v53, v53, v55\n\t" \
    "v_cvt_pk_bf16_f32 v54, v52, v53\n\tv_smfmac_f32_16x16x64_bf16 v[80:83], v[56:59], a[192:199], %[iy]\n\tds_write_b32 %[vw], v54 offset:1024\n\tds_write_b32 %[vw], v198 offset:3072\n\t" \
    "v_smfmac_f32_16x16x64_bf16 v[84:87], v[56:59], a[224:231], %[iy]\n\ts_waitcnt lgkmcnt(2)\n\ts_barrier\n\tds_read_b32 v97, %[ta] offset:1024\n\tds_read_b64_tr_b16 v[64:65], %[fr] offset:1024\n\t" \
    "ds_read_b64_tr_b16 v[66:67], %[fr] offset:1088\n\tv_smfmac_f32_16x16x64_bf16 v[84:87], v[60:63], a[232:239], %[iy]\n\tds_read_b64_tr_b16 v[68:69], %[fr] offset:1536\n\t" \
    "ds_read_b64_tr_b16 v[70:71], %[fr] offset:1600\n\tv_smfmac_f32_16x16x64_bf16 v[80:83], v[60:63], a[200:207], %[iy]\n\tv_smfmac_f32_16x16x64_bf16 v[76:79], v[60:63], a[168:175], %[iy]\n\t" \
    "v_smfmac_f32_16x16x64_bf16 v[72:75], v[60:63], a[136:143], %[iy]\n\ts_waitcnt lgkmcnt(2)\n\t12:\nv_smfmac_f32_16x16x64_bf16 v[72:75], v[64:67], a[144:151], %[iy]\n\tv_min_u32 v96, v96, v97\n\t" \
    "v_smfmac_f32_16x16x64_bf16 v[76:79], v[64:67], a[176:183], %[iy]\n\ts_waitcnt lgkmcnt(0)\n\tv_cmp_eq_u32 vcc, v96, v198\n\tv_smfmac_f32_16x16x64_bf16 v[72:75], v[68:71], a[152:159], %[iy]\n\t" \
    "v_smfmac_f32_16x16x64_bf16 v[76:79], v[68:71], a[184:191], %[iy]\n\tv_smfmac_f32_16x16x64_bf16 v[80:83], v[64:67], a[208:215], %[iy]\n\ts_cmp_eq_u64 vcc, -1\n\tv_cvt_pk_bf16_f32 v88, v72, v73\n\t" \
    "v_smfmac_f32_16x16x64_bf16 v[84:87], v[64:67], a[240:247], %[iy]\n\ts_cselect_b64 s[62:63], -1, 0\n\tv_cvt_pk_bf16_f32 v89, v74, v75\n\t" \
    "v_smfmac_f32_16x16x64_bf16 v[80:83], v[68:71], a[216:223], %[iy]\n\tv_cvt_pk_bf16_f32 v90, v76, v77\n\tv_smfmac_f32_16x16x64_bf16 v[84:87], v[68:71], a[248:255], %[iy]\n\t" \
    "v_cvt_pk_bf16_f32 v91, v78, v79\n\tv_permlane16_swap_b32 v88, v90\n\ts_nop 0\n\tv_permlane16_swap_b32 v89, v91\n\ts_mov_b64 exec, s[62:63]\n\t" \
    "buffer_store_dwordx4 v[88:91], %[so0], %[rs], 0 offen sc1\n\tv_cvt_pk_bf16_f32 v92, v80, v81\n\tv_cvt_pk_bf16_f32 v93, v82, v83\n\tv_cvt_pk_bf16_f32 v94, v84, v85\n\t" \
    "v_cvt_pk_bf16_f32 v95, v86, v87\n\ts_nop 0\n\tv_permlane16_swap_b32 v92, v94\n\tv_permlane16_swap_b32 v93, v95\n\tbuffer_store_dwordx4 v[92:95], %[so1], %[rs], 0 offen sc1\n\ts_mov_b64 exec, -1\n\t" \
    "s_cmp_eq_u32 s62, 0\n\ts_cbranch_scc1 6f\n\tv_max3_u32 v96, v88, v89, v90\n\tv_max3_u32 v96, v96, v91, v92\n\tv_max3_u32 v96, v96, v93, v94\n\tv_max_u32 v96, v96, v95\n\tv_cmp_eq_u32 vcc, -1, v96\n\t" \
    "s_mov_b32 m0, s60\n\ts_cbranch_vccnz 7f\n\ts_branch 9f\n\t6:\nds_read_b32 v96, %[ta]\n\tds_read_b64_tr_b16 v[56:57], %[fr]\n\tds_read_b64_tr_b16 v[58:59], %[fr] offset:64\n\t" \
    "ds_read_b64_tr_b16 v[60:61], %[fr] offset:512\n\tds_read_b64_tr_b16 v[62:63], %[fr] offset:576\n\tds_read_b32 v97, %[ta] offset:1024\n\tds_read_b64_tr_b16 v[64:65], %[fr] offset:1024\n\t" \
    "ds_read_b64_tr_b16 v[66:67], %[fr] offset:1088\n\tds_read_b64_tr_b16 v[68:69], %[fr] offset:1536\n\tds_read_b64_tr_b16 v[70:71], %[fr] offset:1600\n\tv_mov_b64 v[72:73], 0\n\tv_mov_b64 v[74:75], 0\n\t" \
    "v_mov_b64 v[76:77], 0\n\tv_mov_b64 v[78:79], 0\n\tv_mov_b64 v[80:81], 0\n\tv_mov_b64 v[82:83], 0\n\tv_mov_b64 v[84:85], 0\n\tv_mov_b64 v[86:87], 0\n\ts_sub_u32 %[cn], %[cn], 1\n\t" \
    "s_cmp_lt_i32 %[cn], 0\n\ts_waitcnt lgkmcnt(0)\n\tv_smfmac_f32_16x16x64_bf16 v[72:75], v[56:59], a[128:135], %[iy]\n\tv_smfmac_f32_16x16x64_bf16 v[76:79], v[56:59], a[160:167], %[iy]\n\t" \
    "v_smfmac_f32_16x16x64_bf16 v[80:83], v[56:59], a[192:199], %[iy]\n\tv_smfmac_f32_16x16x64_bf16 v[84:87], v[56:59], a[224:231], %[iy]\n\t" \
    "v_smfmac_f32_16x16x64_bf16 v[84:87], v[60:63], a[232:239], %[iy]\n\tv_smfmac_f32_16x16x64_bf16 v[80:83], v[60:63], a[200:207], %[iy]\n\t" \
    "v_smfmac_f32_16x16x64_bf16 v[76:79], v[60:63], a[168:175], %[iy]\n\tv_smfmac_f32_16x16x64_bf16 v[72:75], v[60:63], a[136:143], %[iy]\n\ts_cbranch_scc0 12b\n\tv_mov_b32 v96, v198\n\t" \
    "v_mov_b32 v97, v198\n\ts_branch 12b\n\t7:\nv_min_u32 v88, -2, v88\n\tv_min_u32 v89, -2, v89\n\tv_min_u32 v90, -2, v90\n\tv_min_u32 v91, -2, v91\n\tv_min_u32 v92, -2, v92\n\tv_min_u32 v93, -2, v93\n\t" \
    "v_min_u32 v94, -2, v94\n\tv_min_u32 v95, -2, v95\n\tbuffer_store_dwordx4 v[88:91], %[so0], %[rs], 0 offen sc1\n\tbuffer_store_dwordx4 v[92:95], %[so1], %[rs], 0 offen sc1\n\ts_branch 9f\n\t3:\n" \
    "s_waitcnt lgkmcnt(0)\n\tds_read_b128 v[100:103], %[ml]\n\tds_read_b128 v[104:107], %[ml] offset:1024\n\tds_read_b128 v[108:111], %[ml] offset:2048\n\tds_read_b128 v[112:115], %[ml] offset:3072\n\t" \
    "ds_read_b128 v[116:119], %[ml] offset:4096\n\tds_read_b128 v[120:123], %[ml] offset:5120\n\tds_read_b128 v[124:127], %[ml] offset:6144\n\tds_read_b128 v[128:131], %[ml] offset:7168\n\t" \
    "s_waitcnt lgkmcnt(0)\n\tv_max3_u32 v100, v100, v101, v102\n\tv_max3_u32 v100, v100, v103, v104\n\tv_max3_u32 v100, v100, v105, v106\n\tv_max3_u32 v100, v100, v107, v108\n\t" \
    "v_max3_u32 v100, v100, v109, v110\n\tv_max3_u32 v100, v100, v111, v112\n\tv_max3_u32 v100, v100, v113, v114\n\tv_max3_u32 v100, v100, v115, v116\n\tv_max3_u32 v100, v100, v117, v118\n\t" \
    "v_max3_u32 v100, v100, v119, v120\n\tv_max3_u32 v100, v100, v121, v122\n\tv_max3_u32 v100, v100, v123, v124\n\tv_max3_u32 v100, v100, v125, v126\n\tv_max3_u32 v100, v100, v127, v128\n\t" \
    "v_max3_u32 v100, v100, v129, v130\n\tv_max_u32 v100, v100, v131\n\tv_cmp_eq_u32 vcc, -1, v100\n\ts_cmp_eq_u32 %[s], 0\n\ts_cbranch_scc1 2b\n\ts_cbranch_vccz 2b\n\ts_add_u32 %[cnt], %[cnt], 1\n\t" \
    "s_cmp_gt_u32 %[cnt], %[cap]\n\ts_cselect_b32 %[cap], 0, %[cap]\n\ts_cbranch_scc1 2b\n\ts_barrier\n\ts_branch 1b\n\t9:\n"
#define ASM_CST \
    "s_mov_b32 s60, m0\n\ts_mov_b32 s40, 0\n\ts_mov_b32 s51, 0\n\ts_mov_b32 s52, 0\n\ts_mov_b64 %[fm], 0\n\ts_memrealtime s[64:65]\n\ts_waitcnt lgkmcnt(0)\n\ts_mov_b32 s54, s64\n\t" \
    "s_lshl_b32 s65, s64, 4\n\ts_sub_u32 s65, s65, 0x8000\n\ts_mov_b32 s64, 0\n\ts_setprio 3\n\t40:\ns_and_b32 s61, s40, 3\n\ts_lshl_b32 s62, s61, 15\n\ts_lshl_b32 s61, s61, 3\n\t" \
    "v_add_u32 v136, s62, %[xa]\n\tv_readlane_b32 s41, %[tv], s61\n\ts_add_u32 s61, s61, 1\n\tv_readlane_b32 s42, %[tv], s61\n\ts_add_u32 s61, s61, 1\n\tv_readlane_b32 s43, %[tv], s61\n\t" \
    "s_add_u32 s61, s61, 1\n\tv_readlane_b32 s44, %[tv], s61\n\ts_add_u32 s61, s61, 1\n\tv_readlane_b32 s45, %[tv], s61\n\ts_add_u32 s61, s61, 1\n\tv_readlane_b32 s46, %[tv], s61\n\t" \
    "s_add_u32 s61, s61, 1\n\tv_readlane_b32 s47, %[tv], s61\n\ts_add_u32 s61, s61, 1\n\ts_add_u32 m0, s52, %[lv]\n\ts_add_u32 s52, s52, 0x2000\n\ts_cmp_eq_u32 s52, 0x6000\n\ts_cselect_b32 s52, 0, s52\n\t" \
    "v_add_u32 v137, m0, %[dv]\n\ts_add_u32 s62, s40, 1\n\tv_mov_b32 v138, s62\n\ts_mov_b32 s61, 0\n\t7:\nds_read_b128 v[132:135], %[fl] offset:64\n\ts_add_u32 s61, s61, 1\n\ts_waitcnt lgkmcnt(0)\n\t" \
    "v_min_i32 v134, v134, v135\n\tv_add_u32 v134, 3, v134\n\tv_min_i32 v132, v132, v134\n\tv_cmp_lt_i32 vcc, v132, v138\n\ts_lshr_b32 s62, s61, 3\n\ts_cmp_gt_u32 s62, %[cap]\n\t" \
    "s_cselect_b32 %[cap], 0, %[cap]\n\ts_cbranch_scc1 8f\n\ts_cbranch_vccnz 7b\n\t8:\nds_read_b128 a[0:3], v136\n\tds_read_b128 a[4:7], v136 offset:1024\n\tds_read_b128 a[8:11], v136 offset:2048\n\t" \
    "ds_read_b128 a[12:15], v136 offset:3072\n\tds_read_b128 a[16:19], v136 offset:4096\n\tds_read_b128 a[20:23], v136 offset:5120\n\tds_read_b128 a[24:27], v136 offset:6144\n\t" \
    "ds_read_b128 a[28:31], v136 offset:7168\n\tds_read_b128 a[32:35], v136 offset:8192\n\tds_read_b128 a[36:39], v136 offset:9216\n\tds_read_b128 a[40:43], v136 offset:10240\n\t" \
    "ds_read_b128 a[44:47], v136 offset:11264\n\ts_waitcnt lgkmcnt(3)\n\tds_read_b128 a[48:51], v136 offset:12288\n\tds_read_b128 a[52:55], v136 offset:13312\n\tds_read_b128 a[56:59], v136 offset:14336\n\t" \
    "ds_read_b128 a[60:63], v136 offset:15360\n\tds_read_b128 a[64:67], v136 offset:16384\n\tds_read_b128 a[68:71], v136 offset:17408\n\tds_read_b128 a[72:75], v136 offset:18432\n\t" \
    "ds_read_b128 a[76:79], v136 offset:19456\n\tds_read_b128 a[80:83], v136 offset:20480\n\tds_read_b128 a[84:87], v136 offset:21504\n\tds_read_b128 a[88:91], v136 offset:22528\n\t" \
    "ds_read_b128 a[92:95], v136 offset:23552\n\ts_waitcnt lgkmcnt(3)\n\tds_read_b128 a[96:99], v136 offset:24576\n\tds_read_b128 a[100:103], v136 offset:25600\n\t" \
    "ds_read_b128 a[104:107], v136 offset:26624\n\tds_read_b128 a[108:111], v136 offset:27648\n\tds_read_b128 a[112:115], v136 offset:28672\n\tds_read_b128 a[116:119], v136 offset:29696\n\t" \
    "ds_read_b128 a[120:123], v136 offset:30720\n\tds_read_b128 a[124:127], v136 offset:31744\n\ts_add_u32 s73, s48, 256\n\t10:\ns_waitcnt lgkmcnt(0)\n\ts_cmp_lg_u32 s64, -1\n\ts_cbranch_scc1 11f\n\t" \
    "s_add_u32 s72, s72, 64\n\ts_buffer_load_dwordx2 s[64:65], %[rs], s72\n\ts_cmp_lg_u32 s72, s73\n\ts_cbranch_scc1 10b\n\ts_waitcnt lgkmcnt(0)\n\ts_cmp_lg_u32 s64, -1\n\ts_cbranch_scc1 11f\n\t" \
    "s_memrealtime s[72:73]\n\ts_waitcnt lgkmcnt(0)\n\ts_lshl_b32 s64, s72, 4\n\ts_sub_u32 s64, s64, 0x8000\n\ts_mov_b32 s65, s64\n\t11:\ns_lshr_b32 s61, s40, 3\n\ts_add_u32 s61, s61, 32\n\t" \
    "v_readlane_b32 s48, %[tv], s61\n\ts_and_b32 s61, s40, 7\n\ts_lshl_b32 s61, s61, 10\n\ts_add_u32 s48, s48, s61\n\ts_add_u32 s49, s65, %[ax]\n\ts_add_u32 s50, s65, %[ap]\n\t20:\n" \
    "s_memrealtime s[70:71]\n\ts_waitcnt lgkmcnt(0)\n\ts_lshl_b32 s70, s70, 4\n\ts_sub_u32 s70, s49, s70\n\ts_sub_u32 s70, s70, 512\n\ts_cmp_lt_u32 s70, 0x4000\n\ts_cbranch_scc0 21f\n\ts_sleep 1\n\t" \
    "s_branch 20b\n\t21:\ns_memtime s[70:71]\n\ts_memrealtime s[72:73]\n\ts_nop 2\n\ts_memrealtime s[74:75]\n\ts_nop 2\n\ts_memrealtime s[76:77]\n\ts_nop 2\n\ts_memrealtime s[78:79]\n\ts_nop 2\n\t" \
    "s_memrealtime s[80:81]\n\ts_nop 2\n\ts_memrealtime s[82:83]\n\ts_nop 2\n\ts_memrealtime s[84:85]\n\ts_waitcnt lgkmcnt(0)\n\ts_add_u32 s71, s72, 4\n\ts_cmp_eq_u32 s84, s72\n\t" \
    "s_cselect_b32 s71, s71, s84\n\ts_cselect_b32 s73, 16, 0\n\ts_add_u32 s70, s70, s73\n\ts_cmp_eq_u32 s82, s72\n\ts_cselect_b32 s71, s71, s82\n\ts_cselect_b32 s73, 16, 0\n\ts_add_u32 s70, s70, s73\n\t" \
    "s_cmp_eq_u32 s80, s72\n\ts_cselect_b32 s71, s71, s80\n\ts_cselect_b32 s73, 16, 0\n\ts_add_u32 s70, s70, s73\n\ts_cmp_eq_u32 s78, s72\n\ts_cselect_b32 s71, s71, s78\n\ts_cselect_b32 s73, 16, 0\n\t" \
    "s_add_u32 s70, s70, s73\n\ts_cmp_eq_u32 s76, s72\n\ts_cselect_b32 s71, s71, s76\n\ts_cselect_b32 s73, 16, 0\n\ts_add_u32 s70, s70, s73\n\ts_cmp_eq_u32 s74, s72\n\ts_cselect_b32 s71, s71, s74\n\t" \
    "s_cselect_b32 s73, 16, 0\n\ts_add_u32 s70, s70, s73\n\ts_sub_u32 s72, s70, %[pc]\n\ts_sub_u32 s73, s71, %[pr]\n\ts_mov_b32 %[pc], s70\n\ts_mov_b32 %[pr], s71\n\ts_lshl_b32 s72, s72, 4\n\t" \
    "s_mul_i32 s74, s73, %[f]\n\ts_sub_u32 s72, s72, s74\n\ts_ashr_i32 s72, s72, 8\n\ts_cmp_lt_u32 s73, 320\n\ts_cselect_b32 s72, s72, 0\n\ts_add_u32 %[f], %[f], s72\n\ts_max_i32 %[f], %[f], 240\n\t" \
    "s_min_i32 %[f], %[f], 400\n\ts_lshl_b32 s71, s71, 4\n\ts_lshl_b32 s61, s54, 4\n\ts_sub_u32 s61, s65, s61\n\ts_mov_b32 s51, 0\n\ts_mov_b32 s53, 0\n\ts_cmp_lt_i32 s61, 64\n\ts_cbranch_scc1 44f\n\t41:\n" \
    "s_sub_u32 s72, s49, s71\n\ts_mul_i32 s72, s72, %[f]\n\ts_ashr_i32 s72, s72, 8\n\ts_add_u32 s72, s72, s70\n\ts_memtime s[74:75]\n\ts_getpc_b64 s[76:77]\n\t22:\ns_add_u32 s76, s76, 23f-22b\n\t" \
    "s_addc_u32 s77, s77, 0\n\ts_sub_u32 s72, s72, 120\n\ts_waitcnt lgkmcnt(0)\n\ts_sub_u32 s72, s72, s74\n\ts_max_i32 s72, s72, 0\n\ts_min_i32 s72, s72, 640\n\ts_and_b32 s72, s72, -4\n\t" \
    "s_sub_u32 s76, s76, s72\n\ts_subb_u32 s77, s77, 0\n\ts_setpc_b64 s[76:77]\n\t.rept 160\n\ts_nop 0\n\t.endr\n\t23:\ns_add_u32 s61, s41, 0x1000\n\tbuffer_load_dwordx4 %[dv], %[rs], s41 offen sc1 lds\n\t" \
    "buffer_load_dwordx4 %[dv], %[rs], s41 offen offset:1024 sc1 lds\n\tbuffer_load_dwordx4 %[dv], %[rs], s41 offen offset:2048 sc1 lds\n\t" \
    "buffer_load_dwordx4 %[dv], %[rs], s41 offen offset:3072 sc1 lds\n\ts_add_u32 m0, m0, 0x1000\n\ts_nop 0\n\tbuffer_load_dwordx4 %[dv], %[rs], s61 offen sc1 lds\n\t" \
    "buffer_load_dwordx4 %[dv], %[rs], s61 offen offset:1024 sc1 lds\n\tbuffer_load_dwordx4 %[dv], %[rs], s61 offen offset:2048 sc1 lds\n\t" \
    "buffer_load_dwordx4 %[dv], %[rs], s61 offen offset:3072 sc1 lds\n\ts_sub_u32 m0, m0, 0x1000\n\ts_cmp_eq_u32 s40, 63\n\ts_cbranch_scc1 5f\n\ts_sub_u32 s72, s50, s71\n\ts_mul_i32 s72, s72, %[f]\n\t" \
    "s_ashr_i32 s72, s72, 8\n\ts_add_u32 s72, s72, s70\n\ts_memtime s[74:75]\n\ts_getpc_b64 s[76:77]\n\t24:\ns_add_u32 s76, s76, 25f-24b\n\ts_addc_u32 s77, s77, 0\n\ts_sub_u32 s72, s72, 120\n\t" \
    "s_waitcnt lgkmcnt(0)\n\ts_sub_u32 s72, s72, s74\n\ts_max_i32 s72, s72, 0\n\ts_min_i32 s72, s72, 640\n\ts_and_b32 s72, s72, -4\n\ts_sub_u32 s76, s76, s72\n\ts_subb_u32 s77, s77, 0\n\t" \
    "s_setpc_b64 s[76:77]\n\t.rept 160\n\ts_nop 0\n\t.endr\n\t25:\nbuffer_load_dwordx4 v[48:51], %[dv], %[rs], s42 offen sc1\n\tbuffer_load_dwordx4 v[52:55], %[dv], %[rs], s42 offen offset:1024 sc1\n\t" \
    "buffer_load_dwordx4 v[56:59], %[dv], %[rs], s42 offen offset:2048 sc1\n\tbuffer_load_dwordx4 v[60:63], %[dv], %[rs], s42 offen offset:3072 sc1\n\t" \
    "buffer_load_dwordx4 v[64:67], %[dw], %[rs], s42 offen sc1\n\tbuffer_load_dwordx4 v[68:71], %[dw], %[rs], s42 offen offset:1024 sc1\n\t" \
    "buffer_load_dwordx4 v[72:75], %[dw], %[rs], s42 offen offset:2048 sc1\n\tbuffer_load_dwordx4 v[76:79], %[dw], %[rs], s42 offen offset:3072 sc1\n\t" \
    "buffer_load_dwordx4 v[80:83], %[dv], %[rs], s43 offen sc1\n\tbuffer_load_dwordx4 v[84:87], %[dv], %[rs], s43 offen offset:1024 sc1\n\t" \
    "buffer_load_dwordx4 v[88:91], %[dv], %[rs], s43 offen offset:2048 sc1\n\tbuffer_load_dwordx4 v[92:95], %[dv], %[rs], s43 offen offset:3072 sc1\n\t" \
    "buffer_load_dwordx4 v[96:99], %[dw], %[rs], s43 offen sc1\n\tbuffer_load_dwordx4 v[100:103], %[dw], %[rs], s43 offen offset:1024 sc1\n\t" \
    "buffer_load_dwordx4 v[104:107], %[dw], %[rs], s43 offen offset:2048 sc1\n\tbuffer_load_dwordx4 v[108:111], %[dw], %[rs], s43 offen offset:3072 sc1\n\t" \
    "buffer_load_dwordx4 v[112:115], %[dv], %[rs], s44 offen sc1\n\tbuffer_load_dwordx4 v[116:119], %[dv], %[rs], s44 offen offset:1024 sc1\n\t" \
    "buffer_load_dwordx4 v[120:123], %[dv], %[rs], s44 offen offset:2048 sc1\n\tbuffer_load_dwordx4 v[124:127], %[dv], %[rs], s44 offen offset:3072 sc1\n\tds_write_b32 %[fl], v138\n\t" \
    "v_mov_b64 v[128:129], 0\n\tv_mov_b64 v[130:131], 0\n\ts_nop 0\n\ts_waitcnt vmcnt(19)\n\tv_smfmac_f32_16x16x64_bf16 v[128:131], v[48:51], a[0:7], %[ix]\n\tds_read_b128 v[16:19], v137\n\t" \
    "s_waitcnt vmcnt(18)\n\tv_smfmac_f32_16x16x64_bf16 v[128:131], v[52:55], a[8:15], %[ix]\n\tds_read_b128 v[20:23], v137 offset:1024\n\ts_waitcnt vmcnt(17)\n\t" \
    "v_smfmac_f32_16x16x64_bf16 v[128:131], v[56:59], a[16:23], %[ix]\n\tds_read_b128 v[24:27], v137 offset:2048\n\ts_waitcnt vmcnt(16)\n\t" \
    "v_smfmac_f32_16x16x64_bf16 v[128:131], v[60:63], a[24:31], %[ix]\n\tds_read_b128 v[28:31], v137 offset:3072\n\ts_waitcnt vmcnt(15)\n\t" \
    "v_smfmac_f32_16x16x64_bf16 v[128:131], v[64:67], a[32:39], %[ix]\n\tds_read_b128 v[32:35], v137 offset:4096\n\ts_waitcnt vmcnt(14)\n\t" \
    "v_smfmac_f32_16x16x64_bf16 v[128:131], v[68:71], a[40:47], %[ix]\n\tds_read_b128 v[36:39], v137 offset:5120\n\ts_waitcnt vmcnt(13)\n\t" \
    "v_smfmac_f32_16x16x64_bf16 v[128:131], v[72:75], a[48:55], %[ix]\n\tds_read_b128 v[40:43], v137 offset:6144\n\ts_waitcnt vmcnt(12)\n\t" \
    "v_smfmac_f32_16x16x64_bf16 v[128:131], v[76:79], a[56:63], %[ix]\n\tds_read_b128 v[44:47], v137 offset:7168\n\ts_waitcnt vmcnt(11)\n\t" \
    "v_smfmac_f32_16x16x64_bf16 v[128:131], v[80:83], a[64:71], %[ix]\n\ts_waitcnt vmcnt(10)\n\tv_smfmac_f32_16x16x64_bf16 v[128:131], v[84:87], a[72:79], %[ix]\n\ts_waitcnt vmcnt(9)\n\t" \
    "v_smfmac_f32_16x16x64_bf16 v[128:131], v[88:91], a[80:87], %[ix]\n\ts_waitcnt vmcnt(8)\n\tv_smfmac_f32_16x16x64_bf16 v[128:131], v[92:95], a[88:95], %[ix]\n\ts_waitcnt vmcnt(7)\n\t" \
    "v_smfmac_f32_16x16x64_bf16 v[128:131], v[96:99], a[96:103], %[ix]\n\ts_waitcnt vmcnt(6)\n\tv_smfmac_f32_16x16x64_bf16 v[128:131], v[100:103], a[104:111], %[ix]\n\ts_waitcnt vmcnt(5)\n\t" \
    "v_smfmac_f32_16x16x64_bf16 v[128:131], v[104:107], a[112:119], %[ix]\n\ts_waitcnt vmcnt(4)\n\tv_smfmac_f32_16x16x64_bf16 v[128:131], v[108:111], a[120:127], %[ix]\n\ts_waitcnt vmcnt(3) lgkmcnt(6)\n\t" \
    "v_smfmac_f32_16x16x64_bf16 v[128:131], v[112:115], v[16:23], %[ix]\n\ts_waitcnt vmcnt(2) lgkmcnt(4)\n\tv_smfmac_f32_16x16x64_bf16 v[128:131], v[116:119], v[24:31], %[ix]\n\t" \
    "s_waitcnt vmcnt(1) lgkmcnt(2)\n\tv_smfmac_f32_16x16x64_bf16 v[128:131], v[120:123], v[32:39], %[ix]\n\ts_waitcnt vmcnt(0) lgkmcnt(0)\n\t" \
    "v_smfmac_f32_16x16x64_bf16 v[128:131], v[124:127], v[40:47], %[ix]\n\ts_nop 7\n\tv_pk_add_f32 v[128:129], v[128:129], v[130:131]\n\tv_cmp_u_f32 vcc, v128, v129\n\t" \
    "v_cvt_pk_bf16_f32 v130, v128, v129\n\ts_cbranch_vccnz 3f\n\t2:\nbuffer_store_dword v130, %[mo], %[rs], s45 offen sc1\n\tbuffer_store_dword v130, %[mo], %[rs], s46 offen sc1\n\t" \
    "buffer_store_dword v130, %[mo], %[rs], s47 offen sc1\n\tds_write_b32 %[fl], v138 offset:8\n\ts_memrealtime s[54:55]\n\ts_mov_b32 s72, s48\n\ts_buffer_load_dwordx2 s[64:65], %[rs], s72\n\t" \
    "s_add_u32 s40, s40, 1\n\ts_branch 40b\n\t3:\nbuffer_load_dword v139, off, %[rs], s48 sc1\n\tv_max3_u32 v132, v16, v17, v16\n\tv_max3_u32 v132, v18, v19, v132\n\tv_max3_u32 v132, v20, v21, v132\n\t" \
    "v_max3_u32 v132, v22, v23, v132\n\tv_max3_u32 v132, v24, v25, v132\n\tv_max3_u32 v132, v26, v27, v132\n\tv_max3_u32 v132, v28, v29, v132\n\tv_max3_u32 v132, v30, v31, v132\n\t" \
    "v_max3_u32 v132, v32, v33, v132\n\tv_max3_u32 v132, v34, v35, v132\n\tv_max3_u32 v132, v36, v37, v132\n\tv_max3_u32 v132, v38, v39, v132\n\tv_max3_u32 v132, v40, v41, v132\n\t" \
    "v_max3_u32 v132, v42, v43, v132\n\tv_max3_u32 v132, v44, v45, v132\n\tv_max3_u32 v132, v46, v47, v132\n\tv_max3_u32 v132, v48, v49, v132\n\tv_max3_u32 v132, v50, v51, v132\n\t" \
    "v_max3_u32 v132, v52, v53, v132\n\tv_max3_u32 v132, v54, v55, v132\n\tv_max3_u32 v132, v56, v57, v132\n\tv_max3_u32 v132, v58, v59, v132\n\tv_max3_u32 v132, v60, v61, v132\n\t" \
    "v_max3_u32 v132, v62, v63, v132\n\tv_max3_u32 v132, v64, v65, v132\n\tv_max3_u32 v132, v66, v67, v132\n\tv_max3_u32 v132, v68, v69, v132\n\tv_max3_u32 v132, v70, v71, v132\n\t" \
    "v_max3_u32 v132, v72, v73, v132\n\tv_max3_u32 v132, v74, v75, v132\n\tv_max3_u32 v132, v76, v77, v132\n\tv_max3_u32 v132, v78, v79, v132\n\tv_max3_u32 v132, v80, v81, v132\n\t" \
    "v_max3_u32 v132, v82, v83, v132\n\tv_max3_u32 v132, v84, v85, v132\n\tv_max3_u32 v132, v86, v87, v132\n\tv_max3_u32 v132, v88, v89, v132\n\tv_max3_u32 v132, v90, v91, v132\n\t" \
    "v_max3_u32 v132, v92, v93, v132\n\tv_max3_u32 v132, v94, v95, v132\n\tv_max3_u32 v132, v96, v97, v132\n\tv_max3_u32 v132, v98, v99, v132\n\tv_max3_u32 v132, v100, v101, v132\n\t" \
    "v_max3_u32 v132, v102, v103, v132\n\tv_max3_u32 v132, v104, v105, v132\n\tv_max3_u32 v132, v106, v107, v132\n\tv_max3_u32 v132, v108, v109, v132\n\tv_max3_u32 v132, v110, v111, v132\n\t" \
    "v_max3_u32 v132, v112, v113, v132\n\tv_max3_u32 v132, v114, v115, v132\n\tv_max3_u32 v132, v116, v117, v132\n\tv_max3_u32 v132, v118, v119, v132\n\tv_max3_u32 v132, v120, v121, v132\n\t" \
    "v_max3_u32 v132, v122, v123, v132\n\tv_max3_u32 v132, v124, v125, v132\n\tv_max3_u32 v132, v126, v127, v132\n\tv_cmp_eq_u32 vcc, -1, v132\n\tv_min_u32 v130, -2, v130\n\ts_cbranch_vccz 2b\n\t" \
    "s_bitset1_b64 %[fm], s40\n\ts_add_u32 s51, s51, 1\n\ts_cmp_gt_u32 s51, %[cap]\n\ts_cselect_b32 %[cap], 0, %[cap]\n\ts_cbranch_scc1 2b\n\t42:\ns_mov_b32 s61, 0\n\t43:\ns_waitcnt vmcnt(0)\n\t" \
    "v_readfirstlane_b32 s64, v139\n\ts_add_u32 s61, s61, 1\n\ts_cmp_lg_u32 s64, -1\n\ts_cbranch_scc1 45f\n\ts_cmp_gt_u32 s61, 16\n\ts_cbranch_scc1 41b\n\ts_sleep 1\n\t" \
    "buffer_load_dword v139, off, %[rs], s48 sc1\n\ts_branch 43b\n\t44:\nbuffer_load_dword v139, off, %[rs], s48 sc1\n\ts_branch 42b\n\t45:\ns_add_u32 s49, s64, %[ax]\n\ts_add_u32 s50, s64, %[ap]\n\t46:\n" \
    "s_memrealtime s[72:73]\n\ts_waitcnt lgkmcnt(0)\n\ts_lshl_b32 s72, s72, 4\n\ts_sub_u32 s72, s49, s72\n\ts_sub_u32 s72, s72, 320\n\ts_cmp_lt_u32 s72, 0x4000\n\ts_cbranch_scc1 46b\n\ts_branch 41b\n\t4:\n" \
    "s_add_u32 s61, s41, 0x1000\n\tbuffer_load_dwordx4 %[dv], %[rs], s41 offen sc1 lds\n\tbuffer_load_dwordx4 %[dv], %[rs], s41 offen offset:1024 sc1 lds\n\t" \
    "buffer_load_dwordx4 %[dv], %[rs], s41 offen offset:2048 sc1 lds\n\tbuffer_load_dwordx4 %[dv], %[rs], s41 offen offset:3072 sc1 lds\n\ts_add_u32 m0, m0, 0x1000\n\ts_nop 0\n\t" \
    "buffer_load_dwordx4 %[dv], %[rs], s61 offen sc1 lds\n\tbuffer_load_dwordx4 %[dv], %[rs], s61 offen offset:1024 sc1 lds\n\tbuffer_load_dwordx4 %[dv], %[rs], s61 offen offset:2048 sc1 lds\n\t" \
    "buffer_load_dwordx4 %[dv], %[rs], s61 offen offset:3072 sc1 lds\n\ts_sub_u32 m0, m0, 0x1000\n\t5:\nds_write_b32 %[fl], v138\n\ts_waitcnt vmcnt(0)\n\tds_read_b128 v[16:19], v137\n\t" \
    "ds_read_b128 v[20:23], v137 offset:1024\n\tds_read_b128 v[24:27], v137 offset:2048\n\tds_read_b128 v[28:31], v137 offset:3072\n\tds_read_b128 v[32:35], v137 offset:4096\n\t" \
    "ds_read_b128 v[36:39], v137 offset:5120\n\tds_read_b128 v[40:43], v137 offset:6144\n\tds_read_b128 v[44:47], v137 offset:7168\n\ts_waitcnt lgkmcnt(0)\n\tv_max3_u32 v132, v16, v17, v16\n\t" \
    "v_max3_u32 v132, v18, v19, v132\n\tv_max3_u32 v132, v20, v21, v132\n\tv_max3_u32 v132, v22, v23, v132\n\tv_max3_u32 v132, v24, v25, v132\n\tv_max3_u32 v132, v26, v27, v132\n\t" \
    "v_max3_u32 v132, v28, v29, v132\n\tv_max3_u32 v132, v30, v31, v132\n\tv_max3_u32 v132, v32, v33, v132\n\tv_max3_u32 v132, v34, v35, v132\n\tv_max3_u32 v132, v36, v37, v132\n\t" \
    "v_max3_u32 v132, v38, v39, v132\n\tv_max3_u32 v132, v40, v41, v132\n\tv_max3_u32 v132, v42, v43, v132\n\tv_max3_u32 v132, v44, v45, v132\n\tv_max3_u32 v132, v46, v47, v132\n\t" \
    "v_cmp_eq_u32 vcc, -1, v132\n\ts_nop 0\n\ts_cbranch_vccz 9f\n\ts_bitset1_b64 %[fm], s40\n\ts_add_u32 s51, s51, 1\n\ts_cmp_gt_u32 s51, %[cap]\n\ts_cselect_b32 %[cap], 0, %[cap]\n\ts_cbranch_scc1 9f\n\t" \
    "s_branch 44b\n\t9:\nds_write_b32 %[fl], v138 offset:8\n\ts_mov_b32 m0, s60\n\ts_setprio 0\n\ts_waitcnt lgkmcnt(0)\n\t"
#define ASM_F32 \
    "s_mov_b32 s60, m0\n\ts_mov_b32 m0, %[lx]\n\ts_mov_b32 s61, %[so]\n\tbuffer_load_dwordx4 %[dv], %[rx], s61 offen sc1 lds\n\tbuffer_load_dwordx4 %[dv], %[rx], s61 offen offset:1024 sc1 lds\n\t" \
    "buffer_load_dwordx4 %[dv], %[rx], s61 offen offset:2048 sc1 lds\n\tbuffer_load_dwordx4 %[dv], %[rx], s61 offen offset:3072 sc1 lds\n\ts_add_u32 m0, m0, 0x1000\n\ts_add_u32 s61, s61, 0x1000\n\t" \
    "buffer_load_dwordx4 %[dv], %[rx], s61 offen sc1 lds\n\tbuffer_load_dwordx4 %[dv], %[rx], s61 offen offset:1024 sc1 lds\n\tbuffer_load_dwordx4 %[dv], %[rx], s61 offen offset:2048 sc1 lds\n\t" \
    "buffer_load_dwordx4 %[dv], %[rx], s61 offen offset:3072 sc1 lds\n\ts_add_u32 m0, m0, 0x1000\n\ts_add_u32 s61, s61, 0x1000\n\tbuffer_load_dwordx4 %[dv], %[rx], s61 offen sc1 lds\n\t" \
    "buffer_load_dwordx4 %[dv], %[rx], s61 offen offset:1024 sc1 lds\n\tbuffer_load_dwordx4 %[dv], %[rx], s61 offen offset:2048 sc1 lds\n\t" \
    "buffer_load_dwordx4 %[dv], %[rx], s61 offen offset:3072 sc1 lds\n\ts_add_u32 m0, m0, 0x1000\n\ts_add_u32 s61, s61, 0x1000\n\tbuffer_load_dwordx4 %[dv], %[rx], s61 offen sc1 lds\n\t" \
    "buffer_load_dwordx4 %[dv], %[rx], s61 offen offset:1024 sc1 lds\n\tbuffer_load_dwordx4 %[dv], %[rx], s61 offen offset:2048 sc1 lds\n\t" \
    "buffer_load_dwordx4 %[dv], %[rx], s61 offen offset:3072 sc1 lds\n\ts_add_u32 m0, m0, 0x1000\n\ts_add_u32 s61, s61, 0x1000\n\tbuffer_load_dwordx4 %[dv], %[rx], s61 offen sc1 lds\n\t" \
    "buffer_load_dwordx4 %[dv], %[rx], s61 offen offset:1024 sc1 lds\n\tbuffer_load_dwordx4 %[dv], %[rx], s61 offen offset:2048 sc1 lds\n\t" \
    "buffer_load_dwordx4 %[dv], %[rx], s61 offen offset:3072 sc1 lds\n\ts_add_u32 m0, m0, 0x1000\n\ts_add_u32 s61, s61, 0x1000\n\tbuffer_load_dwordx4 %[dv], %[rx], s61 offen sc1 lds\n\t" \
    "buffer_load_dwordx4 %[dv], %[rx], s61 offen offset:1024 sc1 lds\n\tbuffer_load_dwordx4 %[dv], %[rx], s61 offen offset:2048 sc1 lds\n\t" \
    "buffer_load_dwordx4 %[dv], %[rx], s61 offen offset:3072 sc1 lds\n\ts_add_u32 m0, m0, 0x1000\n\ts_add_u32 s61, s61, 0x1000\n\tbuffer_load_dwordx4 %[dv], %[rx], s61 offen sc1 lds\n\t" \
    "buffer_load_dwordx4 %[dv], %[rx], s61 offen offset:1024 sc1 lds\n\tbuffer_load_dwordx4 %[dv], %[rx], s61 offen offset:2048 sc1 lds\n\t" \
    "buffer_load_dwordx4 %[dv], %[rx], s61 offen offset:3072 sc1 lds\n\ts_add_u32 m0, m0, 0x1000\n\ts_add_u32 s61, s61, 0x1000\n\tbuffer_load_dwordx4 %[dv], %[rx], s61 offen sc1 lds\n\t" \
    "buffer_load_dwordx4 %[dv], %[rx], s61 offen offset:1024 sc1 lds\n\tbuffer_load_dwordx4 %[dv], %[rx], s61 offen offset:2048 sc1 lds\n\t" \
    "buffer_load_dwordx4 %[dv], %[rx], s61 offen offset:3072 sc1 lds\n\ts_mov_b32 m0, s60\n\t"
#define ASM_F16 \
    "s_mov_b32 s60, m0\n\ts_mov_b32 m0, %[lx]\n\ts_mov_b32 s61, %[so]\n\tbuffer_load_dwordx4 %[dv], %[rx], s61 offen sc1 lds\n\tbuffer_load_dwordx4 %[dv], %[rx], s61 offen offset:1024 sc1 lds\n\t" \
    "buffer_load_dwordx4 %[dv], %[rx], s61 offen offset:2048 sc1 lds\n\tbuffer_load_dwordx4 %[dv], %[rx], s61 offen offset:3072 sc1 lds\n\ts_add_u32 m0, m0, 0x1000\n\ts_add_u32 s61, s61, 0x1000\n\t" \
    "buffer_load_dwordx4 %[dv], %[rx], s61 offen sc1 lds\n\tbuffer_load_dwordx4 %[dv], %[rx], s61 offen offset:1024 sc1 lds\n\tbuffer_load_dwordx4 %[dv], %[rx], s61 offen offset:2048 sc1 lds\n\t" \
    "buffer_load_dwordx4 %[dv], %[rx], s61 offen offset:3072 sc1 lds\n\ts_add_u32 m0, m0, 0x1000\n\ts_add_u32 s61, s61, 0x1000\n\tbuffer_load_dwordx4 %[dv], %[rx], s61 offen sc1 lds\n\t" \
    "buffer_load_dwordx4 %[dv], %[rx], s61 offen offset:1024 sc1 lds\n\tbuffer_load_dwordx4 %[dv], %[rx], s61 offen offset:2048 sc1 lds\n\t" \
    "buffer_load_dwordx4 %[dv], %[rx], s61 offen offset:3072 sc1 lds\n\ts_add_u32 m0, m0, 0x1000\n\ts_add_u32 s61, s61, 0x1000\n\tbuffer_load_dwordx4 %[dv], %[rx], s61 offen sc1 lds\n\t" \
    "buffer_load_dwordx4 %[dv], %[rx], s61 offen offset:1024 sc1 lds\n\tbuffer_load_dwordx4 %[dv], %[rx], s61 offen offset:2048 sc1 lds\n\t" \
    "buffer_load_dwordx4 %[dv], %[rx], s61 offen offset:3072 sc1 lds\n\ts_mov_b32 m0, s60\n\t"
#define ASM_F8 \
    "s_mov_b32 s60, m0\n\ts_mov_b32 m0, %[lx]\n\ts_mov_b32 s61, %[so]\n\tbuffer_load_dwordx4 %[dv], %[rx], s61 offen sc1 lds\n\tbuffer_load_dwordx4 %[dv], %[rx], s61 offen offset:1024 sc1 lds\n\t" \
    "buffer_load_dwordx4 %[dv], %[rx], s61 offen offset:2048 sc1 lds\n\tbuffer_load_dwordx4 %[dv], %[rx], s61 offen offset:3072 sc1 lds\n\ts_add_u32 m0, m0, 0x1000\n\ts_add_u32 s61, s61, 0x1000\n\t" \
    "buffer_load_dwordx4 %[dv], %[rx], s61 offen sc1 lds\n\tbuffer_load_dwordx4 %[dv], %[rx], s61 offen offset:1024 sc1 lds\n\tbuffer_load_dwordx4 %[dv], %[rx], s61 offen offset:2048 sc1 lds\n\t" \
    "buffer_load_dwordx4 %[dv], %[rx], s61 offen offset:3072 sc1 lds\n\ts_mov_b32 m0, s60\n\t"
#define ASM_ANCHOR \
    "s_mov_b32 s72, %[ab]\n\ts_buffer_load_dwordx2 s[64:65], %[rs], s72\n\ts_add_u32 s73, %[ab], 256\n\t10:\ns_waitcnt lgkmcnt(0)\n\ts_cmp_lg_u32 s64, -1\n\ts_cbranch_scc1 11f\n\ts_add_u32 s72, s72, 64\n\t" \
    "s_buffer_load_dwordx2 s[64:65], %[rs], s72\n\ts_cmp_lg_u32 s72, s73\n\ts_cbranch_scc1 10b\n\ts_waitcnt lgkmcnt(0)\n\ts_cmp_lg_u32 s64, -1\n\ts_cbranch_scc1 11f\n\ts_memrealtime s[72:73]\n\t" \
    "s_waitcnt lgkmcnt(0)\n\ts_lshl_b32 s64, s72, 4\n\ts_sub_u32 s64, s64, 0x8000\n\ts_mov_b32 s65, s64\n\t11:\ns_mov_b32 %[aw], s65\n\t"
#define C10(p, a) p #a "0", p #a "1", p #a "2", p #a "3", p #a "4", p #a "5", p #a "6", p #a "7", p #a "8", p #a "9"
#define CV16_99 "v16", "v17", "v18", "v19", C10("v", 2), C10("v", 3), C10("v", 4), C10("v", 5), C10("v", 6), C10("v", 7), C10("v", 8), C10("v", 9)
#define CV100_199 C10("v", 10), C10("v", 11), C10("v", 12), C10("v", 13), C10("v", 14), C10("v", 15), C10("v", 16), C10("v", 17), C10("v", 18), C10("v", 19)
#define CA0_99 "a0", "a1", "a2", "a3", "a4", "a5", "a6", "a7", "a8", "a9", C10("a", 1), C10("a", 2), C10("a", 3), C10("a", 4), C10("a", 5), C10("a", 6), C10("a", 7), C10("a", 8), C10("a", 9)
#define CA100_199 C10("a", 10), C10("a", 11), C10("a", 12), C10("a", 13), C10("a", 14), C10("a", 15), C10("a", 16), C10("a", 17), C10("a", 18), C10("a", 19)
#define CS60_69 C10("s", 6)
#define WOUT(w) "=&{a[0:31]}"(w[0]), "=&{a[32:63]}"(w[1]), "=&{a[64:95]}"(w[2]), "=&{a[96:127]}"(w[3]), "=&{a[128:159]}"(w[4]), "=&{a[160:191]}"(w[5]), "=&{a[192:223]}"(w[6]), "=&{a[224:255]}"(w[7])
#define WIN(w) "{a[0:31]}"(w[0]), "{a[32:63]}"(w[1]), "{a[64:95]}"(w[2]), "{a[96:127]}"(w[3]), "{a[128:159]}"(w[4]), "{a[160:191]}"(w[5]), "{a[192:223]}"(w[6]), "{a[224:255]}"(w[7])
__device__ __forceinline__ int lane_id() { int l = __builtin_amdgcn_mbcnt_hi(~0u, __builtin_amdgcn_mbcnt_lo(~0u, 0u)); asm volatile("" : "+v"(l)); return l; }

#define XIMG(n) ((((unsigned)(n) + 3u) & 3u) * 32768u + 32768u)
template <bool PUB>
__device__ __forceinline__ void proj_ws(int kind, const bf16* __restrict__ A, const bf16* __restrict__ W, bf16* __restrict__ out, int c0, int cs,
                                        unsigned* prog, const Hello& hi, unsigned lbase, int wid) {
    const int lane = lane_id(), l16 = lane & 15, g4 = lane >> 4;
    const u32x4 rsA = mk_rs(A, SEQ * DM * 2);
    const unsigned dvo = (unsigned)((wid * 8 + (lane >> 3)) * 1024 + ((lane & 7) ^ ((lane >> 3) & 6)) * 16);
    auto fetch = [&](int ch, int n) {
#pragma unroll
        for (int i = 0; i < 8; ++i) dma16<false>(rsA, dvo, (unsigned)(ch * 32768 + i * 128), lbase + XIMG(n) + (8 * wid + i) * 1024); };
    fetch(c0, 0);
    u32x32 w[8];
    {
        const unsigned a0 = lbase + wid * 4096 + g4 * 1024 + (l16 >> 2) * 128 + ((l16 >> 3) * 4 + (l16 & 3)) * 16;
        asm volatile(ASM_STAGE_PANEL : WOUT(w)
                     : [rw] "s"(mk_rs(W, DM * HD * 2)), [wv] "v"((unsigned)((lane >> 3) * (HD * 2) + wid * 128 + ((lane & 7) ^ ((lane >> 2) & 4)) * 16)),
                       [lb] "s"(pin_s(lbase + wid * 4096)), [a0] "v"(a0), [a1] "v"(a0 ^ 64u)
                     : "memory", "scc", "v16", "v17", CS60_69); }
    if (PUB && wid == 0) hi.wait(hi.peek());
    __builtin_amdgcn_s_barrier(); asm volatile("" ::: "memory");
    fetch(c0 + cs, 1); fetch(c0 + 2 * cs, 2);
    unsigned sp = (unsigned)(c0 - cs) << 18, so = (unsigned)(c0 + 3 * cs) << 15;
#define PROJ_OPS : [sp] "+s"(sp), [so] "+s"(so) \
                 : WIN(w), [xb] "v"(lbase + 32768 + (l16 >> 3) * 8192 + (l16 & 7) * 128 + (g4 ^ (l16 & 6)) * 16), [lb] "s"(pin_s(lbase + 32768 + wid * 8192)), \
                   [co] "v"((unsigned)(wid * 4096 + lane * 16)), [rc] "s"(mk_rs(out, SEQ * HD * 2)), [ra] "s"(rsA), [dv] "v"(dvo), [nn] "s"(NSEG / cs), \
                   [c18] "s"(cs << 18), [c15] "s"(cs << 15), [sc] "s"(C2), [em] "s"(__builtin_amdgcn_ballot_w64(wid + lane == 0)), [pg] "s"(prog) \
                 : "memory", "scc", CV16_99, CS60_69
    if (PUB) { if (kind == 0) asm volatile(ASM_PROJP_Q PROJ_OPS); else asm volatile(ASM_PROJP_K PROJ_OPS); }
    else if (kind == 0) asm volatile(ASM_PROJ_Q PROJ_OPS); else if (kind == 1) asm volatile(ASM_PROJ_K PROJ_OPS); else asm volatile(ASM_PROJ_V PROJ_OPS);
    VMWAIT(0);
    __builtin_amdgcn_s_barrier(); asm volatile("" ::: "memory");
    if (PUB && threadIdx.x == 0) __hip_atomic_store(prog, (unsigned)NSEG, __ATOMIC_RELAXED, __HIP_MEMORY_SCOPE_AGENT);
}

__device__ __forceinline__ f32x4 mfma32(u32x4 a, u32x4 b, f32x4 c) {
    return __builtin_amdgcn_mfma_f32_16x16x32_bf16(__builtin_bit_cast(bf16x8, a), __builtin_bit_cast(bf16x8, b), c, 0, 0, 0); }
__device__ __forceinline__ float xg_max(float x) {
    { auto r = __builtin_amdgcn_permlane16_swap(__float_as_uint(x), __float_as_uint(x), false, false); x = fmaxf(__uint_as_float(r[0]), __uint_as_float(r[1])); }
    { auto r = __builtin_amdgcn_permlane32_swap(__float_as_uint(x), __float_as_uint(x), false, false); x = fmaxf(__uint_as_float(r[0]), __uint_as_float(r[1])); }
    return x; }
__device__ __forceinline__ float xg_sum(float x) {
    { auto r = __builtin_amdgcn_permlane16_swap(__float_as_uint(x), __float_as_uint(x), false, false); x = __uint_as_float(r[0]) + __uint_as_float(r[1]); }
    { auto r = __builtin_amdgcn_permlane32_swap(__float_as_uint(x), __float_as_uint(x), false, false); x = __uint_as_float(r[0]) + __uint_as_float(r[1]); }
    return x; }
constexpr int AUX_SC1 = 16;
namespace attn {
__device__ __forceinline__ void careful_rows(int h, int qb, char* qkv, int wid) {
    const int lane = lane_id(), l16 = lane & 15, g4 = lane >> 4;
    const __amdgpu_buffer_rsrc_t rs = __builtin_amdgcn_make_buffer_rsrc((void*)qkv, 0, (int)(4 * SZ_ACT), 0x00020000);
    auto ld = [&](unsigned so, int o) { return __builtin_amdgcn_raw_buffer_load_b128(rs, lane * 16 + o, so, AUX_SC1); };
#pragma unroll 1
    for (int t = 0; t < 4; ++t) {
        const int rt = qb * 16 + 4 * t + wid;
        const unsigned sq = (unsigned)((rt >> 1) * 262144 + h * 8192 + (rt & 1) * 1024);
        u32x4 q[4];
#pragma unroll
        for (int ks = 0; ks < 4; ++ks) q[ks] = ld(sq, ks * 2048);
        f32x4 o[8] = {}; float m = -__builtin_inff(), l = 0.f;
#pragma unroll 1
        for (int c = 0; c <= (rt >> 1); ++c) {
            const unsigned sk = (unsigned)(SZ_ACT + c * 262144 + h * 8192);
            f32x4 s[2] = {};
#pragma unroll
            for (int j = 0; j < 8; ++j) s[j & 1] = mfma32(ld(sk, j * 1024), q[j >> 1], s[j & 1]);
            float mx = m;
#pragma unroll
            for (int j = 0; j < 8; ++j) { if (c * 32 + (j >> 2) * 16 + 4 * g4 + (j & 3) > rt * 16 + l16) s[j >> 2][j & 3] = -__builtin_inff(); mx = fmaxf(mx, s[j >> 2][j & 3]); }
            mx = xg_max(mx);
            const float al = __builtin_amdgcn_exp2f(m - mx);
            m = mx; l *= al;
#pragma unroll
            for (int j = 0; j < 8; ++j) { s[j >> 2][j & 3] = __builtin_amdgcn_exp2f(s[j >> 2][j & 3] - mx); l += s[j >> 2][j & 3]; }
            const u32x4 p = {cvtpk(s[0][0], s[0][1]), cvtpk(s[0][2], s[0][3]), cvtpk(s[1][0], s[1][1]), cvtpk(s[1][2], s[1][3])};
#pragma unroll
            for (int j = 0; j < 8; ++j) o[j] = mfma32(ld(sk + (unsigned)SZ_ACT, j * 1024), p, o[j] * al);
        }
        const float rl = __builtin_amdgcn_rcpf(xg_sum(l));
#pragma unroll
        for (int ks = 0; ks < 4; ++ks) { const f32x4 a = o[2 * ks] * rl, b = o[2 * ks + 1] * rl;
            __builtin_amdgcn_raw_buffer_store_b128(u32x4{cvtpk(a[0], a[1]), cvtpk(a[2], a[3]), cvtpk(b[0], b[1]), cvtpk(b[2], b[3])}, rs, lane * 16 + ks * 2048, sq + (unsigned)(3 * SZ_ACT), AUX_SC1); }
    }
}
__device__ __forceinline__ void listed(unsigned* list, int b, unsigned c) {
    if (c != (b < 24 ? 5u : 6u)) return;
    list += (b >> 3) * 64;
    const unsigned p = __hip_atomic_fetch_add(list, 1u, __ATOMIC_RELAXED, __HIP_MEMORY_SCOPE_AGENT);
    __hip_atomic_fetch_add(list + 32, ((unsigned)(b & 7) + 1u) << (p * 4), __ATOMIC_RELAXED, __HIP_MEMORY_SCOPE_AGENT); }
__device__ __forceinline__ int pick(unsigned v, int own) {
    int best = 8, k = -1;
#pragma unroll
    for (int i = 0; i < 5; ++i) { const int c = (int)__builtin_amdgcn_readlane(v, i), lv = min(3 + (i < 3 ? c / 6 : c / 7), 8);
        if (c < (i < 3 ? 30 : 35) && (lv < best || (lv == best && i == own))) { best = lv; k = i; } }
    return k; }
__device__ __forceinline__ void causal_block(int h, int qb, char* qkv, unsigned lbase, int wid, unsigned* tmo, int own, int prev, int& nx, unsigned& pa) {
    const int lane = lane_id(), pz = max(prev, 0);
    unsigned long long bad;
    asm volatile(ASM_ATTN : [bad] "=&s"(bad), [nx] "=&s"(nx), [pa] "=&s"(pa)
                 : [rs] "s"(mk_rs(qkv, (unsigned)(4 * SZ_ACT))), [sk] "s"(pin_s((unsigned)SZ_ACT + h * 8192 + wid * 2048)), [sq] "s"(pin_s((qb * 8 + (wid >> 1)) * 262144 + h * 8192 + (wid & 1) * 1024)),
                   [lb] "s"(pin_s(lbase + wid * 2048)), [qb] "s"(qb), [dv] "v"(lane * 16), [dw] "v"(lane * 16 + 4096), [ka] "v"(lbase + lane * 16), [md] "v"(16 * wid + (lane & 15) - 4 * (lane >> 4)),
                   [ep] "s"(__builtin_amdgcn_ballot_w64(wid + lane == 0 && prev >= 0)), [em] "s"(__builtin_amdgcn_ballot_w64(wid + lane == 0)), [qc] "s"(tmo + W_QCNT + pz * 32), [tc] "s"(prev < 24 ? 5u : 6u),
                   [li] "s"(tmo + W_LIST + (pz >> 3) * 64), [le] "s"((pz & 7) + 1), [qu] "s"(tmo + W_AQ + own * 32), [ca] "s"(tmo + W_CTRA), [lq] "v"(lbase + 147456)
                 : "memory", "scc", "vcc", CV16_99, CV100_199, C10("v", 20), "v210", "v211", "v212", "v213", CA0_99, CA100_199, CS60_69);
    if (__builtin_expect(bad != 0, 0)) { VMWAIT(0); careful_rows(h, qb, qkv, wid); }
}
__device__ __forceinline__ void attn_phase(char* qkv, int own, int k, int L, unsigned* tmo, unsigned lbase, int wid) {
    int prev = -1;
    unsigned* const queue = tmo + W_AQ;
    volatile LAS int* lq = (volatile LAS int*)(size_t)(lbase + 147472);
#pragma unroll 1
    for (;;) {
        if (L < 0) {
            if (wid == 0) {
                const int lane = lane_id();
                if (lane == 0 && prev >= 0) listed(tmo + W_LIST, prev, __hip_atomic_fetch_add(tmo + W_QCNT + prev * 32, 1u, __ATOMIC_RELAXED, __HIP_MEMORY_SCOPE_AGENT));
                wave_wait<8>(queue + 160, NPA, tmo, 0x30000u, 0xFFFFu);
                for (;;) {
                    k = pick(__hip_atomic_load(queue + min(lane, 5) * 32, __ATOMIC_RELAXED, __HIP_MEMORY_SCOPE_AGENT), own);
                    if (k < 0) break;
                    if (lane == 0) L = (int)__hip_atomic_fetch_add(queue + k * 32, 1u, __ATOMIC_RELAXED, __HIP_MEMORY_SCOPE_AGENT);
                    L = __builtin_amdgcn_readfirstlane(L);
                    if (L < (k < 3 ? 30 : 35)) break; }
                if (k < 0) wave_wait<8>(queue + 160, 32u << 16, tmo, 0x30002u);
                if (lane == 0) { lq[0] = L; lq[1] = k; } }
            prev = -1;
            LDS_BARRIER();
            k = __builtin_amdgcn_readfirstlane(lq[1]);
            if (k < 0) return;
            L = __builtin_amdgcn_readfirstlane(lq[0]) + (k < 3 ? 18 : 21);
        }
        const int r = k < 3 ? L / 6 : L / 7;
        int nx; unsigned pa;
        causal_block(k < 3 ? 6 * (k - r) + L : 7 * (k - r) - 3 + L, 7 - r, qkv, lbase, wid, tmo, own, prev, nx, pa);
        if ((pa & 0xFFFFu) < NPA && wid == 0) wave_wait<8>(queue + 160, NPA, tmo, 0x30003u, 0xFFFFu);
        VMWAIT(0);
        LDS_BARRIER();
        prev = 8 * k + 7 - r;
        k = own;
        L = nx < (k < 3 ? 30 : 35) ? nx + (k < 3 ? 18 : 21) : -1;
    }
}
}

constexpr int CHAIN_LDS = 163840;
__device__ __forceinline__ unsigned canon(unsigned w) { return min(w, 0xFFFFFFFEu); }
__device__ __forceinline__ void until(unsigned t0, unsigned dt) {
    for (;;) { const unsigned e = (unsigned)__builtin_amdgcn_s_memtime() - t0; if (e >= dt) break; if (dt - e > 160u) __builtin_amdgcn_s_sleep(1); } }
__device__ __forceinline__ void cnt_add(unsigned* w) { __hip_atomic_fetch_add(w, 1u, __ATOMIC_RELAXED, __HIP_MEMORY_SCOPE_AGENT); }
__device__ __forceinline__ void lds_wait(unsigned addr, int target, unsigned* tmo, unsigned code) {
    for (unsigned spins = 0; __builtin_amdgcn_readfirstlane(*(volatile LAS int*)(size_t)addr) < target; ++spins) {
        if ((spins & 1023u) == 1023u && bail(spins, SPIN_CAP << 4, tmo, code)) break; }
    asm volatile("" ::: "memory"); }
#define FETCH(N, lds_, rs_, so_) asm volatile(ASM_F##N :: [lx] "s"(pin_s(lds_)), [so] "s"(pin_s(so_)), [dv] "v"(lane * 16), [rx] "s"(rs_) : "memory", "scc", "s60", "s61")
__device__ __forceinline__ unsigned prog_look(unsigned* w, int lane) { return __hip_atomic_load(w + (lane & 31) * 32, __ATOMIC_RELAXED, __HIP_MEMORY_SCOPE_AGENT); }
__device__ __forceinline__ unsigned prog_min(unsigned v) {
    unsigned m = 0;
#pragma unroll
    for (unsigned b = 64; b; b >>= 1) if (__builtin_amdgcn_ballot_w64(v < (m | b)) == 0) m |= b;
    return m; }
template <int SLP>
__device__ __forceinline__ void prog_wait(unsigned& m, unsigned* w, int lane, unsigned target, unsigned* tmo, unsigned code) {
    for (unsigned spins = 0; m < target; ++spins) {
        if (spins) __builtin_amdgcn_s_sleep(SLP);
        m = prog_min(prog_look(w, lane));
        if (bail(spins, SPIN_CAP << 1, tmo, code)) break; } }
__device__ __forceinline__ void lds_set(unsigned addr, int v) { asm volatile("s_waitcnt lgkmcnt(0)" ::: "memory"); *(volatile LAS int*)(size_t)addr = v; }
#define GST(i) do {} while (0)
#define FAILED(x) do {} while (0)
#define FAILOUT(base) do {} while (0)

__device__ __forceinline__ void oproj_ws(int kq, int nq, unsigned* queue, const bf16* __restrict__ src, const bf16* __restrict__ W, float* __restrict__ part,
                                         unsigned* tmo, unsigned lbase, int wid) {
    const int lane = lane_id();
    volatile LAS unsigned* lf = (volatile LAS unsigned*)(size_t)(lbase + 140032);
    unsigned* const list = tmo + W_LIST + 64 * kq + 32, * const fin = tmo + W_FA;
    const int hb = kq < 3 ? 6 * kq : 7 * kq - 3;
    const u32x4 rsA = mk_rs(src, (unsigned)SZ_ACT), rsC = mk_rs(part + (size_t)kq * SEQ * DM, SEQ * DM * 4);
    const unsigned co = (unsigned)((nq * 8 + 2 * wid) * 2048 + lane * 16), sb = pin_s(hb * 8192 + wid * 2048);
    const unsigned long long em = __builtin_amdgcn_ballot_w64(wid + lane == 0);
    auto fetch = [&](int ch, unsigned im) {
#pragma unroll
        for (int i = 0; i < 14; ++i) if (i < 12 || kq >= 3)
            dma16<true>(rsA, (unsigned)lane * 16u, (unsigned)(ch * 262144 + (i >> 1) * 8192 + (i & 1) * 1024) + sb, lbase + im * 65536 + (i >> 1) * 8192 + wid * 2048 + (i & 1) * 1024); };
    int ch = 0, last = -1, dr = -1;
    unsigned pos = 0, e = 0, im = 1;
    u32x32 w[7];
    {   unsigned c = 0;
        asm volatile(ASM_OPRE : "=&{a[0:31]}"(w[0]), "=&{a[32:63]}"(w[1]), "=&{a[64:95]}"(w[2]), "=&{a[96:127]}"(w[3]), "=&{a[128:159]}"(w[4]), "=&{a[160:191]}"(w[5]), "=&{a[192:223]}"(w[6]),
                       [c] "+v"(c), [pos] "=&s"(pos), [e] "=&s"(e), [nx] "=&s"(ch)
                     : [em] "s"(em), [ep] "s"(0ull), [qu] "s"(queue), [li] "s"(list), [qc] "s"(tmo + W_QCNT),
                       [wb] "s"(pin_s(((nq * 8 + 2 * wid) * 128 + hb * 4) * 1024)), [rw] "s"(mk_rs(W, HD * DM * 2)), [dv] "v"((unsigned)lane * 16u),
                       [fr] "v"(lbase + 140040), [lb] "s"(pin_s(lbase + 65536 + wid * 2048)), [sb] "s"(sb), [kq] "s"(kq), [ra] "s"(rsA)
                     : "memory", "scc", "v64", "v65", "v68", "v69", CS60_69); }
#pragma unroll 1
    for (;; im ^= 1u) {
        if (e == 0u) {
            VMWAIT(0);
            LDS_BARRIER();
            if (threadIdx.x == 0) { if (dr >= 0) cnt_add(fin + (dr >> 2)); if (last >= 0) cnt_add(fin + (last >> 2)); }
            dr = -1; last = -1;
            if (pos >= (unsigned)NSEG) break;
            if (wid == 0) {
                const unsigned lw = wave_wait<20>(list, 1u, tmo, 0x60000u + pos, 15u << ((pos >> 3) * 4));
                if (lane == 0) lf[im * 2 + 1] = lw; }
            LDS_BARRIER();
            ch = (int)(((__builtin_amdgcn_readfirstlane(lf[im * 2 + 1]) >> ((pos >> 3) * 4)) - 1u) & 7u) * 8 + (int)(pos & 7u);
            fetch(ch, im);
            VMWAIT(0);
        }
        __builtin_amdgcn_s_barrier(); asm volatile("" ::: "memory");
        unsigned nx;
        asm volatile(ASM_OPROJ : [pos] "=&s"(pos), [e] "=&s"(e), [nx] "=&s"(nx)
                     : "{a[0:31]}"(w[0]), "{a[32:63]}"(w[1]), "{a[64:95]}"(w[2]), "{a[96:127]}"(w[3]), "{a[128:159]}"(w[4]), "{a[160:191]}"(w[5]), "{a[192:223]}"(w[6]),
                       [xa] "v"(lbase + im * 65536 + lane * 16), [fr] "v"(lbase + 140032 + im * 8), [fw] "v"(lbase + 140040 - im * 8),
                       [em] "s"(em), [dr] "s"(dr), [fin] "s"(fin), [qu] "s"(queue), [li] "s"(list),
                       [lb] "s"(pin_s(lbase + 65536 - im * 65536 + wid * 2048)), [sb] "s"(sb), [ch] "s"(ch), [kq] "s"(kq), [dv] "v"((unsigned)lane * 16u),
                       [ra] "s"(rsA), [co] "v"(co), [rc] "s"(rsC)
                     : "memory", "scc", "vcc", CV16_99, CS60_69);
        dr = last; last = ch * 128; ch = (int)nx;
    }
}
__device__ __forceinline__ unsigned slot_off(unsigned j, unsigned pv) {
    const unsigned o = j * 16384u; return o + (((pv >> (((pv & 0x1FC000u) + o) >> 21)) & 1u) << 13); }
__device__ __forceinline__ unsigned calibrate(__amdgpu_buffer_rsrc_t rs, unsigned pg, unsigned pv0, unsigned* dbg) {
    const unsigned first = pg == 0 ? 0u : (pg << 21) - pv0;
    if (first + 16384u > (unsigned)SZ_RING) return 0u;
    unsigned best[2] = {~0u, ~0u};
#pragma unroll 1
    for (int i = 0; i < CALN; ++i) {
#pragma unroll
        for (int b = 0; b < 2; ++b) {
            const unsigned long long t0 = __builtin_amdgcn_s_memtime();
            asm volatile("s_waitcnt lgkmcnt(0)" ::: "memory");
            unsigned v = __builtin_amdgcn_raw_buffer_load_b32(rs, first + b * 8192u + (unsigned)i * 384u, 0, AUX_SC1);
            asm volatile("s_waitcnt vmcnt(0)" : "+v"(v) :: "memory");
            const unsigned long long t1 = __builtin_amdgcn_s_memtime();
            asm volatile("s_waitcnt lgkmcnt(0)" ::: "memory");
            best[b] = min(best[b], (unsigned)(t1 - t0));
        }
    }
    return best[1] < best[0] ? 1u : 0u;
}
__host__ __device__ __forceinline__ void role_map(int b, int& xg, int& jb) {
    const int g = (GMAP >> ((b & 7) * 4)) & 7, j = b >> 3;
    if (g >= 3) { xg = 3; jb = j * 5 + g - 3; }
    else { xg = (j + g) % 3; jb = j; if (j == 0 && xg != 1) xg = 2 - xg; }
}
#define WO_OPS(W) [ro] "s"(mk_rs(W, DH * DM * 2)), [vo] "v"((unsigned)(g4 * 1024 + wid * 256 + (l16 ^ (2 * g4)) * 16)), \
                  [b0] "v"(b0), [b1] "v"(b0 ^ 32u), [b2] "v"(b0 ^ 64u), [b3] "v"(b0 ^ 96u), [lb] "s"(pin_s(lbase + wid * 4096))
#define WO_B0 (lbase + wid * 4096 + g4 * 1024 + (l16 >> 2) * 288 + (l16 & 3) * 8)
__device__ __forceinline__ void vox_ws(const bf16* __restrict__ x, const bf16* __restrict__ Wv, const bf16* __restrict__ Wo, int h,
                                       u32x4 rsWu, unsigned* done, const Hello& hi, unsigned lbase, int wid) {
    const int lane = lane_id(), l16 = lane & 15, g4 = lane >> 4;
    const u32x4 rsA = mk_rs(x, SEQ * DM * 2);
    const unsigned dvo = (unsigned)((wid * 8 + (lane >> 3)) * 1024 + ((lane & 7) ^ ((lane >> 3) & 6)) * 16);
    auto fetch = [&](int n) {
#pragma unroll
        for (int i = 0; i < 8; ++i) dma16<false>(rsA, dvo, (unsigned)(n * 32768 + i * 128), lbase + XIMG(n) + (8 * wid + i) * 1024); };
    fetch(0);
    u32x32 w[8];
    {   const unsigned b0 = WO_B0;
        asm volatile(ASM_STAGE_VOX : WOUT(w)
                     : [rv] "s"(mk_rs(Wv + h * DH, DM * HD * 2)), [vv] "v"((unsigned)((lane >> 2) * (HD * 2) + wid * 64 + (lane & 3) * 16)),
                       [a0] "v"(lbase + wid * 4096 + g4 * 512 + l16 * 16), WO_OPS(Wo + h * DH * DM)
                     : "memory", "scc", "v16", "v17", "v18", "v19", "v20", CS60_69); }
    if (wid == 0) hi.wait(hi.peek());
    __builtin_amdgcn_s_barrier(); asm volatile("" ::: "memory");
    fetch(1); fetch(2);
    unsigned sp = (unsigned)OFF_VOX - (1u << 20) + 4u * 32768u, so = 3u << 15;
    asm volatile(ASM_VOX : [sp] "+s"(sp), [so] "+s"(so)
                 : WIN(w), [xb] "v"(lbase + 32768 + (l16 >> 3) * 16384 + (l16 & 7) * 128 + (g4 ^ (l16 & 6)) * 16), [lb] "s"(pin_s(lbase + 32768 + wid * 8192)),
                   [st] "v"((unsigned)((8 * wid * 32 + h) * 1024 + lane * 16)), [rw] "s"(rsWu), [ra] "s"(rsA), [dv] "v"(dvo),
                   [vw] "v"(lbase + wid * 2048 + lane * 16), [vi] "v"(lbase + lane * 16), [em] "s"(__builtin_amdgcn_ballot_w64(wid + lane == 0)), [dn] "s"(done)
                 : "memory", "scc", CV16_99, CV100_199, CS60_69);
    VMWAIT(0);
    __builtin_amdgcn_s_barrier(); asm volatile("" ::: "memory");
    if (threadIdx.x == 0) __hip_atomic_store(done, (unsigned)NSEG, __ATOMIC_RELAXED, __HIP_MEMORY_SCOPE_AGENT);
}
__device__ __forceinline__ u32x2 mix4(f32x4 a, f32x4 b, float g) {
    const float og = rbf(1.f - g);
    float r[4];
#pragma unroll
    for (int j = 0; j < 4; ++j) r[j] = rbf(g * rbf(a[j])) + rbf(og * b[j]);
    return u32x2{cvtpk(r[0], r[1]), cvtpk(r[2], r[3])}; }
#define CS60_89 CS60_69, C10("s", 7), C10("s", 8)
#define FS_PUT(k) do {} while (0)
#define FS_OUT(k) do {} while (0)
#define CS(k, s, i) do {} while (0)
#define CS_OUT(k) do {} while (0)
__global__ __launch_bounds__(256, 1) void k_chain(const bf16* __restrict__ mem0, const bf16* __restrict__ Wq, const bf16* __restrict__ Wk,
                                                  const bf16* __restrict__ Wv, const bf16* __restrict__ Wo,
                                                  const bf16* __restrict__ x, char* __restrict__ ws,
                                                  const bf16* __restrict__ ckv, const bf16* __restrict__ awq, const bf16* __restrict__ awuk,
                                                  const bf16* __restrict__ awuv, const bf16* __restrict__ awo,
                                                  const bf16* __restrict__ gw, bf16* __restrict__ out) {
    extern __shared__ __attribute__((aligned(16))) char lds[];
    const int tid = threadIdx.x, wid = __builtin_amdgcn_readfirstlane(tid >> 6), lane = tid & 63, l16 = lane & 15, g4 = lane >> 4;
    unsigned* tmo = (unsigned*)(ws + OFF_TMO);
    int xg, jb; role_map(blockIdx.x, xg, jb);
    const unsigned gx = (GMAP >> ((blockIdx.x & 7) * 4)) & 7;
    char* const wsP = ws + ((0u - (unsigned)(uintptr_t)ws) & 16383u);
    unsigned pv = (unsigned)(uintptr_t)wsP & 0x1FC000u;
    const __amdgpu_buffer_rsrc_t rsP = __builtin_amdgcn_make_buffer_rsrc((void*)wsP, 0, (int)SZ_PREG, 0x00020000);
    const u32x4 rsPu = mk_rs(wsP, (unsigned)SZ_PREG);
    const __amdgpu_buffer_rsrc_t rsW = __builtin_amdgcn_make_buffer_rsrc((void*)ws, 0, (int)WS_END, 0x00020000);
    const int sidx = l16 & 2 ? 0xEEEE : 0x4444;
    const unsigned lbase = (unsigned)(uintptr_t)lds;
    const u32x4 PZ = {0xFFFFFFFFu, 0xFFFFFFFFu, 0xFFFFFFFFu, 0xFFFFFFFFu};
    auto ring = [&](int s, unsigned j) { return slot_off((unsigned)(s & (NR - 1)) * SLOTS + j, pv); };
    auto anchor_off = [&](int s) { return slot_off(NR * SLOTS + (s >> 3), pv) + (s & 7) * 1024; };
    auto anchor = [&](int s) {
        unsigned v;
        asm volatile(ASM_ANCHOR : [aw] "=&s"(v) : [rs] "s"(rsPu), [ab] "s"(pin_s(anchor_off(s) + gx * 320)) : "memory", "scc", "s64", "s65", "s72", "s73");
        return v; };
    auto mem_first = [&]() {
#pragma unroll
        for (int i = 0; i < 8; ++i) { const int j = tid + 256 * i, c = (j >> 6) * 16 + (j & 12) + 2 * (j & 1) + ((j >> 1) & 1), r = (j >> 3) & 6;
            *(unsigned*)(lds + j * 4) = ((const unsigned short*)mem0)[r * DM + c] | (unsigned)((const unsigned short*)mem0)[(r + 1) * DM + c] << 16; } };
    const Hello hi = {(unsigned long long*)(ws + OFF_MAIL + 32768) + blockIdx.x, __builtin_amdgcn_s_memrealtime()};
    if (tid < 16) __hip_atomic_store((unsigned long long*)(ws + OFF_MAIL) + blockIdx.x * 16 + tid, hi.num, __ATOMIC_RELAXED, __HIP_MEMORY_SCOPE_AGENT);
    GST(0);
    if (xg == 3) {
        const int w = jb, j = blockIdx.x >> 3, g5 = w - 5 * j;
        int k0 = g5, L0 = -1;
        if (w < 32) vox_ws(x, Wv, Wo, w, mk_rs(ws, (unsigned)WS_END), tmo + W_VO + 32 * w, hi, lbase, wid);
        else if (w < 64) {
            const int z = (w >> 4) & 1, panel = w & 15;
            proj_ws<true>(z, x, (z == 0 ? Wq : Wk) + panel * 256, (bf16*)(ws + OFF_QB + (size_t)z * SZ_ACT) + panel * 8192, 0, 1, tmo + W_PROG + 32 * (w - 32), hi, lbase, wid);
            const int lane = lane_id(), l16 = lane & 15, g4 = lane >> 4;
            const unsigned b0 = WO_B0;
            asm volatile(ASM_STAGE_WT :: WO_OPS(awo + (w - 32) * DH * DM), [wb] "s"(pin_s((unsigned)OFF_WT + (8 * wid * 128 + 4 * (w - 32)) * 1024)), [dv] "v"(lane * 16), [rw] "s"(mk_rs(ws, (unsigned)WS_END))
                         : "memory", "scc", "v16", "v17", "v18", "v19", CA0_99, CA100_199, CS60_69);
            __builtin_amdgcn_s_barrier(); asm volatile("" ::: "memory");
            if (tid == 0) __hip_atomic_fetch_add(tmo + W_CTRA, 0x10000u, __ATOMIC_RELAXED, __HIP_MEMORY_SCOPE_AGENT);
        } else {
            const int i = j == 12 ? 19 : j - 13, e = g5 + i - 18;
            const int pair = i < 18 ? 3 * g5 + (g5 == 4) + i / 6 : 12, z = i < 18 ? (i % 6) >> 1 : e >> 1, half = (i < 18 ? i : e) & 1, n0 = pair * 256;
            if (TCH && w > 64 && w < 129) {
                const int t = w - 65;
                const u32x4 rt = mk_rs(t < 32 ? Wo + t * DH * DM : (t < 48 ? Wq : Wk) + 256 * HD + (t & 15) * 256, t < 32 ? DH * DM * 2 : 256 * HD * 2 - 8192 + 512);
                const unsigned vt = t < 32 ? (unsigned)tid * 16u : (unsigned)((tid >> 5) * (HD * 2) + (tid & 31) * 16), it = t < 32 ? 4096u : HD * 16u;
#pragma unroll
                for (int q = 0; q < 32; ++q) dma16<false>(rt, vt, q * it, lbase + wid * 1024); }
            if (XT && w > 128 && w < 129 + 5 * XT) {
#pragma unroll
                for (int q = 0; q < 8; ++q) dma16<false>(mk_rs(x, SEQ * DM * 2), (unsigned)lane * 16u, (unsigned)(((w - 124) / 5) * 32768 + (wid * 8 + q) * 1024), lbase + wid * 1024); }
            if (WARM && w < 128) {
#pragma unroll
                for (int q = 0; q < 8; ++q) dma16<false>(mk_rs(ckv, SEQ * DM * 2), (unsigned)lane * 16u, (unsigned)((w - 64) * 32768 + (wid * 8 + q) * 1024), lbase + wid * 1024); }
            const unsigned ts = (unsigned)__builtin_amdgcn_s_memtime();
            if (w == MASTER) {
#pragma unroll
                for (int q = 0; q < 16; ++q) __builtin_amdgcn_raw_buffer_store_b128(u32x4{}, rsW, tid * 16 + q * 4096, (unsigned)OFF_TMO, AUX_SC1);
                VMWAIT(0);
                __syncthreads();
                unsigned long long was = 0;
#pragma unroll 1
                for (unsigned i = 0; i < SPIN_CAP; ++i) {
                    const unsigned long long v = __hip_atomic_load((unsigned long long*)(ws + OFF_MAIL) + tid * 16, __ATOMIC_RELAXED, __HIP_MEMORY_SCOPE_AGENT);
                    if (i == 0 || v != was) __hip_atomic_store(hi.rep - blockIdx.x + tid, v, __ATOMIC_RELAXED, __HIP_MEMORY_SCOPE_AGENT);
                    was = v;
                    if (__builtin_amdgcn_ballot_w64(v + 5000u - hi.num > (1u << 24)) == 0) break; }
            } else if (wid == 0) { until(ts, ADELAY / 2); hi.wait(hi.peek()); }
            if (ADELAY) until(ts, ADELAY);
            VMWAIT(0);
            __builtin_amdgcn_s_barrier(); asm volatile("" ::: "memory");
            proj_ws<false>(z, z == 0 ? x : ckv, (z == 0 ? awq : z == 1 ? awuk : awuv) + n0, (bf16*)(ws + OFF_QA + (size_t)z * SZ_ACT) + n0 * 32, half, 2, nullptr, hi, lbase, wid);
            if (tid == 0) { cnt_add(tmo + W_CTRP + 32 * pair); cnt_add(tmo + W_CTRA); }
            if (g5 < 3 && i == 18) { k0 = g5 < 2 ? 3 : 4; L0 = g5 == 0 ? 19 : 20; } else L0 = i;
            GST(1);
            if (wid == 0) wave_wait<8>(tmo + W_CTRP + 32 * ((k0 < 3 ? 6 * k0 + L0 % 6 : 7 * k0 - 3 + L0 % 7) >> 1), 6u, tmo, 0x30001u);
            __builtin_amdgcn_s_barrier(); asm volatile("" ::: "memory");
        }
        GST(2);
        attn::attn_phase(ws + OFF_QA, g5, k0, L0, tmo, lbase, wid);
        GST(3);
        oproj_ws(g5, j & 3, tmo + W_OQ + (g5 * 4 + (j & 3)) * 32, (const bf16*)(ws + OFF_OA), (const bf16*)(ws + OFF_WT), (float*)(ws + OFF_PART), tmo, lbase, wid);
        GST(4);
        if (w < 2 * NSEG) {
            const int lq = lane_id(), row = w * 16 + (lq & 15);
            if (wid == 0) { unsigned v = 0; wave_wait<20>(tmo + W_FA + 32 * (w >> 1), 20u, tmo, 0xA0000u + w); prog_wait<20>(v, tmo + W_BB, lq, (w >> 1) + 1, tmo, 0xA1000u + w); }
            __builtin_amdgcn_s_barrier(); asm volatile("" ::: "memory");
            const unsigned ti = (unsigned)(((w >> 1) * 32 + 8 * wid) * 2 + (w & 1));
            f32x4 qa[8][5]; u32x2 bb[8];
#pragma unroll
            for (int c = 0; c < 8; ++c) {
#pragma unroll
                for (int k = 0; k < 5; ++k) qa[c][k] = __builtin_bit_cast(f32x4, __builtin_amdgcn_raw_buffer_load_b128(rsW, (unsigned)OFF_PART + (ti + 2 * c) * 1024 + lq * 16, (unsigned)(k * SEQ * DM * 4), AUX_SC1));
                bb[c] = __builtin_amdgcn_raw_buffer_load_b64(rsW, (unsigned)OFF_BB + (ti + 2 * c) * 512 + lq * 8, 0, AUX_SC1); }
            const float g = __uint_as_float(__builtin_amdgcn_raw_buffer_load_b32(rsW, (unsigned)(OFF_G + row * 4), 0, AUX_SC1));
#pragma unroll
            for (int c = 0; c < 8; ++c) {
                const f32x4 bs = {__uint_as_float(bb[c][0] << 16), __uint_as_float(bb[c][0] & 0xFFFF0000u), __uint_as_float(bb[c][1] << 16), __uint_as_float(bb[c][1] & 0xFFFF0000u)};
                *(u32x2*)(out + (size_t)row * DM + (8 * wid + c) * 16 + 4 * (lq >> 4)) = mix4(((qa[c][0] + qa[c][1]) + (qa[c][2] + qa[c][3])) + qa[c][4], bs, g); }
        }
    } else if (xg == 0) {
        const int h = jb;
        const unsigned q2 = l16 >> 2, p2 = lane & 3, r8 = l16 & 7;
        u32x32 w[8];
        asm volatile(ASM_STAGE_H : WOUT(w)
                     : [rq] "s"(mk_rs(Wq + h * DH, DM * HD * 2)), [rk] "s"(mk_rs(Wk + h * DH, DM * HD * 2)), [vv] "v"((unsigned)((lane >> 2) * (HD * 2) + wid * 64 + (lane & 3) * 16)),
                       [a0] "v"(lbase + wid * 4096 + (32 * (g4 & 1) + 8 * (g4 >> 1) + q2) * 64 + p2 * 8), [lb] "s"(pin_s(lbase + wid * 4096))
                     : "memory", "scc", "v16", CS60_69);
        if (wid == 0) hi.wait(hi.peek());
        LDS_BARRIER();
        constexpr unsigned LQ = 16384, LK = 24576;
        GST(1);
        mem_first();
        const unsigned qw = lbase + LQ + (32 * wid + l16) * 16 + g4 * 4, kw = qw + 2048 + ((g4 >> 1) + 2 * (g4 & 1) - g4) * 4;
        const unsigned ta = lbase + LQ + 4096 + p2 * 512, fr = lbase + LQ + (8 * g4 + q2) * 16 + (p2 & 1) * 8;
        const unsigned ma = lbase + (g4 >> 1) * 256 + p2 * 64 + (g4 & 1) * 32 + q2 * 8;
        *(LAS unsigned*)(size_t)(qw + 4096) = 0u; *(LAS unsigned*)(size_t)(kw + 4096) = 0u;
        pv |= wave_wait<8>(tmo + W_CAL + (h & 15) * 32, NH << 16, tmo, 0xB0000u) & 0x3FFFu;
        GST(2);
        unsigned tg = 0, cap = SPIN_CAP >> 5, cnt;
#define H_OUT [cnt] "=&s"(cnt), [cap] "+s"(cap), [tg] "+s"(tg)
#define H_IN WIN(w), [ix] "v"(sidx), [ma] "v"(ma + (s & 1) * 8192), [ml] "v"(lbase + (s & 1) * 8192 + lane * 16), [c2] "s"(C2), [qw] "v"(qw), [kw] "v"(kw), [s] "s"(s)
#define H_CLOB "memory", "vcc", "scc", CV16_99, CV100_199, CS60_89
        auto frags = [&](unsigned o, u32x4 (&F)[4]) {
#pragma unroll
            for (int ks = 0; ks < 4; ++ks) { const char* p = lds + (fr - lbase) + o + ks * 512;
                const u32x2 a = tr_rdu(p), b = tr_rdu(p + 64); F[ks] = u32x4{a[0], a[1], b[0], b[1]}; } };
        auto scores = [&](f32x4 (&S)[3], const u32x4 (&F)[4], const u32x4 (&Q)[4], const char* kx, unsigned d) {
            u32x4 K[8];
#pragma unroll
            for (int i = 0; i < 8; ++i) K[i] = *(const u32x4*)(kx + (i >> 1) * 2048 + (i & 1) * d);
#define MM3(f, q, a, b, c0, c1, c2) "v_mfma_f32_16x16x32_bf16 %0, %" #f ", %" #q ", " c0 "\n\tv_mfma_f32_16x16x32_bf16 %1, %" #a ", %" #q ", " c1 "\n\tv_mfma_f32_16x16x32_bf16 %2, %" #b ", %" #q ", " c2 "\n\t"
            asm volatile("s_nop 1\n\t" MM3(3, 7, 11, 12, "0", "0", "0") MM3(4, 8, 13, 14, "%0", "%1", "%2") MM3(5, 9, 15, 16, "%0", "%1", "%2") MM3(6, 10, 17, 18, "%0", "%1", "%2") "s_nop 7"
                         : "=&v"(S[0]), "=&v"(S[1]), "=&v"(S[2])
                         : "v"(F[0]), "v"(F[1]), "v"(F[2]), "v"(F[3]), "v"(Q[0]), "v"(Q[1]), "v"(Q[2]), "v"(Q[3]),
                           "v"(K[0]), "v"(K[1]), "v"(K[2]), "v"(K[3]), "v"(K[4]), "v"(K[5]), "v"(K[6]), "v"(K[7])); };
        const f32x4 NEG4 = {-__builtin_inff(), -__builtin_inff(), -__builtin_inff(), -__builtin_inff()};
        auto softmax = [&](f32x4 (&S)[3]) {
            float mx = fmaxf(fmaxf(S[0][0], S[0][1]), fmaxf(S[0][2], S[0][3]));
#pragma unroll
            for (int t = 1; t < 3; ++t) mx = fmaxf(mx, fmaxf(fmaxf(S[t][0], S[t][1]), fmaxf(S[t][2], S[t][3])));
            mx = xg_max(mx);
#pragma unroll
            for (int t = 0; t < 3; ++t)
#pragma unroll
                for (int r = 0; r < 4; ++r) S[t][r] = __builtin_amdgcn_exp2f(S[t][r] - mx);
            const f32x4 s4 = (S[0] + S[1]) + S[2];
            const float inv = __builtin_amdgcn_rcpf(xg_sum((s4[0] + s4[1]) + (s4[2] + s4[3])));
#pragma unroll
            for (int t = 0; t < 3; ++t) S[t] *= inv; };
        if (wid == 0) {
            const u32x4 rsK = mk_rs(ws + OFF_QB, (unsigned)(2 * SZ_ACT));
            unsigned kn = 0, fb = 0, due = 0, f = 360, pc = 0, pr = 0;
            unsigned* const pk = tmo + W_PROG + 32 * (h >> 1) + (lane & 1) * 512;
            auto ready = [&](int sg) {
#pragma unroll 1
                for (unsigned spins = 0; kn <= (unsigned)sg; ++spins) {
                    if (spins) __builtin_amdgcn_s_sleep(1);
                    const unsigned v = __hip_atomic_load(pk, __ATOMIC_RELAXED, __HIP_MEMORY_SCOPE_AGENT);
                    kn = min((unsigned)__builtin_amdgcn_readlane(v, 0), (unsigned)__builtin_amdgcn_readlane(v, 1));
                    if (bail(spins, SPIN_CAP << 1, tmo, 0x51000u + (unsigned)(sg << 8) + h)) break; } };
            ready(0);
            FETCH(8, lbase + LK, rsK, (unsigned)SZ_ACT + h * 8192); FETCH(8, lbase + LK + 8192, rsK, h * 8192);
            ready(1);
#pragma unroll 1
            for (int s = 0; s < NSEG; ++s) {
                asm volatile(ASM_HST0 : H_OUT, [f] "+s"(f), [pc] "+s"(pc), [pr] "+s"(pr)
                             : H_IN, [due] "s"(due), [dv] "v"(lane * 16), [rs] "s"(rsPu), [fb] "s"(fb), [lm] "s"(pin_s(lbase + (s & 1) * 8192)),
                               [rk] "s"(rsK), [lk] "s"(pin_s(lbase + LK + ((s + 1) & 3) * 16384)), [ko] "s"(pin_s(((s + 1) * 32 + h) * 8192)) : H_CLOB);
                FS_PUT(0);
                FAILED(cnt);
                if (__builtin_expect(cap == 0, 0)) __hip_atomic_store(tmo, 0x10000u + h, __ATOMIC_RELAXED, __HIP_MEMORY_SCOPE_AGENT);
                CS(4, s, 0);
                unsigned v = kn;
                if (kn <= (unsigned)min(s + 3, NSEG - 1)) v = __hip_atomic_load(pk, __ATOMIC_RELAXED, __HIP_MEMORY_SCOPE_AGENT);
                due = anchor(s);
                CS(4, s, 1);
                kn = min((unsigned)__builtin_amdgcn_readlane(v, 0), (unsigned)__builtin_amdgcn_readlane(v, 1));
                ready(min(s + 2, NSEG - 1));
                CS(4, s, 2);
                CS(4, s, 7);
                fb = pin_s(ring(s + 1, SL_MEM + gx));
            }
        } else if (wid == 1) {
            const unsigned kxa = LK + (l16 >> 3) * 1024 + g4 * 256 + (8 * ((l16 >> 1) & 1) + 2 * ((l16 >> 2) & 1) + (l16 & 1)) * 16;
            unsigned cn = 1u << 16;
            const unsigned ra = (4 * (r8 >> 1) + 2 * (g4 & 1) + (r8 & 1)) * 16, cp = (l16 >> 3) + (g4 & 2);
            const unsigned pxo = (h & 15) * 512 + (g4 >> 1) * 256 + ra + (l16 >> 3) * 8, pmo = (h >> 1) * 256 + ra + (h & 1) * 8;
#pragma unroll 1
            for (int s = 0; s + 1 < NSEG; ++s) {
                const unsigned kb = (s & 3) * 16384, o0 = ring(s, SL_P + (h >> 4)), o1 = ring(s, SL_P + 3 + (h >> 4)), o2 = ring(s, SL_P + 6 + (h >> 4));
                const unsigned pm = cp < 3 ? ring(s, SL_P + 2 + 3 * cp) + pmo : 1u << 31;
                unsigned long long bad;
                asm volatile(ASM_HST1 : H_OUT, [cn] "+s"(cn), [bad] "=&s"(bad)
                             : H_IN, [kx] "v"(lbase + kxa + kb), [ta] "v"(ta), [fr] "v"(fr), [px] "v"(pxo), [pm] "v"(pm), [rs] "s"(rsPu), [o0] "s"(o0), [o1] "s"(o1), [o2] "s"(o2), [qt] "s"(0x3E8000003E800000ull) : H_CLOB);
                FS_PUT(1);
                if (__builtin_expect(bad != 0, 0)) {
                    u32x4 Q[4], F[4];
                    frags(0, Q); frags(2048, F);
                    f32x4 S[3];
                    scores(S, F, Q, lds + kxa + kb, 64);
                    if (g4 >= 2) S[0] = NEG4;
                    softmax(S);
                    const f32x4 p = l16 < 8 ? S[1] : S[2];
                    const u32x2 px = {canon(cvtpk(p[0], p[1])), canon(cvtpk(p[2], p[3]))}, pw = {canon(cvtpk(S[0][0], S[0][1])), canon(cvtpk(S[0][2], S[0][3]))};
#pragma unroll
                    for (int c = 0; c < 3; ++c) if (bad >> lane & 1) {
                        __builtin_amdgcn_raw_buffer_store_b64(px, rsP, pxo, ring(s, SL_P + 3 * c + (h >> 4)), AUX_SC1);
                        if (cp == 0) __builtin_amdgcn_raw_buffer_store_b64(pw, rsP, pmo, ring(s, SL_P + 3 * c + 2), AUX_SC1); }
                }
                if (s > 0) {
#pragma unroll
                    for (int c = 0; c < 3; ++c) __builtin_amdgcn_raw_buffer_store_b64(u32x2{~0u, ~0u}, rsP, pxo, ring(s - 1, SL_P + 3 * c + (h >> 4)), AUX_SC1);
                    if (cp < 3) __builtin_amdgcn_raw_buffer_store_b64(u32x2{~0u, ~0u}, rsP, ring(s - 1, SL_P + 2 + 3 * cp) + pmo, 0, AUX_SC1); }
            }
            const int s = NSEG - 1;
            asm volatile(ASM_HST2 : H_OUT : H_IN : H_CLOB);
        } else {
            const int rt = wid - 2;
            const unsigned kxa = LK + (l16 >> 3) * 1024 + g4 * 256 + r8 * 16, qa = LK + 8192 + rt * 1024 + lane * 16;
            const int i = rt * 16 + l16 - (16 * (g4 >> 1) + 4 * (g4 & 1));
#pragma unroll 1
            for (int s = 0; s < NSEG; ++s) {
                const unsigned kb = (s & 3) * 16384;
                asm volatile(ASM_HST2 : H_OUT : H_IN : H_CLOB);
                for (int n = 0; n < (1 << 14) && __builtin_amdgcn_ballot_w64(*(volatile LAS unsigned*)(size_t)(ta + 2048) != tg) != 0; ++n) {}
                asm volatile("" ::: "memory");
                u32x4 F[4], Q[4];
                frags(2048, F);
#pragma unroll
                for (int ks = 0; ks < 4; ++ks) Q[ks] = *(const u32x4*)(lds + qa + kb + ks * 2048);
                f32x4 S[3];
                scores(S, F, Q, lds + kxa + kb, 128);
                if (g4 < 2) S[0] = NEG4;
#pragma unroll
                for (int r = 0; r < 4; ++r) { if (r > i) S[1][r] = NEG4[0]; if (8 + r > i) S[2][r] = NEG4[0]; }
                softmax(S);
                const unsigned po = (unsigned)SZ_RING + ((s & 7) * 4 + (h >> 3)) * 16384, ox = (rt ? (h & 7) * 1024 : 8192 + (h & 7) * 512) + lane * 16;
                const unsigned om = 12288 + rt * 2048 + (h & 7) * 256 + l16 * 16 + (g4 & 1) * 8;
                const auto sw = __builtin_amdgcn_permlane16_swap(cvtpk(S[0][0], S[0][1]), cvtpk(S[0][2], S[0][3]), false, false);
                if (rt || lane < 32) {
                    __builtin_amdgcn_raw_buffer_store_b128(u32x4{canon(cvtpk(S[1][0], S[1][1])), canon(cvtpk(S[1][2], S[1][3])), canon(cvtpk(S[2][0], S[2][1])), canon(cvtpk(S[2][2], S[2][3]))}, rsP, ox, po, AUX_SC1);
                    if (s >= 4) __builtin_amdgcn_raw_buffer_store_b128(PZ, rsP, ox, (unsigned)SZ_RING + (((s + 4) & 7) * 4 + (h >> 3)) * 16384, AUX_SC1); }
                if (g4 >= 2) {
                    __builtin_amdgcn_raw_buffer_store_b64(u32x2{canon(sw[0]), canon(sw[1])}, rsP, om, po, AUX_SC1);
                    if (s >= 4) __builtin_amdgcn_raw_buffer_store_b64(u32x2{~0u, ~0u}, rsP, om, (unsigned)SZ_RING + (((s + 4) & 7) * 4 + (h >> 3)) * 16384, AUX_SC1); }
            }
        }
        if (wid < 2) FS_OUT(wid);
        if (wid == 0) CS_OUT(4);
        if (wid == 2) CS_OUT(7);
        FAILOUT(0);
        GST(3);
    } else if (xg == 1) {
        const int h = jb;
        if (h < NPAGE) {
            if (wid == 0) { const unsigned b = calibrate(rsP, h, pv, (unsigned*)(ws + WS_END + 79000 * 8));
                hi.wait(hi.peek());
                if (lane < 16) __hip_atomic_fetch_add(tmo + W_CAL + lane * 32, 0x10000u + (b << h), __ATOMIC_RELAXED, __HIP_MEMORY_SCOPE_AGENT); }
            __syncthreads();
        } else {
#pragma unroll 1
            for (unsigned o = (unsigned)(h - NPAGE) * 4096u; o < (unsigned)SZ_PREG; o += (NH - NPAGE) * 4096u) __builtin_amdgcn_raw_buffer_store_b128(PZ, rsP, tid * 16, o, AUX_SC1);
            VMWAIT(0);
            if (wid == 0) hi.wait(hi.peek());
            __syncthreads();
            if (tid < 16) __hip_atomic_fetch_add(tmo + W_CAL + lane * 32, 0x10000u, __ATOMIC_RELAXED, __HIP_MEMORY_SCOPE_AGENT);
        }
        GST(1);
        {
            const int row0 = (h * 4 + wid) * 16;
            const bf16x8 gv = *(const bf16x8*)(gw + lane * 8);
            float gl = 0.f;
#pragma unroll
            for (int r = 0; r < 16; ++r) {
                const bf16x8 xv = *(const bf16x8*)(x + (size_t)(row0 + r) * DM + lane * 8);
                float sg = 0.f;
#pragma unroll
                for (int j = 0; j < 8; ++j) sg += rbf(bf2f((unsigned short)xv[j]) * bf2f((unsigned short)gv[j]));
#pragma unroll
                for (int o = 32; o >= 1; o >>= 1) sg += __shfl_xor(sg, o);
                if (lane == r) gl = sg;
            }
            gl = rbf(gl);
            gl = rbf(1.f / (1.f + __expf(-gl)));
            if (lane < 16) __builtin_amdgcn_raw_buffer_store_b32(__float_as_uint(gl), rsW, (unsigned)(OFF_G + (row0 + lane) * 4), 0, AUX_SC1);
        }
        constexpr unsigned FP = 0, FQ = 8, FX = 64, FR = 72, LP = 4096, LV = 8192, LG = 32768;
        pv |= wave_wait<8>(tmo + W_CAL + (h & 15) * 32, NH << 16, tmo, 0xB0002u) & 0x3FFFu;
        if (tid < 64) *(unsigned*)(lds + tid * 4) = 0u;
        VMWAIT(0);
        __syncthreads();
        GST(2);
        const unsigned mo = h * 256 + g4 * 64 + (l16 & 12) * 4 + (l16 & 1) * 8 + (l16 & 2) * 2;
        if (wid == 0) {
            unsigned cap = SPIN_CAP, f = 360, pc = 0, pr = 0;
            unsigned long long fm;
            const int k8 = lane & 7;
            const unsigned tv = lane >= 32 ? anchor_off(k8 * 8) + gx * 320 : k8 == 0 ? ring(lane >> 3, SL_VM + h) : k8 < 4 ? ring(lane >> 3, SL_P + 3 * gx + k8 - 1) : ring((lane >> 3) + 1, SL_MEM + k8 - 4);
            asm volatile(ASM_CST : [fm] "=&s"(fm), [cap] "+s"(cap), [f] "+s"(f), [pc] "+s"(pc), [pr] "+s"(pr)
                         : [xa] "v"(lbase + LG + lane * 16), [lv] "s"(pin_s(lbase + LV)), [rs] "s"(rsPu), [ax] "s"(AX), [ap] "s"(AP), [dv] "v"(lane * 16), [dw] "v"(lane * 16 + 4096),
                           [fl] "v"(lbase), [ix] "v"(sidx), [mo] "v"(mo), [tv] "v"(tv)
                         : "memory", "vcc", "scc", CV16_99, CV100_199, CA0_99, CA100_199, C10("s", 4), "s50", "s51", "s52", "s53", "s54", "s55", CS60_89);
            if (__builtin_expect(cap == 0, 0)) __hip_atomic_store(tmo, 0x20000u + h, __ATOMIC_RELAXED, __HIP_MEMORY_SCOPE_AGENT);
        } else if (wid == 1) {
            const u32x4 rsX = mk_rs(ws + OFF_VOX, (unsigned)(4 * SZ_ACT));
            unsigned fv = 0;
#pragma unroll 1
            for (int s = -2; s + 2 < NSEG; ++s) {
                const unsigned pz = s > 0 && s + NR < NSEG && g4 < 3 ? ring(s, SL_MEM + g4) + h * 256 + l16 * 16 : 1u << 31;
                prog_wait<4>(fv, tmo + W_VO, lane, s + 3, tmo, 0x50000u + h);
                lds_wait(lbase + FR, s - 1, tmo, 0x22000u + h);
                CS(5, s, 0);
                lds_wait(lbase + FQ, s + 1, tmo, 0x24000u + h);
                CS(5, s, 1);
                __builtin_amdgcn_raw_buffer_store_b128(PZ, rsP, pz, 0, AUX_SC1);
                unsigned v = fv;
                if (fv < (unsigned)min(s + 5, NSEG)) v = prog_look(tmo + W_VO, lane);
                FETCH(32, lbase + LG + ((s + 2) & 3) * 32768, rsX, ((s + 2) << 20) + h * 32768);
                CS(5, s, 2);
                fv = prog_min(v);
                VMWAIT(0);
                CS(5, s, 7);
                lds_set(lbase + FX, s + 3);
            }
        } else {
            const unsigned po = (unsigned)SZ_RING + (wid - 2) * 32768 + lane * 16;
            const unsigned rot = ROT ? h / 3 : 0;
            auto fr = [&](int i) { return i < 8 ? (i + rot) & 7u : i < 12 ? (i + rot) & 3u : (i + rot) & 1u; };
            unsigned lv = 0;
            u32x2 b0 = {}, b1 = {};
            auto put = [&](int tau) {
                const unsigned o = (unsigned)OFF_BB + (tau * 32 + h) * 1024;
                if (lane == 0) __hip_atomic_store(tmo + W_BB + 32 * h, (unsigned)tau, __ATOMIC_RELAXED, __HIP_MEMORY_SCOPE_AGENT);
                __builtin_amdgcn_raw_buffer_store_b64(b0, rsW, lane * 8, o, AUX_SC1); __builtin_amdgcn_raw_buffer_store_b64(b1, rsW, lane * 8 + 512, o, AUX_SC1); };
#pragma unroll 1
            for (int sg = 0; sg < NSEG; ++sg) {
                lds_wait(lbase + FP, min(sg + 2, NSEG), tmo, 0x25000u + h);
                if (wid == 2) CS(6, sg + 1, 0);
                f32x4 a0, a1;
#pragma unroll 1
                for (unsigned spins = 0;; ++spins) {
                    u32x4 pf[2][16];
#pragma unroll
                    for (int i = 0; i < 32; ++i) { if (i == NPRE) { lds_wait(lbase + FQ, min(sg + 2, NSEG), tmo, 0x25000u + h); if (wid == 2 && sg > 0 && spins == 0) put(sg - 1); }
                        pf[i >> 4][i & 15] = __builtin_amdgcn_raw_buffer_load_b128(rsP, po + (i >> 4) * 16384, (unsigned)((sg & 7) * 65536) + (((i & 15) < 8 ? 0 : (i & 15) < 12 ? 8 : i & 14) + fr(i & 15)) * 1024, AUX_SC1); }
                    __builtin_amdgcn_sched_barrier(0);
                    if (wid == 2) CS(6, sg + 1, 1);
                    a0 = f32x4{}; a1 = f32x4{};
#pragma unroll
                    for (int u = 0; u < 2; ++u) {
                        const char* gp = lds + LG + (sg & 3) * 32768 + (2 * wid - 4 + u) * 8192 + lane * 16, * gm = lds + LV + lv + (2 * wid - 4 + u) * 2048 + lane * 16;
                        u32x4 vf[14];
#pragma unroll
                        for (int i = 0; i < 14; ++i) vf[i] = *(const u32x4*)(i < 8 ? gp + fr(i) * 1024 : i < 12 ? gp + fr(i) * 2048 + (g4 >> 1) * 512 : gm + fr(i) * 1024);
                        __builtin_amdgcn_sched_barrier(0);
#pragma unroll
                        for (int i = 0; i < 8; ++i) a1 = mfma32(vf[i], pf[u][i], a1);
#pragma unroll
                        for (int i = 8; i < 14; ++i) a0 = mfma32(vf[i], pf[u][i], a0);
#pragma unroll
                        for (int i = 12; i < 14; ++i) a1 = mfma32(vf[i], pf[u][i + 2], a1);
                    }
                    if (__builtin_expect(__builtin_amdgcn_ballot_w64(__builtin_isunordered(a0[0], a1[0])) == 0, 1)) break;
                    unsigned mx = 0;
#pragma unroll
                    for (int i = 0; i < 32; ++i) { const u32x4 v = pf[i >> 4][i & 15]; mx = max(max(mx, max(v[0], v[1])), max(v[2], v[3])); }
                    if (__builtin_amdgcn_ballot_w64(mx == ~0u) == 0 || bail(spins, SPIN_CAP, tmo, 0x53000u + h)) break;
                    __builtin_amdgcn_s_sleep(2);
                }
                lv = lv == 16384u ? 0u : lv + 8192u;
                char* p = lds + LP + (sg & 1) * 2048 + lane * 16;
                if (wid == 3) {
                    lds_wait(lbase + FR, sg - 1, tmo, 0x26000u + h);
                    *(f32x4*)p = a0; *(f32x4*)(p + 1024) = a1;
                    lds_set(lbase + FR + 4, sg + 1);
                } else {
                    VMWAIT(0);
                    lds_wait(lbase + FR + 4, sg + 1, tmo, 0x27000u + h);
                    a0 += *(const f32x4*)p; a1 += *(const f32x4*)(p + 1024);
                    lds_set(lbase + FR, sg + 1);
                    b0 = u32x2{cvtpk(a0[0], a0[1]), cvtpk(a0[2], a0[3])}; b1 = u32x2{cvtpk(a1[0], a1[1]), cvtpk(a1[2], a1[3])};
                    CS(6, sg + 1, 7);
                }
            }
            if (wid == 2) { put(NSEG - 1); VMWAIT(0); if (lane == 0) __hip_atomic_store(tmo + W_BB + 32 * h, (unsigned)NSEG, __ATOMIC_RELAXED, __HIP_MEMORY_SCOPE_AGENT); }
        }
        if (wid == 0) FS_OUT(3);
        if (wid == 1) CS_OUT(5);
        if (wid == 2) CS_OUT(6);
        FAILOUT(4096);
        GST(3);
    } else {
        const int h = jb;
        const unsigned q2 = l16 >> 2, p2 = lane & 3;
        constexpr unsigned LI = 16384;
        u32x32 w[8];
        {   const unsigned b0 = WO_B0;
            asm volatile(ASM_STAGE_V : WOUT(w)
                         : [rv] "s"(mk_rs(Wv + h * DH, DM * HD * 2)), [vv] "v"((unsigned)((lane >> 2) * (HD * 2) + wid * 64 + (lane & 3) * 16)),
                           [a0] "v"(lbase + wid * 4096 + (32 * (g4 & 1) + 8 * (g4 >> 1) + q2) * 64 + p2 * 8), WO_OPS(Wo + h * DH * DM)
                         : "memory", "scc", CV16_99, CS60_69); }
        if (wid == 0) hi.wait(hi.peek());
        LDS_BARRIER();
        GST(1);
        mem_first();
        const unsigned vw = lbase + LI + (16 * wid + l16) * 16 + g4 * 4, ta = lbase + LI + 2048 + p2 * 256, fr = lbase + LI + (8 * g4 + q2) * 16 + (p2 & 1) * 8;
        const unsigned ma = lbase + (g4 >> 1) * 256 + p2 * 64 + (g4 & 1) * 32 + q2 * 8;
        *(LAS unsigned*)(size_t)(vw + 2048) = 0u; *(LAS unsigned*)(size_t)(vw + 3072) = 0u; *(LAS unsigned*)(size_t)(lbase + 40960) = 0u;
        pv |= wave_wait<8>(tmo + W_CAL + (h & 15) * 32, NH << 16, tmo, 0xB0001u) & 0x3FFFu;
        GST(2);
        unsigned tg = 0, cap = SPIN_CAP >> 5, cn = 1u << 16, fb = 0, due = 0, f = 360, pc = 0, pr = 0, dl = 0, fv = 0, pf = 0;
        const u32x4 rsX = mk_rs(ws + OFF_VOX, (unsigned)(4 * SZ_ACT));
        int lastf = 0;
        const unsigned vb = 8 * wid + 2 * (g4 & 1) + (g4 >> 1), vo = h * 256 + l16 * 16;
        unsigned cnt;
#define V_OUT [cnt] "=&s"(cnt), [cap] "+s"(cap), [tg] "+s"(tg), [cn] "+s"(cn)
#define V_IN WIN(w), [ix] "v"(sidx), [iy] "v"(l16 < 8 ? 0x4444 : 0xEEEE), [ma] "v"(ma + (s & 1) * 8192), [ml] "v"(lbase + (s & 1) * 8192 + lane * 16), [vw] "v"(vw), [ta] "v"(ta), [fr] "v"(fr), \
             [rs] "s"(rsPu), [s] "s"(s), [so0] "v"(ring(s, SL_VM + vb) + vo), [so1] "v"(ring(s, SL_VM + vb + 4) + vo)
        if (wid == 0) {
#pragma unroll 1
            for (int s = 0; s < NSEG; ++s) {
                asm volatile(ASM_VST0 : V_OUT, [f] "+s"(f), [pc] "+s"(pc), [pr] "+s"(pr)
                             : V_IN, [due] "s"(due), [dv] "v"(lane * 16), [fb] "s"(fb), [lm] "s"(pin_s(lbase + (s & 1) * 8192)), [fm] "s"(AFM), [pe] "s"(AM + dl),
                               [am] "s"(__builtin_amdgcn_ballot_w64(h == 0 && lane < 16)), [av] "v"(lane * 64), [ao] "s"(pin_s(anchor_off(s))),
                               [pf] "s"(pf), [pv] "v"(lane * 128), [pw] "s"(pin_s((unsigned)SZ_RING + ((s - 1) & 7) * 65536 + (((h + 2) / 3) & 7) * 8192)), [lk] "s"(pin_s(lbase + 32768)), [rk] "s"(rsX) : H_CLOB);
                FS_PUT(2);
                FAILED(cnt);
                if (__builtin_expect(cap == 0, 0)) __hip_atomic_store(tmo, 0x90000u + h, __ATOMIC_RELAXED, __HIP_MEMORY_SCOPE_AGENT);
                if (ADS && cnt) {
                    if (s >= AS0 && (unsigned)(s - lastf - 2) <= 2u) dl = min(dl + ADS, ADM);
                    lastf = s; }
                due = anchor(s);
                fb = pin_s(ring(s + 1, SL_MEM + gx));
                fv = __builtin_amdgcn_readfirstlane(*(volatile LAS unsigned*)(size_t)(lbase + 40960));
                pf = pin_s((min((unsigned)s + 3u, max(fv, 1u) - 1u) << 20) + ((h + 31) & 31) * 32768);
            }
        } else {
#pragma unroll 1
            for (int s = 0; s < NSEG; ++s) {
                asm volatile(ASM_VST1 : V_OUT : V_IN : H_CLOB);
                if ((PF & 2) && wid == 1 && fv < (unsigned)min(s + 5, NSEG)) { fv = prog_min(prog_look(tmo + W_VO, lane)); *(volatile LAS unsigned*)(size_t)(lbase + 40960) = fv; }
                if (wid > 1 && s > 0 && s + 3 < NSEG) {
                    until((unsigned)__builtin_amdgcn_s_memtime(), FT);
#pragma unroll
                    for (int i = 0; i < 4; ++i) __builtin_amdgcn_raw_buffer_store_b128(PZ, rsP, ring(s - 1, SL_VM + (wid - 2) * 16 + 4 * i + g4) + vo, 0, AUX_SC1); }
            }
        }
        VMWAIT(0);
        if (wid == 0) FS_OUT(2);
        FAILOUT(8192);
        GST(3);
    }
    GST(5);
}

extern "C" void kernel_launch(void* const* d_in, const int* in_sizes, int n_in,
                              void* d_out, int out_size, void* d_ws, size_t ws_size,
                              hipStream_t stream) {
    (void)in_sizes; (void)n_in; (void)out_size; (void)ws_size;
    const bf16* x = (const bf16*)d_in[0];
    const bf16* ckv = (const bf16*)d_in[1];
    const bf16* a_wq = (const bf16*)d_in[2];
    const bf16* a_wuk = (const bf16*)d_in[3];
    const bf16* a_wuv = (const bf16*)d_in[4];
    const bf16* a_wo = (const bf16*)d_in[5];
    const bf16* mem0 = (const bf16*)d_in[7];
    const bf16* bWq = (const bf16*)d_in[8];
    const bf16* bWk = (const bf16*)d_in[9];
    const bf16* bWv = (const bf16*)d_in[10];
    const bf16* bWo = (const bf16*)d_in[11];
    const bf16* gw = (const bf16*)d_in[13];
    char* ws = (char*)d_ws;
    bf16* out = (bf16*)d_out;

    (void)hipFuncSetAttribute((const void*)k_chain, hipFuncAttributeMaxDynamicSharedMemorySize, CHAIN_LDS);
    k_chain<<<256, 256, CHAIN_LDS, stream>>>(mem0, bWq, bWk, bWv, bWo, x, ws, ckv, a_wq, a_wuk, a_wuv, a_wo, gw, out);
}
```
